```python
import torch

def _softmax(x, axis=-1):
    return torch.softmax(x, dim=axis)
def _gelu(x):
    return torch.nn.functional.gelu(x, approximate="tanh")
def _silu(x):
    return torch.nn.functional.silu(x)
def _sigmoid(x):
    return torch.sigmoid(x)
def _layernorm(x, axis=-1, eps=1e-5):
    m = x.mean(dim=axis, keepdim=True)
    v = x.var(dim=axis, keepdim=True, unbiased=False)
    return (x - m) / torch.sqrt(v + eps)
def _scatter_add(tgt, idx, src, axis=0):
    return tgt.clone().index_add_(axis, idx, src)
def _segment_sum(src, seg, n):
    out = torch.zeros((n,) + src.shape[1:], dtype=src.dtype)
    return out.index_add_(0, seg, src)
def _pad(x, pw):
    flat = tuple(v for pair in reversed(pw) for v in pair)
    return torch.nn.functional.pad(x, flat)

kernel_name = 'gated_mix__softmax_full_mha__dense_activation_mish__prompt'

def setup_inputs():

    _w = (lambda a: (lambda t: t.to(torch.bfloat16) if t.is_floating_point() else t)(torch.as_tensor(a)))
    def _A_setup():
        B, S, D = 1, 2048, 512
        return {
            "x": _w((lambda *s: torch.randn(*s, dtype=torch.bfloat16) * 0.05)(B, S, D)),
            "wqkv": _w((lambda *s: torch.randn(*s, dtype=torch.bfloat16) * 0.8)(D, 3 * D)),
            "wo": _w((lambda *s: torch.randn(*s, dtype=torch.bfloat16) * 0.05)(D, D)),
        }
    _A_in = _A_setup()

    def _B_setup():
        B, S, D, F = 1, 2048, 512, 1792
        return {
            "x": _w((lambda *s: torch.randn(*s, dtype=torch.bfloat16) * 0.05)(B, S, D)),
            "w1": _w((lambda *s: torch.randn(*s, dtype=torch.bfloat16) * 12.8)(D, F) * (D ** -0.5)),
            "b1": _w((lambda *s: torch.randn(*s, dtype=torch.bfloat16) * 0.2)(F)),
            "w2": _w((lambda *s: torch.randn(*s, dtype=torch.bfloat16) * 0.1)(F, D) * (F ** -0.5)),
        }
    _B_in = _B_setup()

    return {
        "A_x": _A_in["x"],
        "A_wqkv": _A_in["wqkv"],
        "A_wo": _A_in["wo"],
        "B_x": _B_in["x"],
        "B_w1": _B_in["w1"],
        "B_b1": _B_in["b1"],
        "B_w2": _B_in["w2"],
        "gate_w": _w((lambda *s: torch.randn(*s, dtype=torch.bfloat16) * 0.05)(512) * 16.0),
    }

def reference(A_x, A_wqkv, A_wo, B_x, B_w1, B_b1, B_w2, gate_w):
    def _A_ref(x, wqkv, wo):
        B, S, D = x.shape
        H = 32; Dh = D // H
        qkv = (lambda x, *d: x.permute(*d))((x @ wqkv).reshape(B, S, 3, H, Dh), 2, 0, 3, 1, 4)
        q, k, v = qkv[0], qkv[1], qkv[2]
        scale = Dh ** -0.5
        scores = torch.einsum("bhqd,bhkd->bhqk", q, k) * scale
        p = _softmax(scores, axis=-1)
        o = torch.einsum("bhqk,bhkd->bhqd", p, v)
        return (lambda x, *d: x.permute(*d))(o, 0, 2, 1, 3).reshape(B, S, D) @ wo

    def _B_ref(x, w1, b1, w2):
        h = torch.einsum("bsd,df->bsf", x, w1) + b1
        sp = torch.log1p(torch.exp(-torch.abs(h))) + torch.where(h > 0, h, torch.zeros_like(h))
        act = h * torch.tanh(sp)
        return torch.einsum("bsf,fd->bsd", act, w2)

    x = A_x
    g = _sigmoid((x * gate_w).sum(-1, keepdims=True))
    a = _A_ref(x, A_wqkv, A_wo)
    b = _B_ref(x, B_w1, B_b1, B_w2)
    return g * a + (1 - g) * b
```

```cpp
#include <hip/hip_runtime.h>
#include <hip/hip_bf16.h>
typedef unsigned short u16;
__device__ __forceinline__ float bf2f(u16 v){ return __uint_as_float(((unsigned)v)<<16); }
__device__ __forceinline__ u16 f2bf(float f){ unsigned u=__float_as_uint(f); u += 0x7fffu + ((u>>16)&1u); return (u16)(u>>16); }

template<bool AF>
__global__ void ngemm(const void* A, const u16* B, float* C, int M, int N, int K){
  __shared__ float As[16][17], Bs[16][17];
  int tx=threadIdx.x, ty=threadIdx.y; int r=blockIdx.y*16+ty, c=blockIdx.x*16+tx; float acc=0;
  for(int k0=0;k0<K;k0+=16){
    As[ty][tx]= AF? ((const float*)A)[(size_t)r*K+k0+tx] : bf2f(((const u16*)A)[(size_t)r*K+k0+tx]);
    Bs[ty][tx]= bf2f(B[(size_t)(k0+ty)*N+c]);
    __syncthreads();
    for(int k=0;k<16;k++) acc+=As[ty][k]*Bs[k][tx];
    __syncthreads();
  }
  C[(size_t)r*N+c]=acc;
}
__global__ void nattn(const float* qkv, float* o){
  int h=blockIdx.y, q=blockIdx.x*64+threadIdx.x;
  float qv[16]; for(int d=0;d<16;d++) qv[d]=qkv[(size_t)q*1536+h*16+d]*0.25f;
  float m=-1e30f,l=0,acc[16]; for(int d=0;d<16;d++) acc[d]=0;
  for(int k=0;k<2048;k++){
    const float* kp=qkv+(size_t)k*1536+512+h*16; const float* vp=kp+512;
    float s=0; for(int d=0;d<16;d++) s+=qv[d]*kp[d];
    float mn=fmaxf(m,s); float a=__expf(m-mn), p=__expf(s-mn); l=l*a+p;
    for(int d=0;d<16;d++) acc[d]=acc[d]*a+p*vp[d];
    m=mn;
  }
  for(int d=0;d<16;d++) o[(size_t)q*512+h*16+d]=acc[d]/l;
}
__global__ void nmish(float* hbuf, const u16* b1){
  size_t i=(size_t)blockIdx.x*256+threadIdx.x; int f=i%1792; float h=hbuf[i]+bf2f(b1[f]);
  float sp=log1pf(expf(-fabsf(h)))+fmaxf(h,0.f); hbuf[i]=h*tanhf(sp);
}
__global__ void nfinal(const u16* x,const u16* gw,const float* a,const float* b,u16* out){
  int r=blockIdx.x; float s=0; for(int k=threadIdx.x;k<512;k+=64) s+=bf2f(x[r*512+k])*bf2f(gw[k]);
  for(int o=32;o;o>>=1) s+=__shfl_xor(s,o);
  float g=1.f/(1.f+expf(-s));
  for(int k=threadIdx.x;k<512;k+=64) out[r*512+k]=f2bf(g*a[r*512+k]+(1.f-g)*b[r*512+k]);
}
extern "C" void kernel_launch(void* const* d_in, const int* in_sizes, int n_in, void* d_out, int out_size, void* d_ws, size_t ws_size, hipStream_t stream) {
  const u16* x=(const u16*)d_in[0]; const u16* wqkv=(const u16*)d_in[1]; const u16* wo=(const u16*)d_in[2];
  const u16* w1=(const u16*)d_in[4]; const u16* b1=(const u16*)d_in[5]; const u16* w2=(const u16*)d_in[6]; const u16* gw=(const u16*)d_in[7];
  float* qkv=(float*)d_ws; float* o=qkv+2048*1536; float* hb=o+2048*512; float* a=hb+2048*1792; float* b=a+2048*512;
  dim3 t(16,16);
  ngemm<false><<<dim3(96,128),t,0,stream>>>(x,wqkv,qkv,2048,1536,512);
  nattn<<<dim3(32,32),64,0,stream>>>(qkv,o);
  ngemm<true><<<dim3(32,128),t,0,stream>>>(o,wo,a,2048,512,512);
  ngemm<false><<<dim3(112,128),t,0,stream>>>(x,w1,hb,2048,1792,512);
  nmish<<<2048*1792/256,256,0,stream>>>(hb,b1);
  ngemm<true><<<dim3(32,128),t,0,stream>>>(hb,w2,b,2048,512,1792);
  nfinal<<<2048,64,0,stream>>>(x,gw,a,b,(u16*)d_out);
}
```

```cpp
#include <hip/hip_runtime.h>
#include <hip/hip_bf16.h>

typedef unsigned short u16;
typedef unsigned long long u64;
typedef __bf16 bf16x8 __attribute__((ext_vector_type(8)));
typedef __bf16 bf16x16 __attribute__((ext_vector_type(16)));
typedef __bf16 bf16x2 __attribute__((ext_vector_type(2)));
typedef short s16x4 __attribute__((ext_vector_type(4)));
typedef float f32x2 __attribute__((ext_vector_type(2)));
typedef float f32x4 __attribute__((ext_vector_type(4)));
typedef float f32x8 __attribute__((ext_vector_type(8)));
typedef float f32x16 __attribute__((ext_vector_type(16)));
typedef unsigned u32x2 __attribute__((ext_vector_type(2)));
typedef unsigned u32x4 __attribute__((ext_vector_type(4)));
#define LAS __attribute__((address_space(3)))
#define GAS __attribute__((address_space(1)))
typedef __amdgpu_buffer_rsrc_t rsrc_t;

constexpr unsigned WS_EPOCH = 0;
constexpr unsigned WS_FKV = 4096;
constexpr unsigned WS_FACT = WS_FKV + 8192;
constexpr unsigned WS_FWT = WS_FACT + 8192;
constexpr unsigned WS_KV = 65536;
constexpr unsigned WS_ACT = WS_KV + 32u * 8u * 16384u;
constexpr unsigned WS_WT = WS_ACT + 64u * 114688u;
constexpr unsigned WS_OF = WS_WT + 16u * 144u * 1024u;
constexpr unsigned WS_END = WS_OF + 64u * 32u * 1024u;
constexpr unsigned WS_DIAG = WS_END;

constexpr unsigned STG = 47104;
constexpr unsigned TILE = 2080;
constexpr unsigned L_BP = 64 * TILE;
constexpr unsigned L_Q = L_BP + 16384;
constexpr unsigned L_G = L_Q + 9216;
constexpr unsigned L_FL = L_G + 1024;
constexpr unsigned L_END = L_FL + 512;

constexpr unsigned SPIN_CAP = 1u << 18;
constexpr int AUX_SC1 = 16;

__device__ __forceinline__ unsigned pk_bf16(float a, float b) {
  f32x2 v = {a, b};
  return __builtin_bit_cast(unsigned, __builtin_convertvector(v, bf16x2));
}
__device__ __forceinline__ float bf_lo(unsigned u) { return __uint_as_float(u << 16); }
__device__ __forceinline__ float bf_hi(unsigned u) { return __uint_as_float(u & 0xffff0000u); }
__device__ __forceinline__ rsrc_t mk_rsrc(const void* p, unsigned bytes) {
  return __builtin_amdgcn_make_buffer_rsrc((void*)p, 0, bytes, 0x00020000);
}
#define BARRIER() asm volatile("s_waitcnt lgkmcnt(0)\n\ts_barrier" ::: "memory")
#define WAITV(n) asm volatile("s_waitcnt vmcnt(" #n ")" ::: "memory")
#define WAITL0() asm volatile("s_waitcnt lgkmcnt(0)" ::: "memory")
#define CFENCE() asm volatile("" ::: "memory")

#define STAMP(k) do {} while (0)

__device__ __forceinline__ bf16x8 lds_frag(LAS char* sm, unsigned off) { return *(LAS bf16x8*)(sm + off); }
__device__ __forceinline__ bf16x8 lds_tr2(LAS char* sm, unsigned o0, unsigned o1) {
  s16x4 a = __builtin_amdgcn_ds_read_tr16_b64_v4i16((LAS s16x4*)(sm + o0));
  s16x4 b = __builtin_amdgcn_ds_read_tr16_b64_v4i16((LAS s16x4*)(sm + o1));
  return __builtin_bit_cast(bf16x8, __builtin_shufflevector(a, b, 0, 1, 2, 3, 4, 5, 6, 7));
}
__device__ __forceinline__ unsigned lds_flag_ld(LAS char* sm, unsigned idx) {
  return __hip_atomic_load((LAS unsigned*)(sm + L_FL) + idx, __ATOMIC_RELAXED, __HIP_MEMORY_SCOPE_WORKGROUP);
}
__device__ __forceinline__ void lds_flag_st(LAS char* sm, unsigned idx) {
  __hip_atomic_store((LAS unsigned*)(sm + L_FL) + idx, 1u, __ATOMIC_RELAXED, __HIP_MEMORY_SCOPE_WORKGROUP);
}
__device__ __forceinline__ void lds_wait(LAS char* sm, unsigned idx0, unsigned n, int lane) {
#pragma unroll 1
  for (unsigned s = 0; s < SPIN_CAP * 4; ++s) {
    CFENCE();
    unsigned v = lds_flag_ld(sm, idx0 + ((unsigned)lane < n ? lane : 0));
    if (__all(v != 0)) break;
    __builtin_amdgcn_s_sleep(1);
  }
  CFENCE();
}
__device__ __forceinline__ void glb_wait(rsrc_t rs, unsigned off, u32x2 tag) {
#pragma unroll 1
  for (unsigned s = 0; s < SPIN_CAP; ++s) {
    CFENCE();
    u32x2 v = __builtin_amdgcn_raw_buffer_load_b64(rs, off, 0, AUX_SC1);
    if (__all(v.x == tag.x && v.y == tag.y)) break;
    __builtin_amdgcn_s_sleep(2);
  }
  CFENCE();
}

__global__ void __launch_bounds__(768, 3)
fused_kernel(const u16* __restrict__ x, const u16* __restrict__ wqkv, const u16* __restrict__ wo,
             const u16* __restrict__ w1, const u16* __restrict__ b1, const u16* __restrict__ w2,
             const u16* __restrict__ gw, u16* __restrict__ out, unsigned char* __restrict__ ws) {
  __shared__ __attribute__((aligned(16))) char smem_[L_END];
  LAS char* sm = (LAS char*)smem_;
  const int lane = threadIdx.x & 63;
  const int wave = __builtin_amdgcn_readfirstlane(threadIdx.x >> 6);
  const int b = blockIdx.x, xcd = b & 7, idx = b >> 3;
  const int bi = 4 * (xcd & 1) + (idx & 3);
  const int hd = 8 * (xcd >> 1) + (idx >> 2);
  const int hh = lane >> 5, l31 = lane & 31, l15 = lane & 15, g4 = lane >> 4;
  const int q4 = l15 >> 2, p4 = lane & 3;
  const rsrc_t rws = mk_rsrc(ws, WS_END);
  STAMP(0);

  if (wave >= 4 && wave < 8) {
    const int lw = wave - 4;
    const rsrc_t rx = mk_rsrc(x, 2048u * 1024u), rq = mk_rsrc(wqkv, 512u * 3072u), r1 = mk_rsrc(w1, 512u * 3584u);
    const int r8 = lane >> 3, slot = lane & 7;
    const unsigned xo0 = (unsigned)(256 * bi + 64 * lw + r8) * 1024u + (unsigned)(slot ^ (r8 >> 1)) * 16u;
    const unsigned xo1 = (unsigned)(256 * bi + 64 * lw + r8 + 8) * 1024u + (unsigned)(slot ^ (r8 >> 1) ^ 4) * 16u;
    const int pos = (lane >> 1) & 15, wrow = pos ^ ((pos >> 3) << 2);
    const unsigned qo = (unsigned)(16 * lw + wrow) * 3072u + (unsigned)hh * 1024u + (unsigned)hd * 32u + (lane & 1) * 16u;
    const unsigned vo = (unsigned)(16 * lw + wrow) * 3072u + 2048u + (unsigned)hd * 32u + (lane & 1) * 16u;
    const unsigned wo1 = (unsigned)(16 * lw + (lane >> 2)) * 3584u + (unsigned)hd * 112u + (unsigned)p4 * 16u;
    auto issue = [&](int s, unsigned buf) {
      LAS char* xb = sm + buf + lw * 8192;
#pragma unroll
      for (int n = 0; n < 4; ++n) {
        __builtin_amdgcn_raw_ptr_buffer_load_lds(rx, xb + n * 2048, 16, xo0, s * 128 + n * 16384, 0, 0);
        __builtin_amdgcn_raw_ptr_buffer_load_lds(rx, xb + n * 2048 + 1024, 16, xo1, s * 128 + n * 16384, 0, 0);
      }
      LAS char* wb = sm + buf + 32768 + lw * 3584;
      __builtin_amdgcn_raw_ptr_buffer_load_lds(rq, wb, 16, qo, s * (64 * 3072), 0, 0);
      if (lane < 32) __builtin_amdgcn_raw_ptr_buffer_load_lds(rq, wb + 1024, 16, vo, s * (64 * 3072), 0, 0);
      __builtin_amdgcn_raw_ptr_buffer_load_lds(r1, wb + 1536, 16, wo1, s * (64 * 3584), 0, 0);
      if (p4 != 3) __builtin_amdgcn_raw_ptr_buffer_load_lds(r1, wb + 2560, 16, wo1 + 64, s * (64 * 3584), 0, 0);
    };
    issue(0, 0);
    issue(1, STG);
    issue(2, 2 * STG);
    const int ct = b >> 4, kk9 = (b & 15) * 9;
    {
      const rsrc_t r2 = mk_rsrc(w2, 1792u * 1024u), ro = mk_rsrc(wo, 512u * 1024u);
      const unsigned so = (unsigned)(lane >> 2) * 1024u + (unsigned)ct * 64u + (unsigned)p4 * 16u;
      for (int f = lw; f < 9; f += 4) {
        const int ks = kk9 + f;
        if (ks < 112) __builtin_amdgcn_raw_ptr_buffer_load_lds(r2, sm + L_Q + f * 1024, 16, so, ks * 16384, 0, 0);
        else __builtin_amdgcn_raw_ptr_buffer_load_lds(ro, sm + L_Q + f * 1024, 16, so, (ks - 112) * 16384, 0, 0);
      }
    }
    WAITV(26); BARRIER();
    WAITV(14); BARRIER(); issue(3, 0);
    WAITV(14); BARRIER(); issue(4, STG);
    WAITV(12); BARRIER(); issue(5, 2 * STG);
    WAITV(12); BARRIER(); issue(6, 0);
    WAITV(12); BARRIER(); issue(7, STG);
    {
      const unsigned ta = L_Q + (unsigned)(8 * hh + q4) * 64u + (unsigned)(4 * (g4 & 1) + 2 * (p4 & 1) + (p4 >> 1)) * 8u;
      for (int f = lw; f < 9; f += 4) {
        u32x2 a, c;
        asm volatile("ds_read_b64_tr_b16 %0, %2\n\tds_read_b64_tr_b16 %1, %2 offset:256\n\ts_waitcnt lgkmcnt(0)"
                     : "=&v"(a), "=&v"(c) : "v"((unsigned)(size_t)sm + ta + f * 1024) : "memory");
        u32x4 v = {a.x, a.y, c.x, c.y};
        __builtin_amdgcn_raw_buffer_store_b128(v, rws, WS_WT + lane * 16, (ct * 144 + kk9 + f) * 1024, AUX_SC1);
      }
    }
    WAITV(14); BARRIER();
    WAITV(0); BARRIER();
  }

  auto mk_tag = [&](u32x2 e) {
    u64 z = ((u64)__builtin_amdgcn_readfirstlane(e.y) << 32 | (u64)__builtin_amdgcn_readfirstlane(e.x)) + 0x9E3779B97F4A7C15ull;
    z = (z ^ (z >> 30)) * 0xBF58476D1CE4E5B9ull;
    z = (z ^ (z >> 27)) * 0x94D049BB133111EBull;
    z ^= z >> 31;
    return (u32x2){(unsigned)z, (unsigned)(z >> 32)};
  };

  if (wave < 4) {
    const int j = wave;
    const u32x2 ep = __builtin_amdgcn_raw_buffer_load_b64(rws, WS_EPOCH, 0, AUX_SC1);
    if (wave == 0) ((LAS unsigned*)(sm + L_FL))[lane] = 0u;
    f32x4 acc[4][3];
#pragma unroll
    for (int t = 0; t < 4; ++t)
#pragma unroll
      for (int c = 0; c < 3; ++c) acc[t][c] = (f32x4){0.f, 0.f, 0.f, 0.f};
    const unsigned xa0 = (unsigned)(64 * j + l15) * 128u + (unsigned)(g4 ^ (l15 >> 1)) * 16u;
    const unsigned xa1 = xa0 ^ 64u;
    const unsigned wa0 = 32768u + (unsigned)(g4 >> 1) * 3584u + (unsigned)(8 * (g4 & 1) + 4 * (g4 & 1) + q4) * 32u + p4 * 8u;
    const unsigned wa1 = 32768u + (unsigned)(g4 >> 1) * 3584u + (unsigned)(8 * (g4 & 1) + (4 ^ (4 * (g4 & 1))) + q4) * 32u + p4 * 8u;
    unsigned buf = 0;
    WAITV(0);
    const u32x2 tag = mk_tag(ep);
#pragma unroll 1
    for (int s = 0; s < 8; ++s) {
      BARRIER();
#pragma unroll
      for (int kk = 0; kk < 2; ++kk) {
        bf16x8 xf[4], wf[3];
#pragma unroll
        for (int c = 0; c < 3; ++c) wf[c] = lds_tr2(sm, buf + wa0 + kk * 7168 + c * 512, buf + wa1 + kk * 7168 + c * 512);
#pragma unroll
        for (int t = 0; t < 4; ++t) xf[t] = lds_frag(sm, buf + (kk ? xa1 : xa0) + t * 2048);
#pragma unroll
        for (int t = 0; t < 4; ++t) {
          acc[t][0] = __builtin_amdgcn_mfma_f32_16x16x32_bf16(wf[0], xf[t], acc[t][0], 0, 0, 0);
          acc[t][1] = __builtin_amdgcn_mfma_f32_16x16x32_bf16(wf[1], xf[t], acc[t][1], 0, 0, 0);
          acc[t][2] = __builtin_amdgcn_mfma_f32_16x16x32_bf16(xf[t], wf[2], acc[t][2], 0, 0, 0);
        }
      }
      buf = (buf == 2 * STG) ? 0 : buf + STG;
    }
    STAMP(1);
    const float qs = 0.25f * 1.4426950408889634f;
    const int rho = (l15 & 3) + 8 * ((l15 >> 2) & 1) + 4 * (l15 >> 3);
#pragma unroll
    for (int t = 0; t < 4; ++t) {
      const unsigned tl = 2 * j + (t >> 1);
      const unsigned o = (unsigned)(16 * (t & 1) + l15 + 32 * (g4 >> 1)) * 16u + (g4 & 1) * 8u;
      *(LAS u32x2*)(sm + L_Q + tl * 1024 + o) = (u32x2){pk_bf16(acc[t][0][0] * qs, acc[t][0][1] * qs), pk_bf16(acc[t][0][2] * qs, acc[t][0][3] * qs)};
      *(LAS u32x2*)(sm + tl * TILE + o) = (u32x2){pk_bf16(acc[t][1][0], acc[t][1][1]), pk_bf16(acc[t][1][2], acc[t][1][3])};
      *(LAS u32x2*)(sm + tl * TILE + 1024 + (t & 1) * 528 + (unsigned)((g4 >> 1) * 16 + rho) * 16u + (g4 & 1) * 8u) =
          (u32x2){pk_bf16(acc[t][2][0], acc[t][2][1]), pk_bf16(acc[t][2][2], acc[t][2][3])};
    }
    if (lane < 4) *(LAS u32x4*)(sm + (2 * j + (lane >> 1)) * TILE + 1024 + (lane & 1) * 528 + 512) = (u32x4){0x3f803f80u, 0x3f803f80u, 0x3f803f80u, 0x3f803f80u};
    WAITL0();
    {
      const unsigned go = WS_KV + (unsigned)(hd * 8 + bi) * 16384u + (unsigned)j * 4096u + lane * 16u;
#pragma unroll
      for (int t = 0; t < 2; ++t) {
        u32x4 kf = *(LAS u32x4*)(sm + (2 * j + t) * TILE + lane * 16);
        u32x4 vf = *(LAS u32x4*)(sm + (2 * j + t) * TILE + 1024 + lane * 16 + hh * 16);
        __builtin_amdgcn_raw_buffer_store_b128(kf, rws, go + t * 2048, 0, AUX_SC1);
        __builtin_amdgcn_raw_buffer_store_b128(vf, rws, go + t * 2048 + 1024, 0, AUX_SC1);
      }
    }
    BARRIER();
    if (wave == 0 && lane == 0) __builtin_amdgcn_raw_buffer_store_b64(tag, rws, WS_FWT + (unsigned)b * 8u, 0, AUX_SC1);
    WAITV(0);
    if (lane == 0) __builtin_amdgcn_raw_buffer_store_b64(tag, rws, WS_FKV + (unsigned)((hd * 8 + bi) * 4 + j) * 8u, 0, AUX_SC1);
  } else if (wave < 8) {
    BARRIER();
  } else {
    const int j = wave - 8;
    const u32x2 ep = __builtin_amdgcn_raw_buffer_load_b64(rws, WS_EPOCH, 0, AUX_SC1);
    {
      const u32x4 inv = {0xffffffffu, 0xffffffffu, 0xffffffffu, 0xffffffffu};
      const unsigned oo = WS_OF + (unsigned)((8 * bi + 2 * j) * 32 + hd) * 1024u + lane * 16u;
      __builtin_amdgcn_raw_buffer_store_b128(inv, rws, oo, 0, AUX_SC1);
      __builtin_amdgcn_raw_buffer_store_b128(inv, rws, oo + 32768, 0, AUX_SC1);
    }
    f32x16 acc[2][2];
    unsigned gA, gB;
    {
      const rsrc_t rb = mk_rsrc(b1, 3584u), rg = mk_rsrc(gw, 1024u);
      gA = __builtin_amdgcn_raw_buffer_load_b16(rg, (unsigned)(64 * g4 + 16 * j + l15) * 2u, 0, 0);
      gB = __builtin_amdgcn_raw_buffer_load_b16(rg, (unsigned)(64 * g4 + 16 * j + l15) * 2u + 512u, 0, 0);
#pragma unroll
      for (int c = 0; c < 2; ++c)
#pragma unroll
        for (int rh = 0; rh < 2; ++rh) {
          u32x4 bv = __builtin_amdgcn_raw_buffer_load_b128(rb, (unsigned)hd * 112u + (unsigned)(4 * c + 2 * rh + hh) * 16u, 0, 0);
          if (c == 1 && rh == 1 && hh) bv = (u32x4){0u, 0u, 0u, 0u};
#pragma unroll
          for (int e = 0; e < 4; ++e) {
            acc[0][c][8 * rh + 2 * e] = bf_lo(bv[e]); acc[0][c][8 * rh + 2 * e + 1] = bf_hi(bv[e]);
            acc[1][c][8 * rh + 2 * e] = bf_lo(bv[e]); acc[1][c][8 * rh + 2 * e + 1] = bf_hi(bv[e]);
          }
        }
    }
    const unsigned gwa = 32768u + (unsigned)j * 3584u + 2560u + (unsigned)l15 * 64u + 48u;
    if (g4 < 3) *(LAS u16*)(sm + g4 * STG + gwa) = (u16)gA;
    WAITV(0);
    const u32x2 tag = mk_tag(ep);
    const unsigned xa = (unsigned)(64 * j + l31) * 128u + (unsigned)(hh ^ ((l31 >> 1) & 7)) * 16u;
    const unsigned wa = 32768u + 1536u + (unsigned)(8 * hh + q4) * 64u + (unsigned)(4 * (g4 & 1) + 2 * (p4 & 1) + (p4 >> 1)) * 8u;
    unsigned buf = 0, pbuf = 2 * STG;
#pragma unroll 1
    for (int s = 0; s < 8; ++s) {
      BARRIER();
      if (s >= 1 && s < 6 && g4 == ((s + 2) & 3)) *(LAS u16*)(sm + pbuf + gwa) = (u16)(s >= 2 ? gB : gA);
#pragma unroll
      for (int ks = 0; ks < 4; ++ks) {
        bf16x8 xf[2], wf[2];
#pragma unroll
        for (int c = 0; c < 2; ++c) wf[c] = lds_tr2(sm, buf + wa + ks * 3584 + c * 1024, buf + wa + ks * 3584 + c * 1024 + 256);
#pragma unroll
        for (int t = 0; t < 2; ++t) xf[t] = lds_frag(sm, buf + (xa ^ (unsigned)(ks * 32)) + t * 4096);
#pragma unroll
        for (int t = 0; t < 2; ++t)
#pragma unroll
          for (int c = 0; c < 2; ++c) acc[t][c] = __builtin_amdgcn_mfma_f32_32x32x16_bf16(wf[c], xf[t], acc[t][c], 0, 0, 0);
      }
      pbuf = buf;
      buf = (buf == 2 * STG) ? 0 : buf + STG;
    }
    STAMP(1);
    if (hh) {
#pragma unroll
      for (int t = 0; t < 2; ++t) {
        const float e = __builtin_amdgcn_exp2f(acc[t][1][8] * -1.4426950408889634f);
        *(LAS float*)(sm + L_G + (64 * j + 32 * t + l31) * 4) = __builtin_amdgcn_rcpf(1.f + e);
      }
    }
    BARRIER();
    __builtin_amdgcn_s_setprio(2);
    {
      const unsigned ao = WS_ACT + (unsigned)(8 * bi + 2 * j) * 114688u + (unsigned)hd * 3584u + lane * 16u;
#pragma unroll
      for (int t = 0; t < 2; ++t)
#pragma unroll
        for (int c = 0; c < 2; ++c)
#pragma unroll
          for (int rh = 0; rh < 2; ++rh) {
            u32x4 o;
#pragma unroll
            for (int e = 0; e < 4; ++e) {
              f32x2 hv = {acc[t][c][8 * rh + 2 * e], acc[t][c][8 * rh + 2 * e + 1]};
              f32x2 a = hv * 1.4426950408889634f;
              f32x2 ex = {__builtin_amdgcn_exp2f(a.x), __builtin_amdgcn_exp2f(a.y)};
              f32x2 d = ex * (ex + 2.f) + 2.f;
              f32x2 r = {__builtin_amdgcn_rcpf(d.x), __builtin_amdgcn_rcpf(d.y)};
              f32x2 y = hv * (r * -2.f + 1.f);
              o[e] = pk_bf16(y.x, y.y);
            }
            if (!(c == 1 && rh == 1) || !hh)
              __builtin_amdgcn_raw_buffer_store_b128(o, rws, ao + t * 114688 + (4 * c + 2 * rh) * 512, 0, AUX_SC1);
          }
    }
    STAMP(2);
    {
      const unsigned lo = (unsigned)(2 * j) * TILE + lane * 16u;
      const u32x4 ones = {0x3f803f80u, 0x3f803f80u, 0x3f803f80u, 0x3f803f80u};
      if (lane < 28) *(LAS u32x4*)(sm + (8 * (1 + (lane >> 2)) + 2 * j + ((lane >> 1) & 1)) * TILE + 1024 + (lane & 1) * 528 + 512) = ones;
      bool first = true;
      for (int n0 = 1; n0 < 8;) {
        const int cnt = first ? 1 : 3;
        const int nl = n0 + (lane < cnt ? lane : 0);
        glb_wait(rws, WS_FKV + (unsigned)((hd * 8 + ((bi + nl) & 7)) * 4 + j) * 8u, tag);
        if (first) {
          WAITV(0);
          if (lane == 0) __builtin_amdgcn_raw_buffer_store_b64(tag, rws, WS_FACT + (unsigned)((bi * 4 + j) * 32 + hd) * 8u, 0, AUX_SC1);
        }
        u32x4 d[3][4];
#pragma unroll
        for (int u = 0; u < 3; ++u)
          if (u < cnt) {
            const unsigned go = WS_KV + (unsigned)(hd * 8 + ((bi + n0 + u) & 7)) * 16384u + (unsigned)j * 4096u;
#pragma unroll
            for (int e = 0; e < 4; ++e) d[u][e] = __builtin_amdgcn_raw_buffer_load_b128(rws, lane * 16 + e * 1024, go, AUX_SC1);
          }
#pragma unroll
        for (int u = 0; u < 3; ++u)
          if (u < cnt) {
            LAS char* dst = sm + (n0 + u) * (8 * TILE) + lo;
            *(LAS u32x4*)(dst) = d[u][0];
            *(LAS u32x4*)(dst + 1024 + hh * 16) = d[u][1];
            *(LAS u32x4*)(dst + TILE) = d[u][2];
            *(LAS u32x4*)(dst + TILE + 1024 + hh * 16) = d[u][3];
          }
        WAITL0();
        if (lane < cnt) lds_flag_st(sm, (n0 + lane) * 4 + j);
        n0 += cnt;
        first = false;
      }
    }
    STAMP(3);
    __builtin_amdgcn_s_setprio(0);
    const int tm = hd >> 3, cp = hd & 7;
    glb_wait(rws, hh ? WS_FWT + (unsigned)(32 * cp + l31) * 8u : WS_FACT + (unsigned)((bi * 4 + tm) * 32 + 8 * j + (lane & 7)) * 8u, tag);
    STAMP(4);
    {
#pragma unroll
      for (int t = 0; t < 2; ++t)
#pragma unroll
        for (int c = 0; c < 2; ++c)
#pragma unroll
          for (int r = 0; r < 16; ++r) acc[t][c][r] = 0.f;
      const unsigned ab = WS_ACT + (unsigned)(8 * bi + 2 * tm) * 114688u + (unsigned)(28 * j) * 1024u;
      const unsigned wb = WS_WT + (unsigned)(2 * cp * 144 + 28 * j) * 1024u;
      u32x4 af[4][2], wf[4][2];
#pragma unroll
      for (int u = 0; u < 4; ++u) {
        af[u][0] = __builtin_amdgcn_raw_buffer_load_b128(rws, lane * 16 + u * 1024, ab, AUX_SC1);
        af[u][1] = __builtin_amdgcn_raw_buffer_load_b128(rws, lane * 16 + u * 1024, ab + 114688, AUX_SC1);
        wf[u][0] = __builtin_amdgcn_raw_buffer_load_b128(rws, lane * 16 + u * 1024, wb, AUX_SC1);
        wf[u][1] = __builtin_amdgcn_raw_buffer_load_b128(rws, lane * 16 + u * 1024, wb + 147456, AUX_SC1);
      }
#pragma unroll 1
      for (int it = 1; it <= 7; ++it) {
        const unsigned nx = (it < 7 ? it : 6) * 4096;
#pragma unroll
        for (int u = 0; u < 4; ++u) {
#pragma unroll
          for (int t = 0; t < 2; ++t)
#pragma unroll
            for (int c = 0; c < 2; ++c)
              acc[t][c] = __builtin_amdgcn_mfma_f32_32x32x16_bf16(__builtin_bit_cast(bf16x8, wf[u][c]), __builtin_bit_cast(bf16x8, af[u][t]), acc[t][c], 0, 0, 0);
          af[u][0] = __builtin_amdgcn_raw_buffer_load_b128(rws, lane * 16 + u * 1024, ab + nx, AUX_SC1);
          af[u][1] = __builtin_amdgcn_raw_buffer_load_b128(rws, lane * 16 + u * 1024, ab + nx + 114688, AUX_SC1);
          wf[u][0] = __builtin_amdgcn_raw_buffer_load_b128(rws, lane * 16 + u * 1024, wb + nx, AUX_SC1);
          wf[u][1] = __builtin_amdgcn_raw_buffer_load_b128(rws, lane * 16 + u * 1024, wb + nx + 147456, AUX_SC1);
          __builtin_amdgcn_sched_barrier(0);
        }
      }
      if (j > 0) lds_wait(sm, 32 + j - 1, 1, lane);
#pragma unroll
      for (int t = 0; t < 2; ++t) {
        const float om = 1.f - *(LAS float*)(sm + L_G + (64 * tm + 32 * t + l31) * 4);
#pragma unroll
        for (int c = 0; c < 2; ++c)
#pragma unroll
          for (int rq = 0; rq < 4; ++rq) {
            LAS f32x4* p = (LAS f32x4*)(sm + L_BP + ((t + 2 * c) * 4 + rq) * 1024 + lane * 16);
            f32x4 v = {acc[t][c][4 * rq] * om, acc[t][c][4 * rq + 1] * om, acc[t][c][4 * rq + 2] * om, acc[t][c][4 * rq + 3] * om};
            if (j > 0) v += *p;
            *p = v;
          }
      }
      WAITL0();
      if (lane == 0) lds_flag_st(sm, 32 + j);
    }
    STAMP(5);
    if (b == 0 && j == 0) {
#pragma unroll
      for (int u = 0; u < 4; ++u) glb_wait(rws, WS_FWT + (unsigned)(64 * u + lane) * 8u, tag);
      if (lane == 0) __builtin_amdgcn_raw_buffer_store_b64(tag, rws, WS_EPOCH, 0, AUX_SC1);
    }
    return;
  }

  STAMP(2);
  const int w = wave;
  const bf16x8 qf = lds_frag(sm, L_Q + w * 1024 + lane * 16);
  const unsigned ka = lane * 16;
  const unsigned va = 1024u + (l31 < 16 ? (unsigned)(hh * 16 + l31) * 16u : ((l31 >= 20 && l31 < 28) ? (unsigned)hh * 256u : 512u));
  const int IDX = 0xDDDD;
  f32x16 negm, O;
  {
    f32x16 z;
#pragma unroll
    for (int r = 0; r < 16; ++r) { z[r] = 0.f; O[r] = 0.f; }
    f32x16 s0 = __builtin_amdgcn_mfma_f32_32x32x16_bf16(lds_frag(sm, ka), qf, z, 0, 0, 0);
    float m = s0[0];
#pragma unroll
    for (int r = 1; r < 16; ++r) m = fmaxf(m, s0[r]);
    m = fmaxf(m, __shfl_xor(m, 32));
#pragma unroll
    for (int r = 0; r < 16; ++r) negm[r] = -m;
  }
  {
    f32x16 S = __builtin_amdgcn_mfma_f32_32x32x16_bf16(lds_frag(sm, ka), qf, negm, 0, 0, 0);
    bf16x8 kf = lds_frag(sm, ka + TILE);
    bf16x8 v0 = lds_frag(sm, va), v1 = lds_frag(sm, va + 528);
    unsigned kb = ka, vb = va;
#pragma unroll 1
    for (int n = 0; n < 8; ++n) {
#pragma unroll
      for (int t = 0; t < 8; ++t) {
        if (t == 6 && n < 7) lds_wait(sm, (n + 1) * 4, 4, lane);
        f32x16 Sn = __builtin_amdgcn_mfma_f32_32x32x16_bf16(kf, qf, negm, 0, 0, 0);
        kf = lds_frag(sm, kb + (t + 2) * TILE);
        f32x8 p0, p1;
#pragma unroll
        for (int r = 0; r < 8; ++r) p0[r] = __builtin_amdgcn_exp2f(S[r]);
        O = __builtin_amdgcn_smfmac_f32_32x32x32_bf16(v0, __builtin_bit_cast(bf16x16, p0), O, IDX, 0, 0);
        v0 = lds_frag(sm, vb + (t + 1) * TILE);
#pragma unroll
        for (int r = 0; r < 8; ++r) p1[r] = __builtin_amdgcn_exp2f(S[8 + r]);
        O = __builtin_amdgcn_smfmac_f32_32x32x32_bf16(v1, __builtin_bit_cast(bf16x16, p1), O, IDX, 0, 0);
        v1 = lds_frag(sm, vb + (t + 1) * TILE + 528);
        S = Sn;
      }
      kb += 8 * TILE; vb += 8 * TILE;
    }
  }
  STAMP(3);
  float lsum = hh ? O[12] : O[8];
  {
    float chk = lsum * 0.f;
#pragma unroll
    for (int r = 0; r < 8; ++r) chk = __builtin_fmaf(O[r], 0.f, chk);
    if (__any(chk != chk)) {
      float m = -3.0e38f;
      f32x16 z;
#pragma unroll
      for (int r = 0; r < 16; ++r) { z[r] = 0.f; O[r] = 0.f; }
#pragma unroll 1
      for (int t = 0; t < 64; ++t) {
        f32x16 S = __builtin_amdgcn_mfma_f32_32x32x16_bf16(lds_frag(sm, ka + t * TILE), qf, z, 0, 0, 0);
        float mx = m;
#pragma unroll
        for (int r = 0; r < 16; ++r) mx = fmaxf(mx, S[r]);
        mx = fmaxf(mx, __shfl_xor(mx, 32));
        const float al = __builtin_amdgcn_exp2f(m - mx);
        m = mx;
        f32x8 p0, p1;
#pragma unroll
        for (int r = 0; r < 16; ++r) O[r] *= al;
#pragma unroll
        for (int r = 0; r < 8; ++r) { p0[r] = __builtin_amdgcn_exp2f(S[r] - m); p1[r] = __builtin_amdgcn_exp2f(S[8 + r] - m); }
        O = __builtin_amdgcn_smfmac_f32_32x32x32_bf16(lds_frag(sm, va + t * TILE), __builtin_bit_cast(bf16x16, p0), O, IDX, 0, 0);
        O = __builtin_amdgcn_smfmac_f32_32x32x32_bf16(lds_frag(sm, va + t * TILE + 528), __builtin_bit_cast(bf16x16, p1), O, IDX, 0, 0);
      }
      lsum = hh ? O[12] : O[8];
    }
  }
  {
    const float sc = *(LAS float*)(sm + L_G + (32 * w + l31) * 4) * __builtin_amdgcn_rcpf(lsum);
    u32x4 o;
#pragma unroll
    for (int e = 0; e < 4; ++e) {
      unsigned u = pk_bf16(O[2 * e] * sc, O[2 * e + 1] * sc);
      o[e] = (u == 0xffffffffu) ? 0x7fc07fc0u : u;
    }
    __builtin_amdgcn_raw_buffer_store_b128(o, rws, WS_OF + (unsigned)((8 * bi + w) * 32 + hd) * 1024u + lane * 16u, 0, AUX_SC1);
  }
  {
    const int tm = hd >> 3, cp = hd & 7;
    const int tt = w & 1, tn = (w >> 1) & 1, kh = w >> 2;
    lds_wait(sm, 35, 1, lane);
    STAMP(4);
    u32x4 wf[16], of[16];
    const unsigned wb = WS_WT + (unsigned)((2 * cp + tn) * 144 + 112 + 16 * kh) * 1024u;
#pragma unroll
    for (int u = 0; u < 16; ++u) wf[u] = __builtin_amdgcn_raw_buffer_load_b128(rws, lane * 16 + (u & 3) * 1024, wb + (u >> 2) * 4096, AUX_SC1);
    f32x16 acc;
#pragma unroll
    for (int r = 0; r < 16; ++r) acc[r] = 0.f;
    const unsigned ob = WS_OF + (unsigned)((8 * bi + 2 * tm + tt) * 32 + 16 * kh) * 1024u;
#pragma unroll 1
    for (unsigned s = 0; s < SPIN_CAP; ++s) {
      CFENCE();
      unsigned bad = 0;
#pragma unroll
      for (int u = 0; u < 16; ++u) of[u] = __builtin_amdgcn_raw_buffer_load_b128(rws, lane * 16 + (u & 3) * 1024, ob + (u >> 2) * 4096, AUX_SC1);
#pragma unroll
      for (int u = 0; u < 16; ++u)
#pragma unroll
        for (int e = 0; e < 4; ++e) bad = max(bad, of[u][e]);
      if (__all(bad != 0xffffffffu)) break;
      __builtin_amdgcn_s_sleep(1);
    }
    STAMP(5);
#pragma unroll
    for (int u = 0; u < 16; ++u)
      acc = __builtin_amdgcn_mfma_f32_32x32x16_bf16(__builtin_bit_cast(bf16x8, wf[u]), __builtin_bit_cast(bf16x8, of[u]), acc, 0, 0, 0);
    LAS f32x4* xo = (LAS f32x4*)(sm + L_Q + w * 1024 + lane * 16);
    LAS f32x4* xo2 = (LAS f32x4*)(sm + L_BP + ((tt + 2 * tn) * 4 + 2 * (kh ^ 1)) * 1024 + lane * 16);
    (void)xo;
    {
      f32x4 a = xo2[0], c = xo2[64];
      f32x4 pa = kh ? (f32x4){acc[0], acc[1], acc[2], acc[3]} : (f32x4){acc[8], acc[9], acc[10], acc[11]};
      f32x4 pc = kh ? (f32x4){acc[4], acc[5], acc[6], acc[7]} : (f32x4){acc[12], acc[13], acc[14], acc[15]};
      xo2[0] = a + pa; xo2[64] = c + pc;
    }
    WAITL0();
    if (lane == 0) lds_flag_st(sm, 40 + w);
    lds_wait(sm, 40 + (w ^ 4), 1, lane);
    {
      LAS f32x4* mine = (LAS f32x4*)(sm + L_BP + ((tt + 2 * tn) * 4 + 2 * kh) * 1024 + lane * 16);
      f32x4 a = mine[0], c = mine[64];
      f32x4 pa = kh ? (f32x4){acc[8], acc[9], acc[10], acc[11]} : (f32x4){acc[0], acc[1], acc[2], acc[3]};
      f32x4 pc = kh ? (f32x4){acc[12], acc[13], acc[14], acc[15]} : (f32x4){acc[4], acc[5], acc[6], acc[7]};
      a += pa; c += pc;
      u32x4 o = {pk_bf16(a[0], a[1]), pk_bf16(a[2], a[3]), pk_bf16(c[0], c[1]), pk_bf16(c[2], c[3])};
      const rsrc_t ro = mk_rsrc(out, 2048u * 1024u);
      __builtin_amdgcn_raw_buffer_store_b128(o, ro, (unsigned)(256 * bi + 64 * tm + 32 * tt + l31) * 1024u + (unsigned)(64 * cp + 32 * tn + 16 * kh + 8 * hh) * 2u, 0, 0);
    }
  }
  STAMP(6);
}

extern "C" void kernel_launch(void* const* d_in, const int* in_sizes, int n_in, void* d_out, int out_size,
                              void* d_ws, size_t ws_size, hipStream_t stream) {
  fused_kernel<<<256, 768, 0, stream>>>((const u16*)d_in[0], (const u16*)d_in[1], (const u16*)d_in[2], (const u16*)d_in[4],
                                        (const u16*)d_in[5], (const u16*)d_in[6], (const u16*)d_in[7], (u16*)d_out,
                                        (unsigned char*)d_ws);
}
```

```cpp
#define PADTEST 0
#include <hip/hip_runtime.h>
#include <hip/hip_bf16.h>

typedef unsigned short u16;
typedef unsigned long long u64;
typedef __bf16 bf16x8 __attribute__((ext_vector_type(8)));
typedef __bf16 bf16x16 __attribute__((ext_vector_type(16)));
typedef __bf16 bf16x2 __attribute__((ext_vector_type(2)));
typedef short s16x4 __attribute__((ext_vector_type(4)));
typedef float f32x2 __attribute__((ext_vector_type(2)));
typedef float f32x4 __attribute__((ext_vector_type(4)));
typedef float f32x8 __attribute__((ext_vector_type(8)));
typedef float f32x16 __attribute__((ext_vector_type(16)));
typedef unsigned u32x2 __attribute__((ext_vector_type(2)));
typedef unsigned u32x4 __attribute__((ext_vector_type(4)));
#define LAS __attribute__((address_space(3)))
#define GAS __attribute__((address_space(1)))
typedef __amdgpu_buffer_rsrc_t rsrc_t;

constexpr unsigned WS_EPOCH = 0;
constexpr unsigned WS_FKV = 4096;
constexpr unsigned WS_FACT = WS_FKV + 8192;
constexpr unsigned WS_FWT = WS_FACT + 8192;
constexpr unsigned WS_KV = 65536;
constexpr unsigned WS_ACT = WS_KV + 32u * 8u * 16384u;
constexpr unsigned WS_WT = WS_ACT + 64u * 114688u;
constexpr unsigned WS_OF = WS_WT + 16u * 144u * 1024u;
constexpr unsigned WS_END = WS_OF + 64u * 32u * 1024u;
constexpr unsigned WS_DIAG = WS_END;

constexpr unsigned STG = 47104;
constexpr unsigned TILE = 2080;
constexpr unsigned L_BP = 64 * TILE;
constexpr unsigned L_Q = L_BP + 16384;
constexpr unsigned L_G = L_Q + 9216;
constexpr unsigned L_FL = L_G + 1024;
constexpr unsigned L_END = L_FL + 512;

constexpr unsigned SPIN_CAP = 1u << 18;
constexpr int AUX_SC1 = 16;

__device__ __forceinline__ unsigned pk_bf16(float a, float b) {
  f32x2 v = {a, b};
  return __builtin_bit_cast(unsigned, __builtin_convertvector(v, bf16x2));
}
__device__ __forceinline__ float bf_lo(unsigned u) { return __uint_as_float(u << 16); }
__device__ __forceinline__ float bf_hi(unsigned u) { return __uint_as_float(u & 0xffff0000u); }
__device__ __forceinline__ rsrc_t mk_rsrc(const void* p, unsigned bytes) {
  return __builtin_amdgcn_make_buffer_rsrc((void*)p, 0, bytes, 0x00020000);
}
#define BARRIER() asm volatile("s_waitcnt lgkmcnt(0)\n\ts_barrier" ::: "memory")
#define WAITV(n) asm volatile("s_waitcnt vmcnt(" #n ")" ::: "memory")
#define WAITL0() asm volatile("s_waitcnt lgkmcnt(0)" ::: "memory")
#define CFENCE() asm volatile("" ::: "memory")

#define STAMP(k) do { if ((k) == 0) pad_t0 = __builtin_amdgcn_s_memrealtime(); if ((k) == PADTEST) pad_t1 = __builtin_amdgcn_s_memrealtime(); \
    if ((k) == 6) { while (__builtin_amdgcn_s_memrealtime() < pad_t1 + 6000ull) __builtin_amdgcn_s_sleep(4); } } while (0)

__device__ __forceinline__ bf16x8 lds_frag(LAS char* sm, unsigned off) { return *(LAS bf16x8*)(sm + off); }
__device__ __forceinline__ bf16x8 lds_tr2(LAS char* sm, unsigned o0, unsigned o1) {
  s16x4 a = __builtin_amdgcn_ds_read_tr16_b64_v4i16((LAS s16x4*)(sm + o0));
  s16x4 b = __builtin_amdgcn_ds_read_tr16_b64_v4i16((LAS s16x4*)(sm + o1));
  return __builtin_bit_cast(bf16x8, __builtin_shufflevector(a, b, 0, 1, 2, 3, 4, 5, 6, 7));
}
__device__ __forceinline__ unsigned lds_flag_ld(LAS char* sm, unsigned idx) {
  return __hip_atomic_load((LAS unsigned*)(sm + L_FL) + idx, __ATOMIC_RELAXED, __HIP_MEMORY_SCOPE_WORKGROUP);
}
__device__ __forceinline__ void lds_flag_st(LAS char* sm, unsigned idx) {
  __hip_atomic_store((LAS unsigned*)(sm + L_FL) + idx, 1u, __ATOMIC_RELAXED, __HIP_MEMORY_SCOPE_WORKGROUP);
}
__device__ __forceinline__ void lds_wait(LAS char* sm, unsigned idx0, unsigned n, int lane) {
#pragma unroll 1
  for (unsigned s = 0; s < SPIN_CAP * 4; ++s) {
    CFENCE();
    unsigned v = lds_flag_ld(sm, idx0 + ((unsigned)lane < n ? lane : 0));
    if (__all(v != 0)) break;
    __builtin_amdgcn_s_sleep(1);
  }
  CFENCE();
}
__device__ __forceinline__ void glb_wait(rsrc_t rs, unsigned off, u32x2 tag) {
#pragma unroll 1
  for (unsigned s = 0; s < SPIN_CAP; ++s) {
    CFENCE();
    u32x2 v = __builtin_amdgcn_raw_buffer_load_b64(rs, off, 0, AUX_SC1);
    if (__all(v.x == tag.x && v.y == tag.y)) break;
    __builtin_amdgcn_s_sleep(2);
  }
  CFENCE();
}

__global__ void __launch_bounds__(768, 3)
fused_kernel(const u16* __restrict__ x, const u16* __restrict__ wqkv, const u16* __restrict__ wo,
             const u16* __restrict__ w1, const u16* __restrict__ b1, const u16* __restrict__ w2,
             const u16* __restrict__ gw, u16* __restrict__ out, unsigned char* __restrict__ ws) {
  __shared__ __attribute__((aligned(16))) char smem_[L_END];
  LAS char* sm = (LAS char*)smem_;
  const int lane = threadIdx.x & 63;
  const int wave = __builtin_amdgcn_readfirstlane(threadIdx.x >> 6);
  const int b = blockIdx.x, xcd = b & 7, idx = b >> 3;
  const int bi = 4 * (xcd & 1) + (idx & 3);
  const int hd = 8 * (xcd >> 1) + (idx >> 2);
  const int hh = lane >> 5, l31 = lane & 31, l15 = lane & 15, g4 = lane >> 4;
  const int q4 = l15 >> 2, p4 = lane & 3;
  const rsrc_t rws = mk_rsrc(ws, WS_END);
  u64 pad_t0 = 0, pad_t1 = 0;
  STAMP(0);

  if (wave >= 4 && wave < 8) {
    const int lw = wave - 4;
    const rsrc_t rx = mk_rsrc(x, 2048u * 1024u), rq = mk_rsrc(wqkv, 512u * 3072u), r1 = mk_rsrc(w1, 512u * 3584u);
    const int r8 = lane >> 3, slot = lane & 7;
    const unsigned xo0 = (unsigned)(256 * bi + 64 * lw + r8) * 1024u + (unsigned)(slot ^ (r8 >> 1)) * 16u;
    const unsigned xo1 = (unsigned)(256 * bi + 64 * lw + r8 + 8) * 1024u + (unsigned)(slot ^ (r8 >> 1) ^ 4) * 16u;
    const int pos = (lane >> 1) & 15, wrow = pos ^ ((pos >> 3) << 2);
    const unsigned qo = (unsigned)(16 * lw + wrow) * 3072u + (unsigned)hh * 1024u + (unsigned)hd * 32u + (lane & 1) * 16u;
    const unsigned vo = (unsigned)(16 * lw + wrow) * 3072u + 2048u + (unsigned)hd * 32u + (lane & 1) * 16u;
    const unsigned wo1 = (unsigned)(16 * lw + (lane >> 2)) * 3584u + (unsigned)hd * 112u + (unsigned)p4 * 16u;
    auto issue = [&](int s, unsigned buf) {
      LAS char* xb = sm + buf + lw * 8192;
#pragma unroll
      for (int n = 0; n < 4; ++n) {
        __builtin_amdgcn_raw_ptr_buffer_load_lds(rx, xb + n * 2048, 16, xo0, s * 128 + n * 16384, 0, 0);
        __builtin_amdgcn_raw_ptr_buffer_load_lds(rx, xb + n * 2048 + 1024, 16, xo1, s * 128 + n * 16384, 0, 0);
      }
      LAS char* wb = sm + buf + 32768 + lw * 3584;
      __builtin_amdgcn_raw_ptr_buffer_load_lds(rq, wb, 16, qo, s * (64 * 3072), 0, 0);
      if (lane < 32) __builtin_amdgcn_raw_ptr_buffer_load_lds(rq, wb + 1024, 16, vo, s * (64 * 3072), 0, 0);
      __builtin_amdgcn_raw_ptr_buffer_load_lds(r1, wb + 1536, 16, wo1, s * (64 * 3584), 0, 0);
      if (p4 != 3) __builtin_amdgcn_raw_ptr_buffer_load_lds(r1, wb + 2560, 16, wo1 + 64, s * (64 * 3584), 0, 0);
    };
    issue(0, 0);
    issue(1, STG);
    issue(2, 2 * STG);
    const int ct = b >> 4, kk9 = (b & 15) * 9;
    {
      const rsrc_t r2 = mk_rsrc(w2, 1792u * 1024u), ro = mk_rsrc(wo, 512u * 1024u);
      const unsigned so = (unsigned)(lane >> 2) * 1024u + (unsigned)ct * 64u + (unsigned)p4 * 16u;
      for (int f = lw; f < 9; f += 4) {
        const int ks = kk9 + f;
        if (ks < 112) __builtin_amdgcn_raw_ptr_buffer_load_lds(r2, sm + L_Q + f * 1024, 16, so, ks * 16384, 0, 0);
        else __builtin_amdgcn_raw_ptr_buffer_load_lds(ro, sm + L_Q + f * 1024, 16, so, (ks - 112) * 16384, 0, 0);
      }
    }
    WAITV(26); BARRIER();
    WAITV(14); BARRIER(); issue(3, 0);
    WAITV(14); BARRIER(); issue(4, STG);
    WAITV(12); BARRIER(); issue(5, 2 * STG);
    WAITV(12); BARRIER(); issue(6, 0);
    WAITV(12); BARRIER(); issue(7, STG);
    {
      const unsigned ta = L_Q + (unsigned)(8 * hh + q4) * 64u + (unsigned)(4 * (g4 & 1) + 2 * (p4 & 1) + (p4 >> 1)) * 8u;
      for (int f = lw; f < 9; f += 4) {
        u32x2 a, c;
        asm volatile("ds_read_b64_tr_b16 %0, %2\n\tds_read_b64_tr_b16 %1, %2 offset:256\n\ts_waitcnt lgkmcnt(0)"
                     : "=&v"(a), "=&v"(c) : "v"((unsigned)(size_t)sm + ta + f * 1024) : "memory");
        u32x4 v = {a.x, a.y, c.x, c.y};
        __builtin_amdgcn_raw_buffer_store_b128(v, rws, WS_WT + lane * 16, (ct * 144 + kk9 + f) * 1024, AUX_SC1);
      }
    }
    WAITV(14); BARRIER();
    WAITV(0); BARRIER();
  }

  auto mk_tag = [&](u32x2 e) {
    u64 z = ((u64)__builtin_amdgcn_readfirstlane(e.y) << 32 | (u64)__builtin_amdgcn_readfirstlane(e.x)) + 0x9E3779B97F4A7C15ull;
    z = (z ^ (z >> 30)) * 0xBF58476D1CE4E5B9ull;
    z = (z ^ (z >> 27)) * 0x94D049BB133111EBull;
    z ^= z >> 31;
    return (u32x2){(unsigned)z, (unsigned)(z >> 32)};
  };

  if (wave < 4) {
    const int j = wave;
    const u32x2 ep = __builtin_amdgcn_raw_buffer_load_b64(rws, WS_EPOCH, 0, AUX_SC1);
    if (wave == 0) ((LAS unsigned*)(sm + L_FL))[lane] = 0u;
    f32x4 acc[4][3];
#pragma unroll
    for (int t = 0; t < 4; ++t)
#pragma unroll
      for (int c = 0; c < 3; ++c) acc[t][c] = (f32x4){0.f, 0.f, 0.f, 0.f};
    const unsigned xa0 = (unsigned)(64 * j + l15) * 128u + (unsigned)(g4 ^ (l15 >> 1)) * 16u;
    const unsigned xa1 = xa0 ^ 64u;
    const unsigned wa0 = 32768u + (unsigned)(g4 >> 1) * 3584u + (unsigned)(8 * (g4 & 1) + 4 * (g4 & 1) + q4) * 32u + p4 * 8u;
    const unsigned wa1 = 32768u + (unsigned)(g4 >> 1) * 3584u + (unsigned)(8 * (g4 & 1) + (4 ^ (4 * (g4 & 1))) + q4) * 32u + p4 * 8u;
    unsigned buf = 0;
    WAITV(0);
    const u32x2 tag = mk_tag(ep);
#pragma unroll 1
    for (int s = 0; s < 8; ++s) {
      BARRIER();
#pragma unroll
      for (int kk = 0; kk < 2; ++kk) {
        bf16x8 xf[4], wf[3];
#pragma unroll
        for (int c = 0; c < 3; ++c) wf[c] = lds_tr2(sm, buf + wa0 + kk * 7168 + c * 512, buf + wa1 + kk * 7168 + c * 512);
#pragma unroll
        for (int t = 0; t < 4; ++t) xf[t] = lds_frag(sm, buf + (kk ? xa1 : xa0) + t * 2048);
#pragma unroll
        for (int t = 0; t < 4; ++t) {
          acc[t][0] = __builtin_amdgcn_mfma_f32_16x16x32_bf16(wf[0], xf[t], acc[t][0], 0, 0, 0);
          acc[t][1] = __builtin_amdgcn_mfma_f32_16x16x32_bf16(wf[1], xf[t], acc[t][1], 0, 0, 0);
          acc[t][2] = __builtin_amdgcn_mfma_f32_16x16x32_bf16(xf[t], wf[2], acc[t][2], 0, 0, 0);
        }
      }
      buf = (buf == 2 * STG) ? 0 : buf + STG;
    }
    STAMP(1);
    const float qs = 0.25f * 1.4426950408889634f;
    const int rho = (l15 & 3) + 8 * ((l15 >> 2) & 1) + 4 * (l15 >> 3);
#pragma unroll
    for (int t = 0; t < 4; ++t) {
      const unsigned tl = 2 * j + (t >> 1);
      const unsigned o = (unsigned)(16 * (t & 1) + l15 + 32 * (g4 >> 1)) * 16u + (g4 & 1) * 8u;
      *(LAS u32x2*)(sm + L_Q + tl * 1024 + o) = (u32x2){pk_bf16(acc[t][0][0] * qs, acc[t][0][1] * qs), pk_bf16(acc[t][0][2] * qs, acc[t][0][3] * qs)};
      *(LAS u32x2*)(sm + tl * TILE + o) = (u32x2){pk_bf16(acc[t][1][0], acc[t][1][1]), pk_bf16(acc[t][1][2], acc[t][1][3])};
      *(LAS u32x2*)(sm + tl * TILE + 1024 + (t & 1) * 528 + (unsigned)((g4 >> 1) * 16 + rho) * 16u + (g4 & 1) * 8u) =
          (u32x2){pk_bf16(acc[t][2][0], acc[t][2][1]), pk_bf16(acc[t][2][2], acc[t][2][3])};
    }
    if (lane < 4) *(LAS u32x4*)(sm + (2 * j + (lane >> 1)) * TILE + 1024 + (lane & 1) * 528 + 512) = (u32x4){0x3f803f80u, 0x3f803f80u, 0x3f803f80u, 0x3f803f80u};
    WAITL0();
    {
      const unsigned go = WS_KV + (unsigned)(hd * 8 + bi) * 16384u + (unsigned)j * 4096u + lane * 16u;
#pragma unroll
      for (int t = 0; t < 2; ++t) {
        u32x4 kf = *(LAS u32x4*)(sm + (2 * j + t) * TILE + lane * 16);
        u32x4 vf = *(LAS u32x4*)(sm + (2 * j + t) * TILE + 1024 + lane * 16 + hh * 16);
        __builtin_amdgcn_raw_buffer_store_b128(kf, rws, go + t * 2048, 0, AUX_SC1);
        __builtin_amdgcn_raw_buffer_store_b128(vf, rws, go + t * 2048 + 1024, 0, AUX_SC1);
      }
    }
    BARRIER();
    if (wave == 0 && lane == 0) __builtin_amdgcn_raw_buffer_store_b64(tag, rws, WS_FWT + (unsigned)b * 8u, 0, AUX_SC1);
    WAITV(0);
    if (lane == 0) __builtin_amdgcn_raw_buffer_store_b64(tag, rws, WS_FKV + (unsigned)((hd * 8 + bi) * 4 + j) * 8u, 0, AUX_SC1);
  } else if (wave < 8) {
    BARRIER();
  } else {
    const int j = wave - 8;
    const u32x2 ep = __builtin_amdgcn_raw_buffer_load_b64(rws, WS_EPOCH, 0, AUX_SC1);
    {
      const u32x4 inv = {0xffffffffu, 0xffffffffu, 0xffffffffu, 0xffffffffu};
      const unsigned oo = WS_OF + (unsigned)((8 * bi + 2 * j) * 32 + hd) * 1024u + lane * 16u;
      __builtin_amdgcn_raw_buffer_store_b128(inv, rws, oo, 0, AUX_SC1);
      __builtin_amdgcn_raw_buffer_store_b128(inv, rws, oo + 32768, 0, AUX_SC1);
    }
    f32x16 acc[2][2];
    unsigned gA, gB;
    {
      const rsrc_t rb = mk_rsrc(b1, 3584u), rg = mk_rsrc(gw, 1024u);
      gA = __builtin_amdgcn_raw_buffer_load_b16(rg, (unsigned)(64 * g4 + 16 * j + l15) * 2u, 0, 0);
      gB = __builtin_amdgcn_raw_buffer_load_b16(rg, (unsigned)(64 * g4 + 16 * j + l15) * 2u + 512u, 0, 0);
#pragma unroll
      for (int c = 0; c < 2; ++c)
#pragma unroll
        for (int rh = 0; rh < 2; ++rh) {
          u32x4 bv = __builtin_amdgcn_raw_buffer_load_b128(rb, (unsigned)hd * 112u + (unsigned)(4 * c + 2 * rh + hh) * 16u, 0, 0);
          if (c == 1 && rh == 1 && hh) bv = (u32x4){0u, 0u, 0u, 0u};
#pragma unroll
          for (int e = 0; e < 4; ++e) {
            acc[0][c][8 * rh + 2 * e] = bf_lo(bv[e]); acc[0][c][8 * rh + 2 * e + 1] = bf_hi(bv[e]);
            acc[1][c][8 * rh + 2 * e] = bf_lo(bv[e]); acc[1][c][8 * rh + 2 * e + 1] = bf_hi(bv[e]);
          }
        }
    }
    const unsigned gwa = 32768u + (unsigned)j * 3584u + 2560u + (unsigned)l15 * 64u + 48u;
    if (g4 < 3) *(LAS u16*)(sm + g4 * STG + gwa) = (u16)gA;
    WAITV(0);
    const u32x2 tag = mk_tag(ep);
    const unsigned xa = (unsigned)(64 * j + l31) * 128u + (unsigned)(hh ^ ((l31 >> 1) & 7)) * 16u;
    const unsigned wa = 32768u + 1536u + (unsigned)(8 * hh + q4) * 64u + (unsigned)(4 * (g4 & 1) + 2 * (p4 & 1) + (p4 >> 1)) * 8u;
    unsigned buf = 0, pbuf = 2 * STG;
#pragma unroll 1
    for (int s = 0; s < 8; ++s) {
      BARRIER();
      if (s >= 1 && s < 6 && g4 == ((s + 2) & 3)) *(LAS u16*)(sm + pbuf + gwa) = (u16)(s >= 2 ? gB : gA);
#pragma unroll
      for (int ks = 0; ks < 4; ++ks) {
        bf16x8 xf[2], wf[2];
#pragma unroll
        for (int c = 0; c < 2; ++c) wf[c] = lds_tr2(sm, buf + wa + ks * 3584 + c * 1024, buf + wa + ks * 3584 + c * 1024 + 256);
#pragma unroll
        for (int t = 0; t < 2; ++t) xf[t] = lds_frag(sm, buf + (xa ^ (unsigned)(ks * 32)) + t * 4096);
#pragma unroll
        for (int t = 0; t < 2; ++t)
#pragma unroll
          for (int c = 0; c < 2; ++c) acc[t][c] = __builtin_amdgcn_mfma_f32_32x32x16_bf16(wf[c], xf[t], acc[t][c], 0, 0, 0);
      }
      pbuf = buf;
      buf = (buf == 2 * STG) ? 0 : buf + STG;
    }
    STAMP(1);
    if (hh) {
#pragma unroll
      for (int t = 0; t < 2; ++t) {
        const float e = __builtin_amdgcn_exp2f(acc[t][1][8] * -1.4426950408889634f);
        *(LAS float*)(sm + L_G + (64 * j + 32 * t + l31) * 4) = __builtin_amdgcn_rcpf(1.f + e);
      }
    }
    BARRIER();
    __builtin_amdgcn_s_setprio(2);
    {
      const unsigned ao = WS_ACT + (unsigned)(8 * bi + 2 * j) * 114688u + (unsigned)hd * 3584u + lane * 16u;
#pragma unroll
      for (int t = 0; t < 2; ++t)
#pragma unroll
        for (int c = 0; c < 2; ++c)
#pragma unroll
          for (int rh = 0; rh < 2; ++rh) {
            u32x4 o;
#pragma unroll
            for (int e = 0; e < 4; ++e) {
              f32x2 hv = {acc[t][c][8 * rh + 2 * e], acc[t][c][8 * rh + 2 * e + 1]};
              f32x2 a = hv * 1.4426950408889634f;
              f32x2 ex = {__builtin_amdgcn_exp2f(a.x), __builtin_amdgcn_exp2f(a.y)};
              f32x2 d = ex * (ex + 2.f) + 2.f;
              f32x2 r = {__builtin_amdgcn_rcpf(d.x), __builtin_amdgcn_rcpf(d.y)};
              f32x2 y = hv * (r * -2.f + 1.f);
              o[e] = pk_bf16(y.x, y.y);
            }
            if (!(c == 1 && rh == 1) || !hh)
              __builtin_amdgcn_raw_buffer_store_b128(o, rws, ao + t * 114688 + (4 * c + 2 * rh) * 512, 0, AUX_SC1);
          }
    }
    STAMP(2);
    {
      const unsigned lo = (unsigned)(2 * j) * TILE + lane * 16u;
      const u32x4 ones = {0x3f803f80u, 0x3f803f80u, 0x3f803f80u, 0x3f803f80u};
      if (lane < 28) *(LAS u32x4*)(sm + (8 * (1 + (lane >> 2)) + 2 * j + ((lane >> 1) & 1)) * TILE + 1024 + (lane & 1) * 528 + 512) = ones;
      bool first = true;
      for (int n0 = 1; n0 < 8;) {
        const int cnt = first ? 1 : 3;
        const int nl = n0 + (lane < cnt ? lane : 0);
        glb_wait(rws, WS_FKV + (unsigned)((hd * 8 + ((bi + nl) & 7)) * 4 + j) * 8u, tag);
        if (first) {
          WAITV(0);
          if (lane == 0) __builtin_amdgcn_raw_buffer_store_b64(tag, rws, WS_FACT + (unsigned)((bi * 4 + j) * 32 + hd) * 8u, 0, AUX_SC1);
        }
        u32x4 d[3][4];
#pragma unroll
        for (int u = 0; u < 3; ++u)
          if (u < cnt) {
            const unsigned go = WS_KV + (unsigned)(hd * 8 + ((bi + n0 + u) & 7)) * 16384u + (unsigned)j * 4096u;
#pragma unroll
            for (int e = 0; e < 4; ++e) d[u][e] = __builtin_amdgcn_raw_buffer_load_b128(rws, lane * 16 + e * 1024, go, AUX_SC1);
          }
#pragma unroll
        for (int u = 0; u < 3; ++u)
          if (u < cnt) {
            LAS char* dst = sm + (n0 + u) * (8 * TILE) + lo;
            *(LAS u32x4*)(dst) = d[u][0];
            *(LAS u32x4*)(dst + 1024 + hh * 16) = d[u][1];
            *(LAS u32x4*)(dst + TILE) = d[u][2];
            *(LAS u32x4*)(dst + TILE + 1024 + hh * 16) = d[u][3];
          }
        WAITL0();
        if (lane < cnt) lds_flag_st(sm, (n0 + lane) * 4 + j);
        n0 += cnt;
        first = false;
      }
    }
    STAMP(3);
    __builtin_amdgcn_s_setprio(0);
    const int tm = hd >> 3, cp = hd & 7;
    glb_wait(rws, hh ? WS_FWT + (unsigned)(32 * cp + l31) * 8u : WS_FACT + (unsigned)((bi * 4 + tm) * 32 + 8 * j + (lane & 7)) * 8u, tag);
    STAMP(4);
    {
#pragma unroll
      for (int t = 0; t < 2; ++t)
#pragma unroll
        for (int c = 0; c < 2; ++c)
#pragma unroll
          for (int r = 0; r < 16; ++r) acc[t][c][r] = 0.f;
      const unsigned ab = WS_ACT + (unsigned)(8 * bi + 2 * tm) * 114688u + (unsigned)(28 * j) * 1024u;
      const unsigned wb = WS_WT + (unsigned)(2 * cp * 144 + 28 * j) * 1024u;
      u32x4 af[4][2], wf[4][2];
#pragma unroll
      for (int u = 0; u < 4; ++u) {
        af[u][0] = __builtin_amdgcn_raw_buffer_load_b128(rws, lane * 16 + u * 1024, ab, AUX_SC1);
        af[u][1] = __builtin_amdgcn_raw_buffer_load_b128(rws, lane * 16 + u * 1024, ab + 114688, AUX_SC1);
        wf[u][0] = __builtin_amdgcn_raw_buffer_load_b128(rws, lane * 16 + u * 1024, wb, AUX_SC1);
        wf[u][1] = __builtin_amdgcn_raw_buffer_load_b128(rws, lane * 16 + u * 1024, wb + 147456, AUX_SC1);
      }
#pragma unroll 1
      for (int it = 1; it <= 7; ++it) {
        const unsigned nx = (it < 7 ? it : 6) * 4096;
#pragma unroll
        for (int u = 0; u < 4; ++u) {
#pragma unroll
          for (int t = 0; t < 2; ++t)
#pragma unroll
            for (int c = 0; c < 2; ++c)
              acc[t][c] = __builtin_amdgcn_mfma_f32_32x32x16_bf16(__builtin_bit_cast(bf16x8, wf[u][c]), __builtin_bit_cast(bf16x8, af[u][t]), acc[t][c], 0, 0, 0);
          af[u][0] = __builtin_amdgcn_raw_buffer_load_b128(rws, lane * 16 + u * 1024, ab + nx, AUX_SC1);
          af[u][1] = __builtin_amdgcn_raw_buffer_load_b128(rws, lane * 16 + u * 1024, ab + nx + 114688, AUX_SC1);
          wf[u][0] = __builtin_amdgcn_raw_buffer_load_b128(rws, lane * 16 + u * 1024, wb + nx, AUX_SC1);
          wf[u][1] = __builtin_amdgcn_raw_buffer_load_b128(rws, lane * 16 + u * 1024, wb + nx + 147456, AUX_SC1);
          __builtin_amdgcn_sched_barrier(0);
        }
      }
      if (j > 0) lds_wait(sm, 32 + j - 1, 1, lane);
#pragma unroll
      for (int t = 0; t < 2; ++t) {
        const float om = 1.f - *(LAS float*)(sm + L_G + (64 * tm + 32 * t + l31) * 4);
#pragma unroll
        for (int c = 0; c < 2; ++c)
#pragma unroll
          for (int rq = 0; rq < 4; ++rq) {
            LAS f32x4* p = (LAS f32x4*)(sm + L_BP + ((t + 2 * c) * 4 + rq) * 1024 + lane * 16);
            f32x4 v = {acc[t][c][4 * rq] * om, acc[t][c][4 * rq + 1] * om, acc[t][c][4 * rq + 2] * om, acc[t][c][4 * rq + 3] * om};
            if (j > 0) v += *p;
            *p = v;
          }
      }
      WAITL0();
      if (lane == 0) lds_flag_st(sm, 32 + j);
    }
    STAMP(5);
    if (b == 0 && j == 0) {
#pragma unroll
      for (int u = 0; u < 4; ++u) glb_wait(rws, WS_FWT + (unsigned)(64 * u + lane) * 8u, tag);
      if (lane == 0) __builtin_amdgcn_raw_buffer_store_b64(tag, rws, WS_EPOCH, 0, AUX_SC1);
    }
    return;
  }

  STAMP(2);
  const int w = wave;
  const bf16x8 qf = lds_frag(sm, L_Q + w * 1024 + lane * 16);
  const unsigned ka = lane * 16;
  const unsigned va = 1024u + (l31 < 16 ? (unsigned)(hh * 16 + l31) * 16u : ((l31 >= 20 && l31 < 28) ? (unsigned)hh * 256u : 512u));
  const int IDX = 0xDDDD;
  f32x16 negm, O;
  {
    f32x16 z;
#pragma unroll
    for (int r = 0; r < 16; ++r) { z[r] = 0.f; O[r] = 0.f; }
    f32x16 s0 = __builtin_amdgcn_mfma_f32_32x32x16_bf16(lds_frag(sm, ka), qf, z, 0, 0, 0);
    float m = s0[0];
#pragma unroll
    for (int r = 1; r < 16; ++r) m = fmaxf(m, s0[r]);
    m = fmaxf(m, __shfl_xor(m, 32));
#pragma unroll
    for (int r = 0; r < 16; ++r) negm[r] = -m;
  }
  {
    f32x16 S = __builtin_amdgcn_mfma_f32_32x32x16_bf16(lds_frag(sm, ka), qf, negm, 0, 0, 0);
    bf16x8 kf = lds_frag(sm, ka + TILE);
    bf16x8 v0 = lds_frag(sm, va), v1 = lds_frag(sm, va + 528);
    unsigned kb = ka, vb = va;
#pragma unroll 1
    for (int n = 0; n < 8; ++n) {
#pragma unroll
      for (int t = 0; t < 8; ++t) {
        if (t == 6 && n < 7) lds_wait(sm, (n + 1) * 4, 4, lane);
        f32x16 Sn = __builtin_amdgcn_mfma_f32_32x32x16_bf16(kf, qf, negm, 0, 0, 0);
        kf = lds_frag(sm, kb + (t + 2) * TILE);
        f32x8 p0, p1;
#pragma unroll
        for (int r = 0; r < 8; ++r) p0[r] = __builtin_amdgcn_exp2f(S[r]);
        O = __builtin_amdgcn_smfmac_f32_32x32x32_bf16(v0, __builtin_bit_cast(bf16x16, p0), O, IDX, 0, 0);
        v0 = lds_frag(sm, vb + (t + 1) * TILE);
#pragma unroll
        for (int r = 0; r < 8; ++r) p1[r] = __builtin_amdgcn_exp2f(S[8 + r]);
        O = __builtin_amdgcn_smfmac_f32_32x32x32_bf16(v1, __builtin_bit_cast(bf16x16, p1), O, IDX, 0, 0);
        v1 = lds_frag(sm, vb + (t + 1) * TILE + 528);
        S = Sn;
      }
      kb += 8 * TILE; vb += 8 * TILE;
    }
  }
  STAMP(3);
  float lsum = hh ? O[12] : O[8];
  {
    float chk = lsum * 0.f;
#pragma unroll
    for (int r = 0; r < 8; ++r) chk = __builtin_fmaf(O[r], 0.f, chk);
    if (__any(chk != chk)) {
      float m = -3.0e38f;
      f32x16 z;
#pragma unroll
      for (int r = 0; r < 16; ++r) { z[r] = 0.f; O[r] = 0.f; }
#pragma unroll 1
      for (int t = 0; t < 64; ++t) {
        f32x16 S = __builtin_amdgcn_mfma_f32_32x32x16_bf16(lds_frag(sm, ka + t * TILE), qf, z, 0, 0, 0);
        float mx = m;
#pragma unroll
        for (int r = 0; r < 16; ++r) mx = fmaxf(mx, S[r]);
        mx = fmaxf(mx, __shfl_xor(mx, 32));
        const float al = __builtin_amdgcn_exp2f(m - mx);
        m = mx;
        f32x8 p0, p1;
#pragma unroll
        for (int r = 0; r < 16; ++r) O[r] *= al;
#pragma unroll
        for (int r = 0; r < 8; ++r) { p0[r] = __builtin_amdgcn_exp2f(S[r] - m); p1[r] = __builtin_amdgcn_exp2f(S[8 + r] - m); }
        O = __builtin_amdgcn_smfmac_f32_32x32x32_bf16(lds_frag(sm, va + t * TILE), __builtin_bit_cast(bf16x16, p0), O, IDX, 0, 0);
        O = __builtin_amdgcn_smfmac_f32_32x32x32_bf16(lds_frag(sm, va + t * TILE + 528), __builtin_bit_cast(bf16x16, p1), O, IDX, 0, 0);
      }
      lsum = hh ? O[12] : O[8];
    }
  }
  {
    const float sc = *(LAS float*)(sm + L_G + (32 * w + l31) * 4) * __builtin_amdgcn_rcpf(lsum);
    u32x4 o;
#pragma unroll
    for (int e = 0; e < 4; ++e) {
      unsigned u = pk_bf16(O[2 * e] * sc, O[2 * e + 1] * sc);
      o[e] = (u == 0xffffffffu) ? 0x7fc07fc0u : u;
    }
    __builtin_amdgcn_raw_buffer_store_b128(o, rws, WS_OF + (unsigned)((8 * bi + w) * 32 + hd) * 1024u + lane * 16u, 0, AUX_SC1);
  }
  {
    const int tm = hd >> 3, cp = hd & 7;
    const int tt = w & 1, tn = (w >> 1) & 1, kh = w >> 2;
    lds_wait(sm, 35, 1, lane);
    STAMP(4);
    u32x4 wf[16], of[16];
    const unsigned wb = WS_WT + (unsigned)((2 * cp + tn) * 144 + 112 + 16 * kh) * 1024u;
#pragma unroll
    for (int u = 0; u < 16; ++u) wf[u] = __builtin_amdgcn_raw_buffer_load_b128(rws, lane * 16 + (u & 3) * 1024, wb + (u >> 2) * 4096, AUX_SC1);
    f32x16 acc;
#pragma unroll
    for (int r = 0; r < 16; ++r) acc[r] = 0.f;
    const unsigned ob = WS_OF + (unsigned)((8 * bi + 2 * tm + tt) * 32 + 16 * kh) * 1024u;
#pragma unroll 1
    for (unsigned s = 0; s < SPIN_CAP; ++s) {
      CFENCE();
      unsigned bad = 0;
#pragma unroll
      for (int u = 0; u < 16; ++u) of[u] = __builtin_amdgcn_raw_buffer_load_b128(rws, lane * 16 + (u & 3) * 1024, ob + (u >> 2) * 4096, AUX_SC1);
#pragma unroll
      for (int u = 0; u < 16; ++u)
#pragma unroll
        for (int e = 0; e < 4; ++e) bad = max(bad, of[u][e]);
      if (__all(bad != 0xffffffffu)) break;
      __builtin_amdgcn_s_sleep(1);
    }
    STAMP(5);
#pragma unroll
    for (int u = 0; u < 16; ++u)
      acc = __builtin_amdgcn_mfma_f32_32x32x16_bf16(__builtin_bit_cast(bf16x8, wf[u]), __builtin_bit_cast(bf16x8, of[u]), acc, 0, 0, 0);
    LAS f32x4* xo = (LAS f32x4*)(sm + L_Q + w * 1024 + lane * 16);
    LAS f32x4* xo2 = (LAS f32x4*)(sm + L_BP + ((tt + 2 * tn) * 4 + 2 * (kh ^ 1)) * 1024 + lane * 16);
    (void)xo;
    {
      f32x4 a = xo2[0], c = xo2[64];
      f32x4 pa = kh ? (f32x4){acc[0], acc[1], acc[2], acc[3]} : (f32x4){acc[8], acc[9], acc[10], acc[11]};
      f32x4 pc = kh ? (f32x4){acc[4], acc[5], acc[6], acc[7]} : (f32x4){acc[12], acc[13], acc[14], acc[15]};
      xo2[0] = a + pa; xo2[64] = c + pc;
    }
    WAITL0();
    if (lane == 0) lds_flag_st(sm, 40 + w);
    lds_wait(sm, 40 + (w ^ 4), 1, lane);
    {
      LAS f32x4* mine = (LAS f32x4*)(sm + L_BP + ((tt + 2 * tn) * 4 + 2 * kh) * 1024 + lane * 16);
      f32x4 a = mine[0], c = mine[64];
      f32x4 pa = kh ? (f32x4){acc[8], acc[9], acc[10], acc[11]} : (f32x4){acc[0], acc[1], acc[2], acc[3]};
      f32x4 pc = kh ? (f32x4){acc[12], acc[13], acc[14], acc[15]} : (f32x4){acc[4], acc[5], acc[6], acc[7]};
      a += pa; c += pc;
      u32x4 o = {pk_bf16(a[0], a[1]), pk_bf16(a[2], a[3]), pk_bf16(c[0], c[1]), pk_bf16(c[2], c[3])};
      const rsrc_t ro = mk_rsrc(out, 2048u * 1024u);
      __builtin_amdgcn_raw_buffer_store_b128(o, ro, (unsigned)(256 * bi + 64 * tm + 32 * tt + l31) * 1024u + (unsigned)(64 * cp + 32 * tn + 16 * kh + 8 * hh) * 2u, 0, 0);
    }
  }
  STAMP(6);
}

extern "C" void kernel_launch(void* const* d_in, const int* in_sizes, int n_in, void* d_out, int out_size,
                              void* d_ws, size_t ws_size, hipStream_t stream) {
  fused_kernel<<<256, 768, 0, stream>>>((const u16*)d_in[0], (const u16*)d_in[1], (const u16*)d_in[2], (const u16*)d_in[4],
                                        (const u16*)d_in[5], (const u16*)d_in[6], (const u16*)d_in[7], (u16*)d_out,
                                        (unsigned char*)d_ws);
}
```

```cpp
#include <hip/hip_runtime.h>
#include <hip/hip_bf16.h>

typedef unsigned short u16;
typedef unsigned long long u64;
typedef __bf16 bf16x8 __attribute__((ext_vector_type(8)));
typedef __bf16 bf16x16 __attribute__((ext_vector_type(16)));
typedef __bf16 bf16x2 __attribute__((ext_vector_type(2)));
typedef short s16x4 __attribute__((ext_vector_type(4)));
typedef float f32x2 __attribute__((ext_vector_type(2)));
typedef float f32x4 __attribute__((ext_vector_type(4)));
typedef float f32x8 __attribute__((ext_vector_type(8)));
typedef float f32x16 __attribute__((ext_vector_type(16)));
typedef unsigned u32x2 __attribute__((ext_vector_type(2)));
typedef unsigned u32x4 __attribute__((ext_vector_type(4)));
#define LAS __attribute__((address_space(3)))
#define GAS __attribute__((address_space(1)))
typedef __amdgpu_buffer_rsrc_t rsrc_t;

constexpr unsigned WS_EPOCH = 0;
constexpr unsigned WS_FKV = 4096;
constexpr unsigned WS_FACT = WS_FKV + 8192;
constexpr unsigned WS_FWT = WS_FACT + 8192;
constexpr unsigned WS_KV = 65536;
constexpr unsigned WS_ACT = WS_KV + 32u * 8u * 16384u;
constexpr unsigned WS_WT = WS_ACT + 64u * 114688u;
constexpr unsigned WS_OF = WS_WT + 16u * 144u * 1024u;
constexpr unsigned WS_END = WS_OF + 64u * 32u * 1024u;
constexpr unsigned WS_DIAG = WS_END;

constexpr unsigned STG = 47104;
constexpr unsigned TILE = 2080;
constexpr unsigned L_BP = 64 * TILE;
constexpr unsigned L_Q = L_BP + 16384;
constexpr unsigned L_G = L_Q + 9216;
constexpr unsigned L_FL = L_G + 1024;
constexpr unsigned L_END = L_FL + 512;

constexpr unsigned SPIN_CAP = 1u << 18;
constexpr int AUX_SC1 = 16;

__device__ __forceinline__ unsigned pk_bf16(float a, float b) {
  f32x2 v = {a, b};
  return __builtin_bit_cast(unsigned, __builtin_convertvector(v, bf16x2));
}
__device__ __forceinline__ float bf_lo(unsigned u) { return __uint_as_float(u << 16); }
__device__ __forceinline__ float bf_hi(unsigned u) { return __uint_as_float(u & 0xffff0000u); }
__device__ __forceinline__ rsrc_t mk_rsrc(const void* p, unsigned bytes) {
  return __builtin_amdgcn_make_buffer_rsrc((void*)p, 0, bytes, 0x00020000);
}
#define BARRIER() asm volatile("s_waitcnt lgkmcnt(0)\n\ts_barrier" ::: "memory")
#define WAITV(n) asm volatile("s_waitcnt vmcnt(" #n ")" ::: "memory")
#define WAITL0() asm volatile("s_waitcnt lgkmcnt(0)" ::: "memory")
#define CFENCE() asm volatile("" ::: "memory")

#define STAMP(k) do {} while (0)

__device__ __forceinline__ bf16x8 lds_frag(LAS char* sm, unsigned off) { return *(LAS bf16x8*)(sm + off); }
__device__ __forceinline__ bf16x8 lds_tr2(LAS char* sm, unsigned o0, unsigned o1) {
  s16x4 a = __builtin_amdgcn_ds_read_tr16_b64_v4i16((LAS s16x4*)(sm + o0));
  s16x4 b = __builtin_amdgcn_ds_read_tr16_b64_v4i16((LAS s16x4*)(sm + o1));
  return __builtin_bit_cast(bf16x8, __builtin_shufflevector(a, b, 0, 1, 2, 3, 4, 5, 6, 7));
}
__device__ __forceinline__ unsigned lds_flag_ld(LAS char* sm, unsigned idx) {
  return __hip_atomic_load((LAS unsigned*)(sm + L_FL) + idx, __ATOMIC_RELAXED, __HIP_MEMORY_SCOPE_WORKGROUP);
}
__device__ __forceinline__ void lds_flag_st(LAS char* sm, unsigned idx) {
  __hip_atomic_store((LAS unsigned*)(sm + L_FL) + idx, 1u, __ATOMIC_RELAXED, __HIP_MEMORY_SCOPE_WORKGROUP);
}
__device__ __forceinline__ void lds_wait(LAS char* sm, unsigned idx0, unsigned n, int lane) {
#pragma unroll 1
  for (unsigned s = 0; s < SPIN_CAP * 4; ++s) {
    CFENCE();
    unsigned v = lds_flag_ld(sm, idx0 + ((unsigned)lane < n ? lane : 0));
    if (__all(v != 0)) break;
    __builtin_amdgcn_s_sleep(1);
  }
  CFENCE();
}
__device__ __forceinline__ void glb_wait(rsrc_t rs, unsigned off, u32x2 tag) {
#pragma unroll 1
  for (unsigned s = 0; s < SPIN_CAP; ++s) {
    CFENCE();
    u32x2 v = __builtin_amdgcn_raw_buffer_load_b64(rs, off, 0, AUX_SC1);
    if (__all(v.x == tag.x && v.y == tag.y)) break;
    __builtin_amdgcn_s_sleep(2);
  }
  CFENCE();
}

__global__ void __launch_bounds__(768, 3)
fused_kernel(const u16* __restrict__ x, const u16* __restrict__ wqkv, const u16* __restrict__ wo,
             const u16* __restrict__ w1, const u16* __restrict__ b1, const u16* __restrict__ w2,
             const u16* __restrict__ gw, u16* __restrict__ out, unsigned char* __restrict__ ws) {
  __shared__ __attribute__((aligned(16))) char smem_[L_END];
  LAS char* sm = (LAS char*)smem_;
  const int lane = threadIdx.x & 63;
  const int wave = __builtin_amdgcn_readfirstlane(threadIdx.x >> 6);
  const int b = blockIdx.x, xcd = b & 7, idx = b >> 3;
  const int bi = 4 * (xcd & 1) + (idx & 3);
  const int hd = 8 * (xcd >> 1) + (idx >> 2);
  const int hh = lane >> 5, l31 = lane & 31, l15 = lane & 15, g4 = lane >> 4;
  const int q4 = l15 >> 2, p4 = lane & 3;
  const rsrc_t rws = mk_rsrc(ws, WS_END);
  STAMP(0);

  if (wave >= 4 && wave < 8) {
    const int lw = wave - 4;
    const rsrc_t rx = mk_rsrc(x, 2048u * 1024u), rq = mk_rsrc(wqkv, 512u * 3072u), r1 = mk_rsrc(w1, 512u * 3584u);
    const int r8 = lane >> 3, slot = lane & 7;
    const unsigned xo0 = (unsigned)(256 * bi + 64 * lw + r8) * 1024u + (unsigned)(slot ^ (r8 >> 1)) * 16u;
    const unsigned xo1 = (unsigned)(256 * bi + 64 * lw + r8 + 8) * 1024u + (unsigned)(slot ^ (r8 >> 1) ^ 4) * 16u;
    const int pos = (lane >> 1) & 15, wrow = pos ^ ((pos >> 3) << 2);
    const unsigned qo = (unsigned)(16 * lw + wrow) * 3072u + (unsigned)hh * 1024u + (unsigned)hd * 32u + (lane & 1) * 16u;
    const unsigned vo = (unsigned)(16 * lw + wrow) * 3072u + 2048u + (unsigned)hd * 32u + (lane & 1) * 16u;
    const unsigned wo1 = (unsigned)(16 * lw + (lane >> 2)) * 3584u + (unsigned)hd * 112u + (unsigned)p4 * 16u;
    auto issue = [&](int s, unsigned buf) {
      LAS char* xb = sm + buf + lw * 8192;
#pragma unroll
      for (int n = 0; n < 4; ++n) {
        __builtin_amdgcn_raw_ptr_buffer_load_lds(rx, xb + n * 2048, 16, xo0, s * 128 + n * 16384, 0, 0);
        __builtin_amdgcn_raw_ptr_buffer_load_lds(rx, xb + n * 2048 + 1024, 16, xo1, s * 128 + n * 16384, 0, 0);
      }
      LAS char* wb = sm + buf + 32768 + lw * 3584;
      __builtin_amdgcn_raw_ptr_buffer_load_lds(rq, wb, 16, qo, s * (64 * 3072), 0, 0);
      if (lane < 32) __builtin_amdgcn_raw_ptr_buffer_load_lds(rq, wb + 1024, 16, vo, s * (64 * 3072), 0, 0);
      __builtin_amdgcn_raw_ptr_buffer_load_lds(r1, wb + 1536, 16, wo1, s * (64 * 3584), 0, 0);
      if (p4 != 3) __builtin_amdgcn_raw_ptr_buffer_load_lds(r1, wb + 2560, 16, wo1 + 64, s * (64 * 3584), 0, 0);
    };
    issue(0, 0);
    issue(1, STG);
    issue(2, 2 * STG);
    const int ct = b >> 4, kk9 = (b & 15) * 9;
    {
      const rsrc_t r2 = mk_rsrc(w2, 1792u * 1024u), ro = mk_rsrc(wo, 512u * 1024u);
      const unsigned so = (unsigned)(lane >> 2) * 1024u + (unsigned)ct * 64u + (unsigned)p4 * 16u;
      for (int f = lw; f < 9; f += 4) {
        const int ks = kk9 + f;
        if (ks < 112) __builtin_amdgcn_raw_ptr_buffer_load_lds(r2, sm + L_Q + f * 1024, 16, so, ks * 16384, 0, 0);
        else __builtin_amdgcn_raw_ptr_buffer_load_lds(ro, sm + L_Q + f * 1024, 16, so, (ks - 112) * 16384, 0, 0);
      }
    }
    WAITV(26); BARRIER();
    WAITV(14); BARRIER(); issue(3, 0);
    WAITV(14); BARRIER(); issue(4, STG);
    WAITV(12); BARRIER(); issue(5, 2 * STG);
    WAITV(12); BARRIER(); issue(6, 0);
    WAITV(12); BARRIER(); issue(7, STG);
    {
      const unsigned ta = L_Q + (unsigned)(8 * hh + q4) * 64u + (unsigned)(4 * (g4 & 1) + 2 * (p4 & 1) + (p4 >> 1)) * 8u;
      for (int f = lw; f < 9; f += 4) {
        u32x2 a, c;
        asm volatile("ds_read_b64_tr_b16 %0, %2\n\tds_read_b64_tr_b16 %1, %2 offset:256\n\ts_waitcnt lgkmcnt(0)"
                     : "=&v"(a), "=&v"(c) : "v"((unsigned)(size_t)sm + ta + f * 1024) : "memory");
        u32x4 v = {a.x, a.y, c.x, c.y};
        __builtin_amdgcn_raw_buffer_store_b128(v, rws, WS_WT + lane * 16, (ct * 144 + kk9 + f) * 1024, AUX_SC1);
      }
    }
    WAITV(14); BARRIER();
    WAITV(0); BARRIER();
  }

  auto mk_tag = [&](u32x2 e) {
    u64 z = ((u64)__builtin_amdgcn_readfirstlane(e.y) << 32 | (u64)__builtin_amdgcn_readfirstlane(e.x)) + 0x9E3779B97F4A7C15ull;
    z = (z ^ (z >> 30)) * 0xBF58476D1CE4E5B9ull;
    z = (z ^ (z >> 27)) * 0x94D049BB133111EBull;
    z ^= z >> 31;
    return (u32x2){(unsigned)z, (unsigned)(z >> 32)};
  };

  if (wave < 4) {
    const int j = wave;
    const u32x2 ep = __builtin_amdgcn_raw_buffer_load_b64(rws, WS_EPOCH, 0, AUX_SC1);
    if (wave == 0) ((LAS unsigned*)(sm + L_FL))[lane] = 0u;
    f32x4 acc[4][3];
#pragma unroll
    for (int t = 0; t < 4; ++t)
#pragma unroll
      for (int c = 0; c < 3; ++c) acc[t][c] = (f32x4){0.f, 0.f, 0.f, 0.f};
    const unsigned xa0 = (unsigned)(64 * j + l15) * 128u + (unsigned)(g4 ^ (l15 >> 1)) * 16u;
    const unsigned xa1 = xa0 ^ 64u;
    const unsigned wa0 = 32768u + (unsigned)(g4 >> 1) * 3584u + (unsigned)(8 * (g4 & 1) + 4 * (g4 & 1) + q4) * 32u + p4 * 8u;
    const unsigned wa1 = 32768u + (unsigned)(g4 >> 1) * 3584u + (unsigned)(8 * (g4 & 1) + (4 ^ (4 * (g4 & 1))) + q4) * 32u + p4 * 8u;
    unsigned buf = 0;
    WAITV(0);
    const u32x2 tag = mk_tag(ep);
#pragma unroll 1
    for (int s = 0; s < 8; ++s) {
      BARRIER();
#pragma unroll
      for (int kk = 0; kk < 2; ++kk) {
        bf16x8 xf[4], wf[3];
#pragma unroll
        for (int c = 0; c < 3; ++c) wf[c] = lds_tr2(sm, buf + wa0 + kk * 7168 + c * 512, buf + wa1 + kk * 7168 + c * 512);
#pragma unroll
        for (int t = 0; t < 4; ++t) xf[t] = lds_frag(sm, buf + (kk ? xa1 : xa0) + t * 2048);
#pragma unroll
        for (int t = 0; t < 4; ++t) {
          acc[t][0] = __builtin_amdgcn_mfma_f32_16x16x32_bf16(wf[0], xf[t], acc[t][0], 0, 0, 0);
          acc[t][1] = __builtin_amdgcn_mfma_f32_16x16x32_bf16(wf[1], xf[t], acc[t][1], 0, 0, 0);
          acc[t][2] = __builtin_amdgcn_mfma_f32_16x16x32_bf16(xf[t], wf[2], acc[t][2], 0, 0, 0);
        }
      }
      buf = (buf == 2 * STG) ? 0 : buf + STG;
    }
    STAMP(1);
    const float qs = 0.25f * 1.4426950408889634f;
    const int rho = (l15 & 3) + 8 * ((l15 >> 2) & 1) + 4 * (l15 >> 3);
#pragma unroll
    for (int t = 0; t < 4; ++t) {
      const unsigned tl = 2 * j + (t >> 1);
      const unsigned o = (unsigned)(16 * (t & 1) + l15 + 32 * (g4 >> 1)) * 16u + (g4 & 1) * 8u;
      *(LAS u32x2*)(sm + L_Q + tl * 1024 + o) = (u32x2){pk_bf16(acc[t][0][0] * qs, acc[t][0][1] * qs), pk_bf16(acc[t][0][2] * qs, acc[t][0][3] * qs)};
      *(LAS u32x2*)(sm + tl * TILE + o) = (u32x2){pk_bf16(acc[t][1][0], acc[t][1][1]), pk_bf16(acc[t][1][2], acc[t][1][3])};
      *(LAS u32x2*)(sm + tl * TILE + 1024 + (t & 1) * 528 + (unsigned)((g4 >> 1) * 16 + rho) * 16u + (g4 & 1) * 8u) =
          (u32x2){pk_bf16(acc[t][2][0], acc[t][2][1]), pk_bf16(acc[t][2][2], acc[t][2][3])};
    }
    if (lane < 4) *(LAS u32x4*)(sm + (2 * j + (lane >> 1)) * TILE + 1024 + (lane & 1) * 528 + 512) = (u32x4){0x3f803f80u, 0x3f803f80u, 0x3f803f80u, 0x3f803f80u};
    WAITL0();
    {
      const unsigned go = WS_KV + (unsigned)(hd * 8 + bi) * 16384u + (unsigned)j * 4096u + lane * 16u;
#pragma unroll
      for (int t = 0; t < 2; ++t) {
        u32x4 kf = *(LAS u32x4*)(sm + (2 * j + t) * TILE + lane * 16);
        u32x4 vf = *(LAS u32x4*)(sm + (2 * j + t) * TILE + 1024 + lane * 16 + hh * 16);
        __builtin_amdgcn_raw_buffer_store_b128(kf, rws, go + t * 2048, 0, AUX_SC1);
        __builtin_amdgcn_raw_buffer_store_b128(vf, rws, go + t * 2048 + 1024, 0, AUX_SC1);
      }
    }
    BARRIER();
    if (wave == 0 && lane == 0) __builtin_amdgcn_raw_buffer_store_b64(tag, rws, WS_FWT + (unsigned)b * 8u, 0, AUX_SC1);
    WAITV(0);
    if (lane == 0) __builtin_amdgcn_raw_buffer_store_b64(tag, rws, WS_FKV + (unsigned)((hd * 8 + bi) * 4 + j) * 8u, 0, AUX_SC1);
  } else if (wave < 8) {
    BARRIER();
  } else {
    const int j = wave - 8;
    const u32x2 ep = __builtin_amdgcn_raw_buffer_load_b64(rws, WS_EPOCH, 0, AUX_SC1);
    {
      const u32x4 inv = {0xffffffffu, 0xffffffffu, 0xffffffffu, 0xffffffffu};
      const unsigned oo = WS_OF + (unsigned)((8 * bi + 2 * j) * 32 + hd) * 1024u + lane * 16u;
      __builtin_amdgcn_raw_buffer_store_b128(inv, rws, oo, 0, AUX_SC1);
      __builtin_amdgcn_raw_buffer_store_b128(inv, rws, oo + 32768, 0, AUX_SC1);
    }
    f32x16 acc[2][2];
    unsigned gA, gB;
    {
      const rsrc_t rb = mk_rsrc(b1, 3584u), rg = mk_rsrc(gw, 1024u);
      gA = __builtin_amdgcn_raw_buffer_load_b16(rg, (unsigned)(64 * g4 + 16 * j + l15) * 2u, 0, 0);
      gB = __builtin_amdgcn_raw_buffer_load_b16(rg, (unsigned)(64 * g4 + 16 * j + l15) * 2u + 512u, 0, 0);
#pragma unroll
      for (int c = 0; c < 2; ++c)
#pragma unroll
        for (int rh = 0; rh < 2; ++rh) {
          u32x4 bv = __builtin_amdgcn_raw_buffer_load_b128(rb, (unsigned)hd * 112u + (unsigned)(4 * c + 2 * rh + hh) * 16u, 0, 0);
          if (c == 1 && rh == 1 && hh) bv = (u32x4){0u, 0u, 0u, 0u};
#pragma unroll
          for (int e = 0; e < 4; ++e) {
            acc[0][c][8 * rh + 2 * e] = bf_lo(bv[e]); acc[0][c][8 * rh + 2 * e + 1] = bf_hi(bv[e]);
            acc[1][c][8 * rh + 2 * e] = bf_lo(bv[e]); acc[1][c][8 * rh + 2 * e + 1] = bf_hi(bv[e]);
          }
        }
    }
    const unsigned gwa = 32768u + (unsigned)j * 3584u + 2560u + (unsigned)l15 * 64u + 48u;
    if (g4 < 3) *(LAS u16*)(sm + g4 * STG + gwa) = (u16)gA;
    WAITV(0);
    const u32x2 tag = mk_tag(ep);
    const unsigned xa = (unsigned)(64 * j + l31) * 128u + (unsigned)(hh ^ ((l31 >> 1) & 7)) * 16u;
    const unsigned wa = 32768u + 1536u + (unsigned)(8 * hh + q4) * 64u + (unsigned)(4 * (g4 & 1) + 2 * (p4 & 1) + (p4 >> 1)) * 8u;
    unsigned buf = 0, pbuf = 2 * STG;
#pragma unroll 1
    for (int s = 0; s < 8; ++s) {
      BARRIER();
      if (s >= 1 && s < 6 && g4 == ((s + 2) & 3)) *(LAS u16*)(sm + pbuf + gwa) = (u16)(s >= 2 ? gB : gA);
#pragma unroll
      for (int ks = 0; ks < 4; ++ks) {
        bf16x8 xf[2], wf[2];
#pragma unroll
        for (int c = 0; c < 2; ++c) wf[c] = lds_tr2(sm, buf + wa + ks * 3584 + c * 1024, buf + wa + ks * 3584 + c * 1024 + 256);
#pragma unroll
        for (int t = 0; t < 2; ++t) xf[t] = lds_frag(sm, buf + (xa ^ (unsigned)(ks * 32)) + t * 4096);
#pragma unroll
        for (int t = 0; t < 2; ++t)
#pragma unroll
          for (int c = 0; c < 2; ++c) acc[t][c] = __builtin_amdgcn_mfma_f32_32x32x16_bf16(wf[c], xf[t], acc[t][c], 0, 0, 0);
      }
      pbuf = buf;
      buf = (buf == 2 * STG) ? 0 : buf + STG;
    }
    STAMP(1);
    if (hh) {
#pragma unroll
      for (int t = 0; t < 2; ++t) {
        const float e = __builtin_amdgcn_exp2f(acc[t][1][8] * -1.4426950408889634f);
        *(LAS float*)(sm + L_G + (64 * j + 32 * t + l31) * 4) = __builtin_amdgcn_rcpf(1.f + e);
      }
    }
    BARRIER();
    __builtin_amdgcn_s_setprio(2);
    {
      const unsigned ao = WS_ACT + (unsigned)(8 * bi + 2 * j) * 114688u + (unsigned)hd * 3584u + lane * 16u;
#pragma unroll
      for (int t = 0; t < 2; ++t)
#pragma unroll
        for (int c = 0; c < 2; ++c)
#pragma unroll
          for (int rh = 0; rh < 2; ++rh) {
            u32x4 o;
#pragma unroll
            for (int e = 0; e < 4; ++e) {
              f32x2 hv = {acc[t][c][8 * rh + 2 * e], acc[t][c][8 * rh + 2 * e + 1]};
              f32x2 a = hv * 1.4426950408889634f;
              f32x2 ex = {__builtin_amdgcn_exp2f(a.x), __builtin_amdgcn_exp2f(a.y)};
              f32x2 d = ex * (ex + 2.f) + 2.f;
              f32x2 r = {__builtin_amdgcn_rcpf(d.x), __builtin_amdgcn_rcpf(d.y)};
              f32x2 y = hv * (r * -2.f + 1.f);
              o[e] = pk_bf16(y.x, y.y);
            }
            if (!(c == 1 && rh == 1) || !hh)
              __builtin_amdgcn_raw_buffer_store_b128(o, rws, ao + t * 114688 + (4 * c + 2 * rh) * 512, 0, AUX_SC1);
          }
    }
    STAMP(2);
    {
      const unsigned lo = (unsigned)(2 * j) * TILE + lane * 16u;
      const u32x4 ones = {0x3f803f80u, 0x3f803f80u, 0x3f803f80u, 0x3f803f80u};
      if (lane < 28) *(LAS u32x4*)(sm + (8 * (1 + (lane >> 2)) + 2 * j + ((lane >> 1) & 1)) * TILE + 1024 + (lane & 1) * 528 + 512) = ones;
      bool first = true;
      for (int n0 = 1; n0 < 8;) {
        const int cnt = first ? 1 : 3;
        const int nl = n0 + (lane < cnt ? lane : 0);
        glb_wait(rws, WS_FKV + (unsigned)((hd * 8 + ((bi + nl) & 7)) * 4 + j) * 8u, tag);
        if (first) {
          WAITV(0);
          if (lane == 0) __builtin_amdgcn_raw_buffer_store_b64(tag, rws, WS_FACT + (unsigned)((bi * 4 + j) * 32 + hd) * 8u, 0, AUX_SC1);
        }
        u32x4 d[3][4];
#pragma unroll
        for (int u = 0; u < 3; ++u)
          if (u < cnt) {
            const unsigned go = WS_KV + (unsigned)(hd * 8 + ((bi + n0 + u) & 7)) * 16384u + (unsigned)j * 4096u;
#pragma unroll
            for (int e = 0; e < 4; ++e) d[u][e] = __builtin_amdgcn_raw_buffer_load_b128(rws, lane * 16 + e * 1024, go, AUX_SC1);
          }
#pragma unroll
        for (int u = 0; u < 3; ++u)
          if (u < cnt) {
            LAS char* dst = sm + (n0 + u) * (8 * TILE) + lo;
            *(LAS u32x4*)(dst) = d[u][0];
            *(LAS u32x4*)(dst + 1024 + hh * 16) = d[u][1];
            *(LAS u32x4*)(dst + TILE) = d[u][2];
            *(LAS u32x4*)(dst + TILE + 1024 + hh * 16) = d[u][3];
          }
        WAITL0();
        if (lane < cnt) lds_flag_st(sm, (n0 + lane) * 4 + j);
        n0 += cnt;
        first = false;
      }
    }
    STAMP(3);
    __builtin_amdgcn_s_setprio(0);
    const int tm = hd >> 3, cp = hd & 7;
    glb_wait(rws, hh ? WS_FWT + (unsigned)(32 * cp + l31) * 8u : WS_FACT + (unsigned)((bi * 4 + tm) * 32 + 8 * j + (lane & 7)) * 8u, tag);
    STAMP(4);
    {
#pragma unroll
      for (int t = 0; t < 2; ++t)
#pragma unroll
        for (int c = 0; c < 2; ++c)
#pragma unroll
          for (int r = 0; r < 16; ++r) acc[t][c][r] = 0.f;
      const unsigned ab = WS_ACT + (unsigned)(8 * bi + 2 * tm) * 114688u + (unsigned)(28 * j) * 1024u;
      const unsigned wb = WS_WT + (unsigned)(2 * cp * 144 + 28 * j) * 1024u;
      u32x4 af[4][2], wf[4][2];
#pragma unroll
      for (int u = 0; u < 4; ++u) {
        af[u][0] = __builtin_amdgcn_raw_buffer_load_b128(rws, lane * 16 + u * 1024, ab, AUX_SC1);
        af[u][1] = __builtin_amdgcn_raw_buffer_load_b128(rws, lane * 16 + u * 1024, ab + 114688, AUX_SC1);
        wf[u][0] = __builtin_amdgcn_raw_buffer_load_b128(rws, lane * 16 + u * 1024, wb, AUX_SC1);
        wf[u][1] = __builtin_amdgcn_raw_buffer_load_b128(rws, lane * 16 + u * 1024, wb + 147456, AUX_SC1);
      }
#pragma unroll 1
      for (int it = 1; it <= 7; ++it) {
        const unsigned nx = (it < 7 ? it : 6) * 4096;
#pragma unroll
        for (int u = 0; u < 4; ++u) {
#pragma unroll
          for (int t = 0; t < 2; ++t)
#pragma unroll
            for (int c = 0; c < 2; ++c)
              acc[t][c] = __builtin_amdgcn_mfma_f32_32x32x16_bf16(__builtin_bit_cast(bf16x8, wf[u][c]), __builtin_bit_cast(bf16x8, af[u][t]), acc[t][c], 0, 0, 0);
          af[u][0] = __builtin_amdgcn_raw_buffer_load_b128(rws, lane * 16 + u * 1024, ab + nx, AUX_SC1);
          af[u][1] = __builtin_amdgcn_raw_buffer_load_b128(rws, lane * 16 + u * 1024, ab + nx + 114688, AUX_SC1);
          wf[u][0] = __builtin_amdgcn_raw_buffer_load_b128(rws, lane * 16 + u * 1024, wb + nx, AUX_SC1);
          wf[u][1] = __builtin_amdgcn_raw_buffer_load_b128(rws, lane * 16 + u * 1024, wb + nx + 147456, AUX_SC1);
          __builtin_amdgcn_sched_barrier(0);
        }
      }
      if (j > 0) lds_wait(sm, 32 + j - 1, 1, lane);
#pragma unroll
      for (int t = 0; t < 2; ++t) {
        const float om = 1.f - *(LAS float*)(sm + L_G + (64 * tm + 32 * t + l31) * 4);
#pragma unroll
        for (int c = 0; c < 2; ++c)
#pragma unroll
          for (int rq = 0; rq < 4; ++rq) {
            LAS f32x4* p = (LAS f32x4*)(sm + L_BP + ((t + 2 * c) * 4 + rq) * 1024 + lane * 16);
            f32x4 v = {acc[t][c][4 * rq] * om, acc[t][c][4 * rq + 1] * om, acc[t][c][4 * rq + 2] * om, acc[t][c][4 * rq + 3] * om};
            if (j > 0) v += *p;
            *p = v;
          }
      }
      WAITL0();
      if (lane == 0) lds_flag_st(sm, 32 + j);
    }
    STAMP(5);
    if (b == 0 && j == 0) {
#pragma unroll
      for (int u = 0; u < 4; ++u) glb_wait(rws, WS_FWT + (unsigned)(64 * u + lane) * 8u, tag);
      if (lane == 0) __builtin_amdgcn_raw_buffer_store_b64(tag, rws, WS_EPOCH, 0, AUX_SC1);
    }
    return;
  }

  STAMP(2);
  const int w = wave;
  const bf16x8 qf = lds_frag(sm, L_Q + w * 1024 + lane * 16);
  const unsigned ka = lane * 16;
  const unsigned va = 1024u + (l31 < 16 ? (unsigned)(hh * 16 + l31) * 16u : ((l31 >= 20 && l31 < 28) ? (unsigned)hh * 256u : 512u));
  const int IDX = 0xDDDD;
  f32x16 negm, O;
  {
    f32x16 z;
#pragma unroll
    for (int r = 0; r < 16; ++r) { z[r] = 0.f; O[r] = 0.f; }
    f32x16 s0 = __builtin_amdgcn_mfma_f32_32x32x16_bf16(lds_frag(sm, ka), qf, z, 0, 0, 0);
    float m = s0[0];
#pragma unroll
    for (int r = 1; r < 16; ++r) m = fmaxf(m, s0[r]);
    m = fmaxf(m, __shfl_xor(m, 32));
#pragma unroll
    for (int r = 0; r < 16; ++r) negm[r] = -m;
  }
  {
    f32x16 S = __builtin_amdgcn_mfma_f32_32x32x16_bf16(lds_frag(sm, ka), qf, negm, 0, 0, 0);
    bf16x8 kf = lds_frag(sm, ka + TILE);
    bf16x8 v0 = lds_frag(sm, va), v1 = lds_frag(sm, va + 528);
    unsigned kb = ka, vb = va;
#pragma unroll 1
    for (int n = 0; n < 8; ++n) {
#pragma unroll
      for (int t = 0; t < 8; ++t) {
        if (t == 6 && n < 7) lds_wait(sm, (n + 1) * 4, 4, lane);
        f32x16 Sn = __builtin_amdgcn_mfma_f32_32x32x16_bf16(kf, qf, negm, 0, 0, 0);
        kf = lds_frag(sm, kb + (t + 2) * TILE);
        f32x8 p0, p1;
#pragma unroll
        for (int r = 0; r < 8; ++r) p0[r] = __builtin_amdgcn_exp2f(S[r]);
        O = __builtin_amdgcn_smfmac_f32_32x32x32_bf16(v0, __builtin_bit_cast(bf16x16, p0), O, IDX, 0, 0);
        v0 = lds_frag(sm, vb + (t + 1) * TILE);
#pragma unroll
        for (int r = 0; r < 8; ++r) p1[r] = __builtin_amdgcn_exp2f(S[8 + r]);
        O = __builtin_amdgcn_smfmac_f32_32x32x32_bf16(v1, __builtin_bit_cast(bf16x16, p1), O, IDX, 0, 0);
        v1 = lds_frag(sm, vb + (t + 1) * TILE + 528);
        S = Sn;
      }
      kb += 8 * TILE; vb += 8 * TILE;
    }
  }
  STAMP(3);
  float lsum = hh ? O[12] : O[8];
  {
    float chk = lsum * 0.f;
#pragma unroll
    for (int r = 0; r < 8; ++r) chk = __builtin_fmaf(O[r], 0.f, chk);
    if (__any(chk != chk)) {
      float m = -3.0e38f;
      f32x16 z;
#pragma unroll
      for (int r = 0; r < 16; ++r) { z[r] = 0.f; O[r] = 0.f; }
#pragma unroll 1
      for (int t = 0; t < 64; ++t) {
        f32x16 S = __builtin_amdgcn_mfma_f32_32x32x16_bf16(lds_frag(sm, ka + t * TILE), qf, z, 0, 0, 0);
        float mx = m;
#pragma unroll
        for (int r = 0; r < 16; ++r) mx = fmaxf(mx, S[r]);
        mx = fmaxf(mx, __shfl_xor(mx, 32));
        const float al = __builtin_amdgcn_exp2f(m - mx);
        m = mx;
        f32x8 p0, p1;
#pragma unroll
        for (int r = 0; r < 16; ++r) O[r] *= al;
#pragma unroll
        for (int r = 0; r < 8; ++r) { p0[r] = __builtin_amdgcn_exp2f(S[r] - m); p1[r] = __builtin_amdgcn_exp2f(S[8 + r] - m); }
        O = __builtin_amdgcn_smfmac_f32_32x32x32_bf16(lds_frag(sm, va + t * TILE), __builtin_bit_cast(bf16x16, p0), O, IDX, 0, 0);
        O = __builtin_amdgcn_smfmac_f32_32x32x32_bf16(lds_frag(sm, va + t * TILE + 528), __builtin_bit_cast(bf16x16, p1), O, IDX, 0, 0);
      }
      lsum = hh ? O[12] : O[8];
    }
  }
  {
    const float sc = *(LAS float*)(sm + L_G + (32 * w + l31) * 4) * __builtin_amdgcn_rcpf(lsum);
    u32x4 o;
#pragma unroll
    for (int e = 0; e < 4; ++e) {
      unsigned u = pk_bf16(O[2 * e] * sc, O[2 * e + 1] * sc);
      o[e] = (u == 0xffffffffu) ? 0x7fc07fc0u : u;
    }
    __builtin_amdgcn_raw_buffer_store_b128(o, rws, WS_OF + (unsigned)((8 * bi + w) * 32 + hd) * 1024u + lane * 16u, 0, AUX_SC1);
  }
  {
    const int tm = hd >> 3, cp = hd & 7;
    const int tt = w & 1, tn = (w >> 1) & 1, kh = w >> 2;
    lds_wait(sm, 35, 1, lane);
    STAMP(4);
    u32x4 wf[16], of[16];
    const unsigned wb = WS_WT + (unsigned)((2 * cp + tn) * 144 + 112 + 16 * kh) * 1024u;
#pragma unroll
    for (int u = 0; u < 16; ++u) wf[u] = __builtin_amdgcn_raw_buffer_load_b128(rws, lane * 16 + (u & 3) * 1024, wb + (u >> 2) * 4096, AUX_SC1);
    f32x16 acc;
#pragma unroll
    for (int r = 0; r < 16; ++r) acc[r] = 0.f;
    const unsigned ob = WS_OF + (unsigned)((8 * bi + 2 * tm + tt) * 32 + 16 * kh) * 1024u;
#pragma unroll 1
    for (unsigned s = 0; s < SPIN_CAP; ++s) {
      CFENCE();
      unsigned bad = 0;
#pragma unroll
      for (int u = 0; u < 16; ++u) of[u] = __builtin_amdgcn_raw_buffer_load_b128(rws, lane * 16 + (u & 3) * 1024, ob + (u >> 2) * 4096, AUX_SC1);
#pragma unroll
      for (int u = 0; u < 16; ++u)
#pragma unroll
        for (int e = 0; e < 4; ++e) bad = max(bad, of[u][e]);
      if (__all(bad != 0xffffffffu)) break;
      __builtin_amdgcn_s_sleep(1);
    }
    STAMP(5);
#pragma unroll
    for (int u = 0; u < 16; ++u)
      acc = __builtin_amdgcn_mfma_f32_32x32x16_bf16(__builtin_bit_cast(bf16x8, wf[u]), __builtin_bit_cast(bf16x8, of[u]), acc, 0, 0, 0);
    LAS f32x4* xo = (LAS f32x4*)(sm + L_Q + w * 1024 + lane * 16);
    LAS f32x4* xo2 = (LAS f32x4*)(sm + L_BP + ((tt + 2 * tn) * 4 + 2 * (kh ^ 1)) * 1024 + lane * 16);
    (void)xo;
    {
      f32x4 a = xo2[0], c = xo2[64];
      f32x4 pa = kh ? (f32x4){acc[0], acc[1], acc[2], acc[3]} : (f32x4){acc[8], acc[9], acc[10], acc[11]};
      f32x4 pc = kh ? (f32x4){acc[4], acc[5], acc[6], acc[7]} : (f32x4){acc[12], acc[13], acc[14], acc[15]};
      xo2[0] = a + pa; xo2[64] = c + pc;
    }
    WAITL0();
    if (lane == 0) lds_flag_st(sm, 40 + w);
    lds_wait(sm, 40 + (w ^ 4), 1, lane);
    {
      LAS f32x4* mine = (LAS f32x4*)(sm + L_BP + ((tt + 2 * tn) * 4 + 2 * kh) * 1024 + lane * 16);
      f32x4 a = mine[0], c = mine[64];
      f32x4 pa = kh ? (f32x4){acc[8], acc[9], acc[10], acc[11]} : (f32x4){acc[0], acc[1], acc[2], acc[3]};
      f32x4 pc = kh ? (f32x4){acc[12], acc[13], acc[14], acc[15]} : (f32x4){acc[4], acc[5], acc[6], acc[7]};
      a += pa; c += pc;
      u32x4 o = {pk_bf16(a[0], a[1]), pk_bf16(a[2], a[3]), pk_bf16(c[0], c[1]), pk_bf16(c[2], c[3])};
      const rsrc_t ro = mk_rsrc(out, 2048u * 1024u);
      __builtin_amdgcn_raw_buffer_store_b128(o, ro, (unsigned)(256 * bi + 64 * tm + 32 * tt + l31) * 1024u + (unsigned)(64 * cp + 32 * tn + 16 * kh + 8 * hh) * 2u, 0, 0);
    }
  }
  STAMP(6);
}

__global__ void tiny(unsigned char* ws){ if(threadIdx.x==0) *(GAS u64*)((GAS char*)ws + WS_DIAG) = 1; }
extern "C" void kernel_launch(void* const* d_in, const int* in_sizes, int n_in, void* d_out, int out_size,
                              void* d_ws, size_t ws_size, hipStream_t stream) {
  tiny<<<1,64,0,stream>>>((unsigned char*)d_ws);
  fused_kernel<<<256, 768, 0, stream>>>((const u16*)d_in[0], (const u16*)d_in[1], (const u16*)d_in[2], (const u16*)d_in[4],
                                        (const u16*)d_in[5], (const u16*)d_in[6], (const u16*)d_in[7], (u16*)d_out,
                                        (unsigned char*)d_ws);
}
```

```cpp
#include <hip/hip_runtime.h>
#include <hip/hip_bf16.h>

typedef unsigned short u16;
typedef unsigned long long u64;
typedef __bf16 bf16x8 __attribute__((ext_vector_type(8)));
typedef __bf16 bf16x16 __attribute__((ext_vector_type(16)));
typedef __bf16 bf16x2 __attribute__((ext_vector_type(2)));
typedef short s16x4 __attribute__((ext_vector_type(4)));
typedef float f32x2 __attribute__((ext_vector_type(2)));
typedef float f32x4 __attribute__((ext_vector_type(4)));
typedef float f32x8 __attribute__((ext_vector_type(8)));
typedef float f32x16 __attribute__((ext_vector_type(16)));
typedef unsigned u32x2 __attribute__((ext_vector_type(2)));
typedef unsigned u32x4 __attribute__((ext_vector_type(4)));
#define LAS __attribute__((address_space(3)))
#define GAS __attribute__((address_space(1)))
typedef __amdgpu_buffer_rsrc_t rsrc_t;

constexpr unsigned WS_EPOCH = 0;
constexpr unsigned WS_FKV = 4096;
constexpr unsigned WS_FACT = WS_FKV + 8192;
constexpr unsigned WS_G = 32768;
constexpr unsigned WS_KV = 65536;
constexpr unsigned WS_ACT = WS_KV + 32u * 131072u;
constexpr unsigned WS_WT = WS_ACT + 64u * 114688u;
constexpr unsigned WS_OF = WS_WT + 16u * 144u * 1024u;
constexpr unsigned WS_END = WS_OF + 64u * 32u * 1024u;
constexpr unsigned WS_DIAG = WS_END;

constexpr unsigned STG = 47104;
constexpr unsigned TILE = 2080;
constexpr unsigned L_RT = 3 * STG;
constexpr unsigned L_BP = 64 * TILE;
constexpr unsigned L_Q = L_BP + 16384;
constexpr unsigned L_B1 = L_Q + 8192;
constexpr unsigned L_G = 158720;
constexpr unsigned L_FL = L_G + 1024;
constexpr unsigned L_END = L_FL + 512;

constexpr unsigned SPIN_CAP = 1u << 15;
constexpr int AUX_SC1 = 16;
#define POLL_SLEEP 4

__device__ __forceinline__ unsigned pk_bf16(float a, float b) {
  f32x2 v = {a, b};
  return __builtin_bit_cast(unsigned, __builtin_convertvector(v, bf16x2));
}
__device__ __forceinline__ float bf_lo(unsigned u) { return __uint_as_float(u << 16); }
__device__ __forceinline__ float bf_hi(unsigned u) { return __uint_as_float(u & 0xffff0000u); }
__device__ __forceinline__ rsrc_t mk_rsrc(const void* p, unsigned bytes) {
  return __builtin_amdgcn_make_buffer_rsrc((void*)p, 0, bytes, 0x00020000);
}
__device__ __forceinline__ u32x4 mk_desc(const void* p, unsigned bytes) {
  const u64 a = (u64)p;
  return (u32x4){(unsigned)a, (unsigned)(a >> 32) & 0xffffu, bytes, 0x00020000u};
}
__device__ __forceinline__ void dma16(u32x4 desc, unsigned lds_addr, unsigned voff, unsigned soff) {
  asm volatile("s_mov_b32 m0, %0\n\ts_nop 3\n\tbuffer_load_dwordx4 %1, %2, %3 offen lds"
               :: "s"(lds_addr), "v"(voff), "s"(desc), "s"(soff) : "memory");
}
template <int OFF>
__device__ __forceinline__ void dma16_next(u32x4 desc, unsigned voff, unsigned soff) {
  asm volatile("buffer_load_dwordx4 %0, %1, %2 offen offset:%3 lds" :: "v"(voff), "s"(desc), "s"(soff), "i"(OFF) : "memory");
}
#define BARRIER() asm volatile("s_waitcnt lgkmcnt(0)\n\ts_barrier" ::: "memory")
#define WAITV(n) asm volatile("s_waitcnt vmcnt(" #n ")" ::: "memory")
#define WAITL0() asm volatile("s_waitcnt lgkmcnt(0)" ::: "memory")
#define CFENCE() asm volatile("" ::: "memory")
#define LD128(off, soff) __builtin_amdgcn_raw_buffer_load_b128(rws, off, soff, AUX_SC1)
#define ST128(v, off, soff) __builtin_amdgcn_raw_buffer_store_b128(v, rws, off, soff, AUX_SC1)

#define STAMP(k) do {} while (0)
#define STAMP_OUT() do {} while (0)
#define ACC_BEG() do {} while (0)
#define ACC_END(a) do {} while (0)

#define STAMP2(k) do {} while (0)
__device__ __forceinline__ bf16x8 lds_frag(LAS char* sm, unsigned off) { return *(LAS bf16x8*)(sm + off); }
__device__ __forceinline__ bf16x8 lds_tr2(LAS char* sm, unsigned o0, unsigned o1) {
  s16x4 a = __builtin_amdgcn_ds_read_tr16_b64_v4i16((LAS s16x4*)(sm + o0));
  s16x4 b = __builtin_amdgcn_ds_read_tr16_b64_v4i16((LAS s16x4*)(sm + o1));
  return __builtin_bit_cast(bf16x8, __builtin_shufflevector(a, b, 0, 1, 2, 3, 4, 5, 6, 7));
}
__device__ __forceinline__ void lds_add(LAS char* sm, unsigned idx, unsigned v) {
  __hip_atomic_fetch_add((LAS unsigned*)(sm + L_FL) + idx, v, __ATOMIC_RELAXED, __HIP_MEMORY_SCOPE_WORKGROUP);
}
__device__ __forceinline__ void lds_wait(LAS char* sm, unsigned idx, unsigned want) {
#pragma unroll 1
  for (unsigned s = 0; s < SPIN_CAP * 4; ++s) {
    CFENCE();
    unsigned v = __hip_atomic_load((LAS unsigned*)(sm + L_FL) + idx, __ATOMIC_RELAXED, __HIP_MEMORY_SCOPE_WORKGROUP);
    if ((unsigned)__builtin_amdgcn_readfirstlane(v) >= want) break;
    __builtin_amdgcn_s_sleep(1);
  }
  CFENCE();
}
__device__ __forceinline__ void glb_wait(rsrc_t rs, unsigned off, u32x2 tag) {
#pragma unroll 1
  for (unsigned s = 0; s < SPIN_CAP; ++s) {
    CFENCE();
    u32x2 v = __builtin_amdgcn_raw_buffer_load_b64(rs, off, 0, AUX_SC1);
    if (__all(v.x == tag.x && v.y == tag.y)) break;
    __builtin_amdgcn_s_sleep(POLL_SLEEP);
  }
  CFENCE();
}

#define AE(b, i) "v_exp_f32 v[" #b "+" #i "], v[" #b "+" #i "]\n"
#define ATILE(cur, oth, t)                                                                                          \
  AE(cur, 0) AE(cur, 1)                                                                                              \
  "s_waitcnt lgkmcnt(2)\n v_smfmac_f32_32x32x32_bf16 %0, %3, v[" #oth "+8:" #oth "+15], %13\n"                       \
  "ds_read_b128 %3, %5 offset:2080*" #t "+528\n"                                                                     \
  AE(cur, 2) AE(cur, 3) AE(cur, 4) AE(cur, 5)                                                                        \
  "s_waitcnt lgkmcnt(2)\n v_mfma_f32_32x32x16_bf16 v[" #oth ":" #oth "+15], %1, %11, %12\n"                          \
  "ds_read_b128 %1, %4 offset:2080*(" #t "+2)\n"                                                                     \
  AE(cur, 6) AE(cur, 7) AE(cur, 8) AE(cur, 9) AE(cur, 10)                                                            \
  "s_waitcnt lgkmcnt(2)\n v_smfmac_f32_32x32x32_bf16 %0, %2, v[" #cur ":" #cur "+7], %13\n"                          \
  "ds_read_b128 %2, %5 offset:2080*(" #t "+1)\n"                                                                     \
  AE(cur, 11) AE(cur, 12) AE(cur, 13) AE(cur, 14) AE(cur, 15)
#define ATTN_LOOP                                                                                                   \
  "ds_read_b128 %1, %4\n ds_read_b128 %3, %5\n"                                                                      \
  "v_mov_b32 v120, 0\n v_mov_b32 v121, 0\n v_mov_b32 v122, 0\n v_mov_b32 v123, 0\n"                                  \
  "v_mov_b32 v124, 0\n v_mov_b32 v125, 0\n v_mov_b32 v126, 0\n v_mov_b32 v127, 0\n"                                  \
  "s_waitcnt lgkmcnt(0)\n v_mfma_f32_32x32x16_bf16 v[96:111], %1, %11, %12\n"                                        \
  "ds_read_b128 %1, %4 offset:2080\n ds_read_b128 %2, %5\n s_nop 11\n"                                               \
  "1:\n"                                                                                                             \
  ATILE(96, 112, 0) ATILE(112, 96, 1) ATILE(96, 112, 2) ATILE(112, 96, 3) ATILE(96, 112, 4)                          \
  "ds_read_b32 %8, %7\n"                                                                                             \
  ATILE(112, 96, 5)                                                                                                  \
  "v_readfirstlane_b32 %9, %8\n s_nop 3\n s_cmp_eq_u32 %9, 8\n s_cbranch_scc1 3f\n s_mov_b32 %10, 0x20000\n"         \
  "2:\n s_sleep 1\n ds_read_b32 %8, %7\n s_waitcnt lgkmcnt(0)\n v_readfirstlane_b32 %9, %8\n s_sub_u32 %10, %10, 1\n" \
  "s_cmp_eq_u32 %10, 0\n s_cbranch_scc1 3f\n s_cmp_eq_u32 %9, 8\n s_cbranch_scc0 2b\n"                               \
  "3:\n"                                                                                                             \
  ATILE(96, 112, 6) ATILE(112, 96, 7)                                                                                \
  "v_add_u32 %4, 8*2080, %4\n v_add_u32 %5, 8*2080, %5\n v_add_u32 %7, 4, %7\n s_sub_u32 %6, %6, 1\n"                \
  "s_cmp_lg_u32 %6, 0\n s_cbranch_scc1 1b\n"                                                                         \
  "s_waitcnt lgkmcnt(0)\n v_smfmac_f32_32x32x32_bf16 %0, %3, v[120:127], %13\n s_nop 15\n"
#define ATTN_CLOBS "v96","v97","v98","v99","v100","v101","v102","v103","v104","v105","v106","v107","v108","v109","v110","v111", \
  "v112","v113","v114","v115","v116","v117","v118","v119","v120","v121","v122","v123","v124","v125","v126","v127","scc","memory"
#define CODE_BYTES 17408u
#define W2_N 7
#define SPLIT_AT 6

__global__ void __launch_bounds__(768, 3)
fused_kernel(const u16* __restrict__ x, const u16* __restrict__ wqkv, const u16* __restrict__ wo,
             const u16* __restrict__ w1, const u16* __restrict__ b1, const u16* __restrict__ w2,
             const u16* __restrict__ gw, u16* __restrict__ out, unsigned char* __restrict__ ws) {
  __shared__ __attribute__((aligned(16))) char smem_[L_END];
  LAS char* sm = (LAS char*)smem_;
  const unsigned sb = (unsigned)(size_t)sm;
  u64 pc0;
  asm volatile("s_getpc_b64 %0" : "=s"(pc0));
  pc0 &= ~255ull;
  asm volatile("" :: "s"(x), "s"(wqkv), "s"(wo), "s"(w1), "s"(b1), "s"(w2), "s"(gw), "s"(out), "s"(ws));
  const int lane = threadIdx.x & 63;
  const int wave = __builtin_amdgcn_readfirstlane(threadIdx.x >> 6);
  const int b = blockIdx.x, xcd = b & 7, idx = b >> 3;
  const int bi = 4 * (xcd & 1) + (idx & 3), hd = 8 * (xcd >> 1) + (idx >> 2);
  const bool has_w1 = (hd & 7) != 7;
  const int cw = hd - (hd >> 3);
  const int rot = ((hd + bi) & 3) * 2;
  const int hh = lane >> 5, l31 = lane & 31, l15 = lane & 15, g4 = lane >> 4;
  const int q4 = l15 >> 2, p4 = lane & 3, r8 = lane >> 3, sl8 = lane & 7;
  const rsrc_t rws = mk_rsrc(ws, WS_END);
  const u32x4 ones = {0x3f803f80u, 0x3f803f80u, 0x3f803f80u, 0x3f803f80u};
  STAMP(0);

  auto mk_tag = [&](u32x2 e) {
    u64 z = ((u64)(unsigned)__builtin_amdgcn_readfirstlane(e.y) << 32 | (u64)(unsigned)__builtin_amdgcn_readfirstlane(e.x)) + 0x9E3779B97F4A7C15ull;
    z = (z ^ (z >> 30)) * 0xBF58476D1CE4E5B9ull;
    z = (z ^ (z >> 27)) * 0x94D049BB133111EBull;
    z ^= z >> 31;
    return (u32x2){(unsigned)z, (unsigned)(z >> 32)};
  };
  u32x2 tag;
  auto get_tag = [&]() {
    const u32x2 t = *(LAS u32x2*)(sm + L_FL + 256);
    tag = (u32x2){(unsigned)__builtin_amdgcn_readfirstlane(t.x), (unsigned)__builtin_amdgcn_readfirstlane(t.y)};
  };
  const unsigned xa0 = (unsigned)(64 * (wave & 3) + l15) * 128u + (unsigned)(g4 ^ (l15 >> 1)) * 16u;
  const unsigned xa1 = xa0 ^ 64u;
#define NEXT_BUF(v) v = (v == 2 * STG) ? 0 : v + STG
  const unsigned qo = (unsigned)(16 * (wave & 3) + (((lane >> 1) & 15) ^ ((g4 & 1) << 2))) * 3072u + (unsigned)hh * 1024u + (unsigned)hd * 32u + (lane & 1) * 16u;
  auto qkv_issue = [&](int n, unsigned buf) {
    const u32x4 d = mk_desc(wqkv, 512u * 3072u);
    const unsigned so = ((unsigned)(n + rot) & 7u) * (64u * 3072u);
    dma16(d, sb + buf + 32768u + (wave & 3) * 3584u, qo, so);
    if (lane < 32) dma16_next<1024>(d, qo, so + 1024u);
  };

  if (__builtin_expect(wave >= 4 && wave < 8, 1)) {
    const int lw = wave - 4;
    const u32x4 dx = mk_desc(x, 2048u * 1024u);
    const unsigned xo0 = (unsigned)(256 * bi + 64 * lw + r8) * 1024u + (unsigned)(sl8 ^ (r8 >> 1)) * 16u;
    const unsigned xo1 = (xo0 ^ 64u) + 8192u - 1024u;
    const u32x4 d1 = mk_desc(w1, 512u * 3584u);
    const unsigned wo0 = (unsigned)(16 * lw + r8) * 3584u + (unsigned)cw * 128u + (unsigned)(sl8 ^ (r8 & 2)) * 16u;
    const unsigned wo1 = (wo0 ^ 64u) + 8u * 3584u - 1024u;
    unsigned buf = 0;
    STAMP(16);
#pragma unroll 1
    for (int n = 0; n < 8; ++n) {
      const unsigned ks = (unsigned)(n + rot) & 7u, d = sb + buf + lw * 8192u;
      if (n < 2) qkv_issue(n, buf);
#pragma unroll
      for (int u = 0; u < 2; ++u) {
        dma16(dx, d + u * 4096u, xo0, ks * 128u + u * 32768u);
        dma16_next<1024>(dx, xo1, ks * 128u + u * 32768u);
        dma16_next<2048>(dx, xo0, ks * 128u + u * 32768u + 16384u - 2048u);
        dma16_next<3072>(dx, xo1, ks * 128u + u * 32768u + 16384u - 2048u);
      }
      if (has_w1) {
        dma16(d1, sb + buf + 32768u + 1536u + lw * 3584u, wo0, ks * (64u * 3584u));
        dma16_next<1024>(d1, wo1, ks * (64u * 3584u));
      }
      if (n == 0 && lw == 0) {
        if (has_w1) { if (lane < 8) dma16(mk_desc(b1, 3584u), sb + L_B1, lane * 16u + (unsigned)cw * 128u, 0u); }
        else dma16(mk_desc(gw, 1024u), sb + L_G, lane * 16u, 0u);
      }
      if (n == 3) {
        if (has_w1) {
          const u32x4 d2 = mk_desc(w2, 1792u * 1024u);
          const unsigned o2 = (unsigned)(16 * (r8 >> 2) + (r8 & 3)) * 1024u + (unsigned)bi * 128u + (unsigned)sl8 * 16u;
          const unsigned so = (unsigned)(64 * cw + 32 * (lw >> 1) + 8 * (lw & 1)) * 1024u;
          dma16(d2, sb + L_RT + lw * 2048u, o2, so); dma16_next<1024>(d2, o2, so + 3072u);
        } else {
          const u32x4 dO = mk_desc(wo, 512u * 1024u);
          const unsigned oo = (unsigned)r8 * 1024u + (unsigned)bi * 128u + (unsigned)sl8 * 16u;
          const unsigned so = (unsigned)((hd >> 3) * 8 + 2 * lw) * 16384u;
          dma16(dO, sb + L_RT + lw * 2048u, oo, so); dma16_next<1024>(dO, oo, so + 7168u);
          dma16(dO, sb + 2 * STG + 32768u + 1536u + lw * 3584u, oo, so + 16384u); dma16_next<1024>(dO, oo, so + 16384u + 7168u);
        }
      }
      NEXT_BUF(buf);
      if (n >= 1) {
        if (n == 3) WAITV(12); else if (n == 1) { if (has_w1) WAITV(12); else WAITV(10); } else if (has_w1) WAITV(10); else WAITV(8);
        if (n == 1) STAMP(20);
        BARRIER();
        if (n == 1) STAMP(24);
      }
    }
    WAITV(0); BARRIER();
    BARRIER();
  } else if (wave < 4) {
    const int j = wave;
    __builtin_amdgcn_s_setprio(1);
    if (wave == 0) ((LAS unsigned*)(sm + L_FL))[lane] = (lane == 8 || lane == 0) ? 8u : 0u;
    f32x4 acc[4][3];
#pragma unroll
    for (int t = 0; t < 4; ++t)
#pragma unroll
      for (int c = 0; c < 3; ++c) acc[t][c] = (f32x4){0.f, 0.f, 0.f, 0.f};
    const unsigned wa0 = 32768u + (unsigned)(g4 >> 1) * 3584u + (unsigned)(12 * (g4 & 1) + q4) * 32u + p4 * 8u;
    const unsigned wa1 = wa0 ^ 128u;
    bf16x8 xA[4], wA[3], xB[4], wB[3];
#pragma unroll
    for (int t = 0; t < 4; ++t) xB[t] = __builtin_bit_cast(bf16x8, (u32x4){0u, 0u, 0u, 0u});
#pragma unroll
    for (int c = 0; c < 3; ++c) wB[c] = __builtin_bit_cast(bf16x8, (u32x4){0u, 0u, 0u, 0u});
#define QKV_RD(X, W, kk)                                                                                           \
    _Pragma("unroll") for (int c = 0; c < 3; ++c) W[c] = lds_tr2(sm, buf + wa0 + kk * 7168 + c * 512, buf + wa1 + kk * 7168 + c * 512); \
    _Pragma("unroll") for (int t = 0; t < 4; ++t) X[t] = lds_frag(sm, buf + (kk ? xa1 : xa0) + t * 2048);
#define QKV_MM(X, W)                                                                                               \
    _Pragma("unroll") for (int t = 0; t < 4; ++t) {                                                                \
      acc[t][0] = __builtin_amdgcn_mfma_f32_16x16x32_bf16(W[0], X[t], acc[t][0], 0, 0, 0);                         \
      acc[t][1] = __builtin_amdgcn_mfma_f32_16x16x32_bf16(W[1], X[t], acc[t][1], 0, 0, 0);                         \
      acc[t][2] = __builtin_amdgcn_mfma_f32_16x16x32_bf16(X[t], W[2], acc[t][2], 0, 0, 0);                         \
    }                                                                                                              \
    _Pragma("unroll") for (int u = 0; u < 10; ++u) { __builtin_amdgcn_sched_group_barrier(0x008, 1, 0); __builtin_amdgcn_sched_group_barrier(0x100, 1, 0); } \
    __builtin_amdgcn_sched_group_barrier(0x008, 2, 0);                                                             \
    asm volatile("" : "+v"(acc[0][0]), "+v"(acc[0][1]), "+v"(acc[0][2]), "+v"(acc[1][0]), "+v"(acc[1][1]), "+v"(acc[1][2]), \
                      "+v"(acc[2][0]), "+v"(acc[2][1]), "+v"(acc[2][2]), "+v"(acc[3][0]), "+v"(acc[3][1]), "+v"(acc[3][2]) :: "memory"); \
    __builtin_amdgcn_sched_barrier(0);
    unsigned buf = 0, nbuf = 2 * STG;
#pragma unroll 1
    for (int n = 0; n < 8; ++n) {
      BARRIER();
      QKV_RD(xA, wA, 0)
      QKV_MM(xB, wB)
      if (n < 6) qkv_issue(n + 2, nbuf);
      QKV_RD(xB, wB, 1)
      QKV_MM(xA, wA)
      NEXT_BUF(buf); NEXT_BUF(nbuf);
      if (n < 6) WAITV(2); else WAITV(0);
    }
    get_tag();
    QKV_MM(xB, wB)
    STAMP(1);
    const float qs = 0.25f * 1.4426950408889634f;
    const int rho = (l15 & 3) + 8 * ((l15 >> 2) & 1) + 4 * (l15 >> 3);
    const unsigned vo = 1024u + (unsigned)(g4 & 1) * 512u + (unsigned)((g4 >> 1) * 16 + rho) * 16u;
    const unsigned go = WS_KV + (unsigned)(hd * 64 + bi * 8 + 2 * j) * 2048u;
#pragma unroll
    for (int c = 2; c >= 0; --c)
#pragma unroll
      for (int tp = 0; tp < 2; ++tp) {
        const float sc = c ? 1.f : qs;
        const f32x4 a = acc[2 * tp][c] * sc, e = acc[2 * tp + 1][c] * sc;
        const auto r0 = __builtin_amdgcn_permlane16_swap(pk_bf16(a[0], a[1]), pk_bf16(e[0], e[1]), false, false);
        const auto r1 = __builtin_amdgcn_permlane16_swap(pk_bf16(a[2], a[3]), pk_bf16(e[2], e[3]), false, false);
        const u32x4 o = {r0[0], r1[0], r0[1], r1[1]};
        if (c == 0) *(LAS u32x4*)(sm + L_Q + (2 * j + tp) * 1024 + lane * 16) = o;
        else {
          ST128(o, (c == 1 ? lane * 16u : vo) + tp * 2048, go);
          *(LAS u32x4*)(sm + (2 * j + tp) * TILE + (c == 1 ? lane * 16u : vo + (g4 & 1) * 16u)) = o;
        }
      }
    if (lane < 4) *(LAS u32x4*)(sm + (2 * j + (lane >> 1)) * TILE + 1536 + (lane & 1) * 528) = ones;
    STAMP(10);
    BARRIER();
    WAITV(0);
    if (lane == 0) __builtin_amdgcn_raw_buffer_store_b64(tag, rws, WS_FKV + (unsigned)((hd * 8 + bi) * 4 + j) * 8u, 0, AUX_SC1);
    STAMP(11);
  } else {
    const int j = wave - 8;
    u32x2 ep = {0u, 0u};
    if (j == 0) ep = __builtin_amdgcn_raw_buffer_load_b64(rws, WS_EPOCH, 0, AUX_SC1);
    if (j == 1 && lane < 5)
      ep.x = __builtin_amdgcn_raw_buffer_load_b32(mk_rsrc((const void*)pc0, CODE_BYTES), (unsigned)(lane * 32 + idx) * 128u, 0, 0);
    auto put_tag = [&]() { if (j == 0) *(LAS u32x2*)(sm + L_FL + 256) = mk_tag(ep); else asm volatile("" :: "v"(ep)); };
    auto retile = [&](unsigned img, int ks) {
      const unsigned ta = img + (unsigned)(8 * hh + q4) * 128u + (unsigned)(4 * (g4 & 1) + 2 * (p4 & 1) + (p4 >> 1)) * 8u;
      ST128(__builtin_bit_cast(u32x4, lds_tr2(sm, ta, ta + 512)), WS_WT + lane * 16, (2 * bi * 144 + ks) * 1024);
      ST128(__builtin_bit_cast(u32x4, lds_tr2(sm, ta + 64, ta + 576)), WS_WT + lane * 16, ((2 * bi + 1) * 144 + ks) * 1024);
    };
    auto clear_o = [&]() {
      const u32x4 inv = {0xffffffffu, 0xffffffffu, 0xffffffffu, 0xffffffffu};
      const unsigned oo = WS_OF + (unsigned)((8 * bi + 2 * j) * 32 + hd) * 1024u + lane * 16u;
      ST128(inv, oo, 0); ST128(inv, oo + 32768, 0);
    };
    u32x4 kvd[3][4];
    auto kv_load = [&](int n0, int cnt) {
      glb_wait(rws, WS_FKV + (unsigned)((hd * 8 + ((bi + n0 + (lane < cnt ? lane : 0)) & 7)) * 4 + j) * 8u, tag);
#pragma unroll
      for (int u = 0; u < 3; ++u)
        if (u < cnt) {
          const unsigned go = WS_KV + (unsigned)(hd * 64 + ((bi + n0 + u) & 7) * 8 + 2 * j) * 2048u;
#pragma unroll
          for (int e = 0; e < 4; ++e) kvd[u][e] = LD128(lane * 16 + e * 1024, go);
        }
    };
    auto kv_store = [&](int n0, int cnt) {
#pragma unroll
      for (int u = 0; u < 3; ++u)
        if (u < cnt) {
          LAS char* dst = sm + ((n0 + u) * 8 + 2 * j) * TILE + lane * 16;
          *(LAS u32x4*)(dst) = kvd[u][0];
          *(LAS u32x4*)(dst + 1024 + hh * 16) = kvd[u][1];
          *(LAS u32x4*)(dst + TILE) = kvd[u][2];
          *(LAS u32x4*)(dst + TILE + 1024 + hh * 16) = kvd[u][3];
        }
      if (lane < 4 * cnt) *(LAS u32x4*)(sm + ((n0 + (lane >> 2)) * 8 + 2 * j + ((lane >> 1) & 1)) * TILE + 1536 + (lane & 1) * 528) = ones;
      WAITL0();
      if (lane < cnt) lds_add(sm, n0 + lane, 2u);
    };
    if (has_w1) {
      f32x4 acc[4][4];
      const unsigned wa = 32768u + 1536u + (unsigned)(g4 >> 1) * 3584u + (unsigned)(8 * (g4 & 1) + q4) * 128u +
                          (unsigned)((q4 >> 1) | ((g4 & 1) << 1)) * 32u + p4 * 8u;
      bf16x8 xA[4], wA[4], xB[4], wB[4];
#pragma unroll
      for (int t = 0; t < 4; ++t) { xB[t] = __builtin_bit_cast(bf16x8, (u32x4){0u, 0u, 0u, 0u}); wB[t] = xB[t]; }
#define W1_RD(X, W, kk)                                                                                            \
      _Pragma("unroll") for (int c = 0; c < 4; ++c) W[c] = lds_tr2(sm, buf + (wa ^ (unsigned)(c * 32)) + kk * 7168, buf + (wa ^ (unsigned)(c * 32)) + kk * 7168 + 512); \
      _Pragma("unroll") for (int t = 0; t < 4; ++t) X[t] = lds_frag(sm, buf + (kk ? xa1 : xa0) + t * 2048);
#define W1_MM(X, W)                                                                                                \
      _Pragma("unroll") for (int t = 0; t < 4; ++t)                                                                \
        _Pragma("unroll") for (int c = 0; c < 4; ++c) acc[t][c] = __builtin_amdgcn_mfma_f32_16x16x32_bf16(W[c], X[t], acc[t][c], 0, 0, 0); \
      _Pragma("unroll") for (int u = 0; u < 12; ++u) { __builtin_amdgcn_sched_group_barrier(0x008, 1, 0); __builtin_amdgcn_sched_group_barrier(0x100, 1, 0); } \
      __builtin_amdgcn_sched_group_barrier(0x008, 4, 0);                                                           \
      asm volatile("" : "+v"(acc[0][0]), "+v"(acc[0][1]), "+v"(acc[0][2]), "+v"(acc[0][3]), "+v"(acc[1][0]), "+v"(acc[1][1]), "+v"(acc[1][2]), "+v"(acc[1][3]), \
                        "+v"(acc[2][0]), "+v"(acc[2][1]), "+v"(acc[2][2]), "+v"(acc[2][3]), "+v"(acc[3][0]), "+v"(acc[3][1]), "+v"(acc[3][2]), "+v"(acc[3][3]) :: "memory"); \
      __builtin_amdgcn_sched_barrier(0);
      unsigned buf = 0;
      BARRIER();
#pragma unroll
      for (int c = 0; c < 4; ++c) {
        const u32x2 bv = *(LAS u32x2*)(sm + L_B1 + (16 * c + 4 * g4) * 2);
#pragma unroll
        for (int t = 0; t < 4; ++t) acc[t][c] = (f32x4){bf_lo(bv.x), bf_hi(bv.x), bf_lo(bv.y), bf_hi(bv.y)};
      }
#pragma unroll 1
      for (int n = 0; n < 8; ++n) {
        W1_RD(xA, wA, 0)
        W1_MM(xB, wB)
        W1_RD(xB, wB, 1)
        W1_MM(xA, wA)
        NEXT_BUF(buf);
        if (n == 2) { put_tag(); clear_o(); }
        if (n == 5) retile(L_RT + j * 2048u, 4 * cw + j);
        if (n < 7) BARRIER();
      }
      WAITV(0); BARRIER();
      __builtin_amdgcn_s_setprio(3);
      get_tag();
      W1_MM(xB, wB)
      STAMP(1);
      const unsigned ao = WS_ACT + (unsigned)(8 * bi + 2 * j) * 114688u + (unsigned)cw * 4096u + (unsigned)g4 * 512u + l15 * 16u;
      auto mish2 = [&](int t0) {
#pragma unroll
        for (int t = t0; t < t0 + 2; ++t)
#pragma unroll
          for (int pc = 0; pc < 2; ++pc) {
            u32x4 o;
#pragma unroll
            for (int e = 0; e < 4; ++e) {
              f32x2 hv = {acc[t][2 * pc + (e >> 1)][2 * (e & 1)], acc[t][2 * pc + (e >> 1)][2 * (e & 1) + 1]};
              f32x2 a = hv * 1.4426950408889634f;
              f32x2 ex = {__builtin_amdgcn_exp2f(a.x), __builtin_amdgcn_exp2f(a.y)};
              f32x2 d = ex * (ex + 2.f) + 2.f;
              f32x2 r = {__builtin_amdgcn_rcpf(d.x), __builtin_amdgcn_rcpf(d.y)};
              f32x2 y = hv * (r * -2.f + 1.f);
              o[e] = pk_bf16(y.x, y.y);
            }
            ST128(o, ao + (t >> 1) * 114688 + pc * 2048 + (t & 1) * 256, 0);
          }
      };
      mish2(0);
      kv_load(1, 1);
      mish2(2);
      STAMP(2);
      kv_store(1, 1);
      WAITV(0);
      if (lane == 0) __builtin_amdgcn_raw_buffer_store_b64(tag, rws, WS_FACT + (unsigned)((bi * 4 + j) * 32 + hd) * 8u, 0, AUX_SC1);
    } else {
      f32x4 acc[4];
#pragma unroll
      for (int t = 0; t < 4; ++t) acc[t] = (f32x4){0.f, 0.f, 0.f, 0.f};
      unsigned buf = 0;
#pragma unroll 1
      for (int n = 0; n < 8; ++n) {
        BARRIER();
        if (hd == 7) {
#pragma unroll
          for (int kk = 0; kk < 2; ++kk) {
            const bf16x8 gf = lds_frag(sm, L_G + ((n + rot) & 7) * 128 + kk * 64 + g4 * 16);
#pragma unroll
            for (int t = 0; t < 4; ++t) acc[t] = __builtin_amdgcn_mfma_f32_16x16x32_bf16(gf, lds_frag(sm, buf + (kk ? xa1 : xa0) + t * 2048), acc[t], 0, 0, 0);
          }
        }
        if (n == 2) { put_tag(); clear_o(); }
        if (n == 5 || n == 6) retile(n == 5 ? L_RT + j * 2048u : 2 * STG + 32768u + 1536u + j * 3584u, 112 + (hd >> 3) * 8 + 2 * j + (n - 5));
        NEXT_BUF(buf);
      }
      WAITV(0);
      get_tag();
      BARRIER();
      __builtin_amdgcn_s_setprio(3);
      STAMP(1);
      if (hd == 7) {
        const float lg = g4 == 0 ? acc[0][0] : g4 == 1 ? acc[1][0] : g4 == 2 ? acc[2][0] : acc[3][0];
        const float gv = __builtin_amdgcn_rcpf(1.f + __builtin_amdgcn_exp2f(lg * -1.4426950408889634f));
        __builtin_amdgcn_raw_buffer_store_b32(__float_as_uint(gv), rws, WS_G + (unsigned)(256 * bi + 64 * j + lane) * 4u, 0, AUX_SC1);
      }
      WAITV(0);
      if (lane == 0) __builtin_amdgcn_raw_buffer_store_b64(tag, rws, WS_FACT + (unsigned)((bi * 4 + j) * 32 + hd) * 8u, 0, AUX_SC1);
      kv_load(1, 1);
      STAMP(2);
      kv_store(1, 1);
    }
#pragma unroll 1
    for (int n0 = 2; n0 < 8; n0 += 3) {
      kv_load(n0, 3);
      kv_store(n0, 3);
    }
    STAMP(3);
    const int tm = hd >> 3, cp = hd & 7;
    if (j == 0) {
      glb_wait(rws, !hh ? WS_FACT + (unsigned)((4 * bi + tm) * 32 + l31) * 8u : WS_FKV + (unsigned)(l31 * 8 + cp) * 32u, tag);
      glb_wait(rws, WS_FACT + (unsigned)((4 * bi + p4) * 32 + 7) * 8u, tag);
      if (lane == 0) lds_add(sm, 18, 1u);
    } else lds_wait(sm, 18, 1u);
    STAMP(4);
    {
      *(LAS unsigned*)(sm + L_G + (64 * j + lane) * 4) = __builtin_amdgcn_raw_buffer_load_b32(rws, WS_G + (unsigned)(256 * bi + 64 * j + lane) * 4u, 0, AUX_SC1);
      f32x16 acc[2][2];
#pragma unroll
      for (int t = 0; t < 2; ++t)
#pragma unroll
        for (int c = 0; c < 2; ++c)
#pragma unroll
          for (int r = 0; r < 16; ++r) acc[t][c][r] = 0.f;
      const unsigned ab = WS_ACT + (unsigned)(8 * bi + 2 * tm + (j & 1)) * 114688u + (unsigned)(28 * j) * 1024u;
      const unsigned ab1 = WS_ACT + (unsigned)(8 * bi + 2 * tm + ((j & 1) ^ 1)) * 114688u + (unsigned)(28 * j) * 1024u;
      const unsigned wb = WS_WT + (unsigned)((2 * cp + (j >> 1)) * 144 + 28 * j) * 1024u;
      const unsigned wb1 = WS_WT + (unsigned)((2 * cp + ((j >> 1) ^ 1)) * 144 + 28 * j) * 1024u;
      u32x4 af[4][2], wf[4][2];
#pragma unroll
      for (int u = 0; u < 4; ++u) {
        af[u][0] = LD128(lane * 16 + u * 1024, ab); af[u][1] = LD128(lane * 16 + u * 1024, ab1);
        wf[u][0] = LD128(lane * 16 + u * 1024, wb); wf[u][1] = LD128(lane * 16 + u * 1024, wb1);
      }
      WAITL0();
      if (lane == 0) lds_add(sm, 17, 1u);
#pragma unroll 1
      for (int it = 1; it <= W2_N; ++it) {
        const unsigned nx = (it < 7) ? it * 4096u : 0u;
        const rsrc_t rws = mk_rsrc(ws, it < 7 ? WS_END : 0u);
#pragma unroll
        for (int u = 0; u < 4; ++u) {
#pragma unroll
          for (int t = 0; t < 2; ++t)
#pragma unroll
            for (int c = 0; c < 2; ++c)
              acc[t][c] = __builtin_amdgcn_mfma_f32_32x32x16_bf16(__builtin_bit_cast(bf16x8, wf[u][c]), __builtin_bit_cast(bf16x8, af[u][t]), acc[t][c], 0, 0, 0);
          af[u][0] = LD128(lane * 16 + u * 1024, ab + nx); af[u][1] = LD128(lane * 16 + u * 1024, ab1 + nx);
          wf[u][0] = LD128(lane * 16 + u * 1024, wb + nx); wf[u][1] = LD128(lane * 16 + u * 1024, wb1 + nx);
          __builtin_amdgcn_sched_barrier(0);
        }
      }
#pragma unroll
      for (int q = 0; q < 4; ++q) {
        LAS f32x4* bp = (LAS f32x4*)(sm + L_BP + (unsigned)(q ^ j) * 4096u + lane * 16);
        const f32x16& av = acc[q & 1][q >> 1];
        f32x4 v[4];
#pragma unroll
        for (int u = 0; u < 4; ++u) v[u] = (f32x4){av[4 * u], av[4 * u + 1], av[4 * u + 2], av[4 * u + 3]};
        if (q > 0) {
          lds_wait(sm, 32, 4u * q);
#pragma unroll
          for (int u = 0; u < 4; ++u) v[u] += bp[u * 64];
        }
#pragma unroll
        for (int u = 0; u < 4; ++u) bp[u * 64] = v[u];
        WAITL0();
        if (q < 3 && lane == 0) lds_add(sm, 32, 1u);
      }
    }
    STAMP(5);
    STAMP_OUT();
    if (b == 0 && j == 0) {
#pragma unroll 1
      for (int u = 0; u < 4; ++u) glb_wait(rws, WS_FACT + (unsigned)((2 * u + hh) * 128 + l31) * 8u, tag);
      if (lane == 0) __builtin_amdgcn_raw_buffer_store_b64(tag, rws, WS_EPOCH, 0, AUX_SC1);
    }
    return;
  }

  STAMP(2);
  const int w = wave;
  const unsigned ka = lane * 16;
  const unsigned va = 1024u + (l31 < 16 ? (unsigned)(hh * 16 + l31) * 16u : ((l31 >= 20 && l31 < 28) ? (unsigned)hh * 256u : 512u));
  const int IDX = 0xDDDD;
  const bf16x8 qf = lds_frag(sm, L_Q + w * 1024 + lane * 16);
  f32x16 negm, O;
  {
    f32x16 z;
#pragma unroll
    for (int r = 0; r < 16; ++r) { z[r] = 0.f; O[r] = 0.f; }
    f32x16 s0 = __builtin_amdgcn_mfma_f32_32x32x16_bf16(lds_frag(sm, ka), qf, z, 0, 0, 0);
    float m = s0[0];
#pragma unroll
    for (int r = 1; r < 16; ++r) m = fmaxf(m, s0[r]);
    m = fmaxf(m, __shfl_xor(m, 32));
#pragma unroll
    for (int r = 0; r < 16; ++r) negm[r] = -m;
  }
  STAMP(7);
  const int tm = hd >> 3, cp = hd & 7;
  const int tt = w & 1, kq = w >> 1;
  u32x4 wf[2][8];
  {
    u32x4 kf, v0, v1;
    unsigned kb = sb + ka, vb = sb + va, fb = sb + L_FL + 4, ft;
    int cnt = SPLIT_AT, st, sp;
    if (w < 4) __builtin_amdgcn_s_setprio(2); else __builtin_amdgcn_s_setprio(1);
    asm volatile(ATTN_LOOP
                 : "+v"(O), "=&v"(kf), "=&v"(v0), "=&v"(v1), "+v"(kb), "+v"(vb), "+s"(cnt), "+v"(fb), "=&v"(ft), "=&s"(st), "=&s"(sp)
                 : "v"(qf), "v"(negm), "v"(IDX)
                 : ATTN_CLOBS);
    STAMP(12);
    if (w < 4) __builtin_amdgcn_s_setprio(0);
    lds_wait(sm, 17, 4u);
    const unsigned wb = WS_WT + (unsigned)(2 * cp * 144 + 112 + 8 * kq) * 1024u;
#pragma unroll
    for (int u = 0; u < 8; ++u) {
      wf[0][u] = LD128(lane * 16 + (u & 3) * 1024, wb + (u >> 2) * 4096);
      wf[1][u] = LD128(lane * 16 + (u & 3) * 1024, wb + 147456 + (u >> 2) * 4096);
    }
    STAMP(13);
    cnt = 8 - SPLIT_AT;
    asm volatile(ATTN_LOOP
                 : "+v"(O), "=&v"(kf), "=&v"(v0), "=&v"(v1), "+v"(kb), "+v"(vb), "+s"(cnt), "+v"(fb), "=&v"(ft), "=&s"(st), "=&s"(sp)
                 : "v"(qf), "v"(negm), "v"(IDX)
                 : ATTN_CLOBS);
  }
  STAMP(3);
  float lsum = hh ? O[12] : O[8];
  {
    float chk = lsum * 0.f;
#pragma unroll
    for (int r = 0; r < 8; ++r) chk = __builtin_fmaf(O[r], 0.f, chk);
    if (__any(chk != chk)) {
      float m = -3.0e38f;
      f32x16 z;
#pragma unroll
      for (int r = 0; r < 16; ++r) { z[r] = 0.f; O[r] = 0.f; }
#pragma unroll 1
      for (int t = 0; t < 64; ++t) {
        f32x16 S = __builtin_amdgcn_mfma_f32_32x32x16_bf16(lds_frag(sm, ka + t * TILE), qf, z, 0, 0, 0);
        float mx = m;
#pragma unroll
        for (int r = 0; r < 16; ++r) mx = fmaxf(mx, S[r]);
        mx = fmaxf(mx, __shfl_xor(mx, 32));
        const float al = __builtin_amdgcn_exp2f(m - mx);
        m = mx;
        f32x8 p0, p1;
#pragma unroll
        for (int r = 0; r < 16; ++r) O[r] *= al;
#pragma unroll
        for (int r = 0; r < 8; ++r) { p0[r] = __builtin_amdgcn_exp2f(S[r] - m); p1[r] = __builtin_amdgcn_exp2f(S[8 + r] - m); }
        O = __builtin_amdgcn_smfmac_f32_32x32x32_bf16(lds_frag(sm, va + t * TILE), __builtin_bit_cast(bf16x16, p0), O, IDX, 0, 0);
        O = __builtin_amdgcn_smfmac_f32_32x32x32_bf16(lds_frag(sm, va + t * TILE + 528), __builtin_bit_cast(bf16x16, p1), O, IDX, 0, 0);
      }
      lsum = hh ? O[12] : O[8];
    }
  }
  {
    const float sc = *(LAS float*)(sm + L_G + (32 * w + l31) * 4) * __builtin_amdgcn_rcpf(lsum);
    u32x4 o;
#pragma unroll
    for (int e = 0; e < 4; ++e) {
      unsigned u = pk_bf16(O[2 * e] * sc, O[2 * e + 1] * sc);
      o[e] = (u == 0xffffffffu) ? 0x7fc07fc0u : u;
    }
    ST128(o, WS_OF + (unsigned)((8 * bi + w) * 32 + hd) * 1024u + lane * 16u, 0);
  }
  {
    STAMP(4);
    BARRIER();
    u32x4 of[8];
    const unsigned ob = WS_OF + (unsigned)((8 * bi + 2 * tm + tt) * 32 + 8 * kq) * 1024u;
    unsigned todo = 0xffu;
#pragma unroll 1
    for (unsigned s = 0; s < SPIN_CAP && todo; ++s) {
      CFENCE();
#pragma unroll
      for (int u = 0; u < 8; ++u)
        if (todo & (1u << u)) of[u] = LD128(lane * 16 + (u & 3) * 1024, ob + (u >> 2) * 4096);
#pragma unroll
      for (int u = 0; u < 8; ++u) {
        const unsigned mx = max(max(of[u][0], of[u][1]), max(of[u][2], of[u][3]));
        if (__all(mx != 0xffffffffu)) todo &= ~(1u << u);
      }
    }
    STAMP(5);
    f32x16 acc[2];
#pragma unroll
    for (int r = 0; r < 16; ++r) { acc[0][r] = 0.f; acc[1][r] = 0.f; }
#pragma unroll
    for (int u = 0; u < 8; ++u) {
      acc[0] = __builtin_amdgcn_mfma_f32_32x32x16_bf16(__builtin_bit_cast(bf16x8, wf[0][u]), __builtin_bit_cast(bf16x8, of[u]), acc[0], 0, 0, 0);
      acc[1] = __builtin_amdgcn_mfma_f32_32x32x16_bf16(__builtin_bit_cast(bf16x8, wf[1][u]), __builtin_bit_cast(bf16x8, of[u]), acc[1], 0, 0, 0);
    }
    const float om = 1.f - *(LAS float*)(sm + L_G + (64 * tm + 32 * tt + l31) * 4);
    f32x4 a, c;
    {
      LAS f32x4* bp = (LAS f32x4*)(sm + L_BP + ((tt + 2 * (kq >> 1)) * 4 + 2 * (kq & 1)) * 1024 + lane * 16);
      a = bp[0] * om; c = bp[64] * om;
    }
#pragma unroll
    for (int p = 0; p < 4; ++p) {
      const f32x4 lo = {acc[p >> 1][8 * (p & 1)], acc[p >> 1][8 * (p & 1) + 1], acc[p >> 1][8 * (p & 1) + 2], acc[p >> 1][8 * (p & 1) + 3]};
      const f32x4 hi = {acc[p >> 1][8 * (p & 1) + 4], acc[p >> 1][8 * (p & 1) + 5], acc[p >> 1][8 * (p & 1) + 6], acc[p >> 1][8 * (p & 1) + 7]};
      if (p != kq) {
        LAS f32x4* d = (LAS f32x4*)(sm + ((tt * 4 + p) * 4 + kq) * 2048 + lane * 16);
        d[0] = lo; d[64] = hi;
      } else { a += lo; c += hi; }
    }
    BARRIER();
#pragma unroll
    for (int q = 1; q < 4; ++q) {
      LAS f32x4* d = (LAS f32x4*)(sm + ((tt * 4 + kq) * 4 + ((kq + q) & 3)) * 2048 + lane * 16);
      a += d[0]; c += d[64];
    }
    u32x4 o = {pk_bf16(a[0], a[1]), pk_bf16(a[2], a[3]), pk_bf16(c[0], c[1]), pk_bf16(c[2], c[3])};
    const rsrc_t ro = mk_rsrc(out, 2048u * 1024u);
    __builtin_amdgcn_raw_buffer_store_b128(o, ro, (unsigned)(256 * bi + 64 * tm + 32 * tt + l31) * 1024u + (unsigned)(64 * cp + 16 * kq + 8 * hh) * 2u, 0, 0);
  }
  STAMP(6);
  STAMP_OUT();
}

extern "C" void kernel_launch(void* const* d_in, const int* in_sizes, int n_in, void* d_out, int out_size,
                              void* d_ws, size_t ws_size, hipStream_t stream) {
  fused_kernel<<<256, 768, 0, stream>>>((const u16*)d_in[0], (const u16*)d_in[1], (const u16*)d_in[2], (const u16*)d_in[4],
                                        (const u16*)d_in[5], (const u16*)d_in[6], (const u16*)d_in[7], (u16*)d_out,
                                        (unsigned char*)d_ws);
}
```

```cpp
#define PADTEST 0
#include <hip/hip_runtime.h>
#include <hip/hip_bf16.h>

typedef unsigned short u16;
typedef unsigned long long u64;
typedef __bf16 bf16x8 __attribute__((ext_vector_type(8)));
typedef __bf16 bf16x16 __attribute__((ext_vector_type(16)));
typedef __bf16 bf16x2 __attribute__((ext_vector_type(2)));
typedef short s16x4 __attribute__((ext_vector_type(4)));
typedef float f32x2 __attribute__((ext_vector_type(2)));
typedef float f32x4 __attribute__((ext_vector_type(4)));
typedef float f32x8 __attribute__((ext_vector_type(8)));
typedef float f32x16 __attribute__((ext_vector_type(16)));
typedef unsigned u32x2 __attribute__((ext_vector_type(2)));
typedef unsigned u32x4 __attribute__((ext_vector_type(4)));
#define LAS __attribute__((address_space(3)))
#define GAS __attribute__((address_space(1)))
typedef __amdgpu_buffer_rsrc_t rsrc_t;

constexpr unsigned WS_EPOCH = 0;
constexpr unsigned WS_FKV = 4096;
constexpr unsigned WS_FACT = WS_FKV + 8192;
constexpr unsigned WS_G = 32768;
constexpr unsigned WS_KV = 65536;
constexpr unsigned WS_ACT = WS_KV + 32u * 131072u;
constexpr unsigned WS_WT = WS_ACT + 64u * 114688u;
constexpr unsigned WS_OF = WS_WT + 16u * 144u * 1024u;
constexpr unsigned WS_END = WS_OF + 64u * 32u * 1024u;
constexpr unsigned WS_DIAG = WS_END;

constexpr unsigned STG = 47104;
constexpr unsigned TILE = 2080;
constexpr unsigned L_RT = 3 * STG;
constexpr unsigned L_BP = 64 * TILE;
constexpr unsigned L_Q = L_BP + 16384;
constexpr unsigned L_B1 = L_Q + 8192;
constexpr unsigned L_G = 158720;
constexpr unsigned L_FL = L_G + 1024;
constexpr unsigned L_END = L_FL + 512;

constexpr unsigned SPIN_CAP = 1u << 15;
constexpr int AUX_SC1 = 16;
#define POLL_SLEEP 4

__device__ __forceinline__ unsigned pk_bf16(float a, float b) {
  f32x2 v = {a, b};
  return __builtin_bit_cast(unsigned, __builtin_convertvector(v, bf16x2));
}
__device__ __forceinline__ float bf_lo(unsigned u) { return __uint_as_float(u << 16); }
__device__ __forceinline__ float bf_hi(unsigned u) { return __uint_as_float(u & 0xffff0000u); }
__device__ __forceinline__ rsrc_t mk_rsrc(const void* p, unsigned bytes) {
  return __builtin_amdgcn_make_buffer_rsrc((void*)p, 0, bytes, 0x00020000);
}
__device__ __forceinline__ u32x4 mk_desc(const void* p, unsigned bytes) {
  const u64 a = (u64)p;
  return (u32x4){(unsigned)a, (unsigned)(a >> 32) & 0xffffu, bytes, 0x00020000u};
}
__device__ __forceinline__ void dma16(u32x4 desc, unsigned lds_addr, unsigned voff, unsigned soff) {
  asm volatile("s_mov_b32 m0, %0\n\ts_nop 3\n\tbuffer_load_dwordx4 %1, %2, %3 offen lds"
               :: "s"(lds_addr), "v"(voff), "s"(desc), "s"(soff) : "memory");
}
template <int OFF>
__device__ __forceinline__ void dma16_next(u32x4 desc, unsigned voff, unsigned soff) {
  asm volatile("buffer_load_dwordx4 %0, %1, %2 offen offset:%3 lds" :: "v"(voff), "s"(desc), "s"(soff), "i"(OFF) : "memory");
}
#define BARRIER() asm volatile("s_waitcnt lgkmcnt(0)\n\ts_barrier" ::: "memory")
#define WAITV(n) asm volatile("s_waitcnt vmcnt(" #n ")" ::: "memory")
#define WAITL0() asm volatile("s_waitcnt lgkmcnt(0)" ::: "memory")
#define CFENCE() asm volatile("" ::: "memory")
#define LD128(off, soff) __builtin_amdgcn_raw_buffer_load_b128(rws, off, soff, AUX_SC1)
#define ST128(v, off, soff) __builtin_amdgcn_raw_buffer_store_b128(v, rws, off, soff, AUX_SC1)

#define STAMP(k) do { if ((int)(k) == PADTEST) { pad_t = __builtin_amdgcn_s_memrealtime(); asm volatile("s_waitcnt lgkmcnt(0)" ::: "memory"); } } while (0)
#define STAMP_OUT() do { for (int z_ = 0; z_ < 4000 && __builtin_amdgcn_s_memrealtime() < pad_t + 6000ull; ++z_) __builtin_amdgcn_s_sleep(2); } while (0)
#define ACC_BEG() do {} while (0)
#define ACC_END(a) do {} while (0)

#define STAMP2(k) do {} while (0)
__device__ __forceinline__ bf16x8 lds_frag(LAS char* sm, unsigned off) { return *(LAS bf16x8*)(sm + off); }
__device__ __forceinline__ bf16x8 lds_tr2(LAS char* sm, unsigned o0, unsigned o1) {
  s16x4 a = __builtin_amdgcn_ds_read_tr16_b64_v4i16((LAS s16x4*)(sm + o0));
  s16x4 b = __builtin_amdgcn_ds_read_tr16_b64_v4i16((LAS s16x4*)(sm + o1));
  return __builtin_bit_cast(bf16x8, __builtin_shufflevector(a, b, 0, 1, 2, 3, 4, 5, 6, 7));
}
__device__ __forceinline__ void lds_add(LAS char* sm, unsigned idx, unsigned v) {
  __hip_atomic_fetch_add((LAS unsigned*)(sm + L_FL) + idx, v, __ATOMIC_RELAXED, __HIP_MEMORY_SCOPE_WORKGROUP);
}
__device__ __forceinline__ void lds_wait(LAS char* sm, unsigned idx, unsigned want) {
#pragma unroll 1
  for (unsigned s = 0; s < SPIN_CAP * 4; ++s) {
    CFENCE();
    unsigned v = __hip_atomic_load((LAS unsigned*)(sm + L_FL) + idx, __ATOMIC_RELAXED, __HIP_MEMORY_SCOPE_WORKGROUP);
    if ((unsigned)__builtin_amdgcn_readfirstlane(v) >= want) break;
    __builtin_amdgcn_s_sleep(1);
  }
  CFENCE();
}
__device__ __forceinline__ void glb_wait(rsrc_t rs, unsigned off, u32x2 tag) {
#pragma unroll 1
  for (unsigned s = 0; s < SPIN_CAP; ++s) {
    CFENCE();
    u32x2 v = __builtin_amdgcn_raw_buffer_load_b64(rs, off, 0, AUX_SC1);
    if (__all(v.x == tag.x && v.y == tag.y)) break;
    __builtin_amdgcn_s_sleep(POLL_SLEEP);
  }
  CFENCE();
}

#define AE(b, i) "v_exp_f32 v[" #b "+" #i "], v[" #b "+" #i "]\n"
#define ATILE(cur, oth, t)                                                                                          \
  AE(cur, 0) AE(cur, 1)                                                                                              \
  "s_waitcnt lgkmcnt(2)\n v_smfmac_f32_32x32x32_bf16 %0, %3, v[" #oth "+8:" #oth "+15], %13\n"                       \
  "ds_read_b128 %3, %5 offset:2080*" #t "+528\n"                                                                     \
  AE(cur, 2) AE(cur, 3) AE(cur, 4) AE(cur, 5)                                                                        \
  "s_waitcnt lgkmcnt(2)\n v_mfma_f32_32x32x16_bf16 v[" #oth ":" #oth "+15], %1, %11, %12\n"                          \
  "ds_read_b128 %1, %4 offset:2080*(" #t "+2)\n"                                                                     \
  AE(cur, 6) AE(cur, 7) AE(cur, 8) AE(cur, 9) AE(cur, 10)                                                            \
  "s_waitcnt lgkmcnt(2)\n v_smfmac_f32_32x32x32_bf16 %0, %2, v[" #cur ":" #cur "+7], %13\n"                          \
  "ds_read_b128 %2, %5 offset:2080*(" #t "+1)\n"                                                                     \
  AE(cur, 11) AE(cur, 12) AE(cur, 13) AE(cur, 14) AE(cur, 15)
#define ATTN_LOOP                                                                                                   \
  "ds_read_b128 %1, %4\n ds_read_b128 %3, %5\n"                                                                      \
  "v_mov_b32 v120, 0\n v_mov_b32 v121, 0\n v_mov_b32 v122, 0\n v_mov_b32 v123, 0\n"                                  \
  "v_mov_b32 v124, 0\n v_mov_b32 v125, 0\n v_mov_b32 v126, 0\n v_mov_b32 v127, 0\n"                                  \
  "s_waitcnt lgkmcnt(0)\n v_mfma_f32_32x32x16_bf16 v[96:111], %1, %11, %12\n"                                        \
  "ds_read_b128 %1, %4 offset:2080\n ds_read_b128 %2, %5\n s_nop 11\n"                                               \
  "1:\n"                                                                                                             \
  ATILE(96, 112, 0) ATILE(112, 96, 1) ATILE(96, 112, 2) ATILE(112, 96, 3) ATILE(96, 112, 4)                          \
  "ds_read_b32 %8, %7\n"                                                                                             \
  ATILE(112, 96, 5)                                                                                                  \
  "v_readfirstlane_b32 %9, %8\n s_nop 3\n s_cmp_eq_u32 %9, 8\n s_cbranch_scc1 3f\n s_mov_b32 %10, 0x20000\n"         \
  "2:\n s_sleep 1\n ds_read_b32 %8, %7\n s_waitcnt lgkmcnt(0)\n v_readfirstlane_b32 %9, %8\n s_sub_u32 %10, %10, 1\n" \
  "s_cmp_eq_u32 %10, 0\n s_cbranch_scc1 3f\n s_cmp_eq_u32 %9, 8\n s_cbranch_scc0 2b\n"                               \
  "3:\n"                                                                                                             \
  ATILE(96, 112, 6) ATILE(112, 96, 7)                                                                                \
  "v_add_u32 %4, 8*2080, %4\n v_add_u32 %5, 8*2080, %5\n v_add_u32 %7, 4, %7\n s_sub_u32 %6, %6, 1\n"                \
  "s_cmp_lg_u32 %6, 0\n s_cbranch_scc1 1b\n"                                                                         \
  "s_waitcnt lgkmcnt(0)\n v_smfmac_f32_32x32x32_bf16 %0, %3, v[120:127], %13\n s_nop 15\n"
#define ATTN_CLOBS "v96","v97","v98","v99","v100","v101","v102","v103","v104","v105","v106","v107","v108","v109","v110","v111", \
  "v112","v113","v114","v115","v116","v117","v118","v119","v120","v121","v122","v123","v124","v125","v126","v127","scc","memory"
#define CODE_BYTES 17408u
#define W2_N 7
#define OUT_AUX 0
#define SPLIT_AT 6

__global__ void __launch_bounds__(768, 3)
fused_kernel(const u16* __restrict__ x, const u16* __restrict__ wqkv, const u16* __restrict__ wo,
             const u16* __restrict__ w1, const u16* __restrict__ b1, const u16* __restrict__ w2,
             const u16* __restrict__ gw, u16* __restrict__ out, unsigned char* __restrict__ ws) {
  __shared__ __attribute__((aligned(16))) char smem_[L_END];
  LAS char* sm = (LAS char*)smem_;
  const unsigned sb = (unsigned)(size_t)sm;
  u64 pc0;
  asm volatile("s_getpc_b64 %0" : "=s"(pc0));
  pc0 &= ~255ull;
  asm volatile("" :: "s"(x), "s"(wqkv), "s"(wo), "s"(w1), "s"(b1), "s"(w2), "s"(gw), "s"(out), "s"(ws));
  const int lane = threadIdx.x & 63;
  const int wave = __builtin_amdgcn_readfirstlane(threadIdx.x >> 6);
  const int b = blockIdx.x, xcd = b & 7, idx = b >> 3;
  const int bi = 4 * (xcd & 1) + (idx & 3), hd = 8 * (xcd >> 1) + (idx >> 2);
  const bool has_w1 = (hd & 7) != 7;
  const int cw = hd - (hd >> 3);
  const int rot = ((hd + bi) & 3) * 2;
  const int hh = lane >> 5, l31 = lane & 31, l15 = lane & 15, g4 = lane >> 4;
  const int q4 = l15 >> 2, p4 = lane & 3, r8 = lane >> 3, sl8 = lane & 7;
  const rsrc_t rws = mk_rsrc(ws, WS_END);
  const u32x4 ones = {0x3f803f80u, 0x3f803f80u, 0x3f803f80u, 0x3f803f80u};
  u64 pad_t = 0;
  STAMP(0);

  auto mk_tag = [&](u32x2 e) {
    u64 z = ((u64)(unsigned)__builtin_amdgcn_readfirstlane(e.y) << 32 | (u64)(unsigned)__builtin_amdgcn_readfirstlane(e.x)) + 0x9E3779B97F4A7C15ull;
    z = (z ^ (z >> 30)) * 0xBF58476D1CE4E5B9ull;
    z = (z ^ (z >> 27)) * 0x94D049BB133111EBull;
    z ^= z >> 31;
    return (u32x2){(unsigned)z, (unsigned)(z >> 32)};
  };
  u32x2 tag;
  auto get_tag = [&]() {
    const u32x2 t = *(LAS u32x2*)(sm + L_FL + 256);
    tag = (u32x2){(unsigned)__builtin_amdgcn_readfirstlane(t.x), (unsigned)__builtin_amdgcn_readfirstlane(t.y)};
  };
  const unsigned xa0 = (unsigned)(64 * (wave & 3) + l15) * 128u + (unsigned)(g4 ^ (l15 >> 1)) * 16u;
  const unsigned xa1 = xa0 ^ 64u;
#define NEXT_BUF(v) v = (v == 2 * STG) ? 0 : v + STG
  const unsigned qo = (unsigned)(16 * (wave & 3) + (((lane >> 1) & 15) ^ ((g4 & 1) << 2))) * 3072u + (unsigned)hh * 1024u + (unsigned)hd * 32u + (lane & 1) * 16u;
  auto qkv_issue = [&](int n, unsigned buf) {
    const u32x4 d = mk_desc(wqkv, 512u * 3072u);
    const unsigned so = ((unsigned)(n + rot) & 7u) * (64u * 3072u);
    dma16(d, sb + buf + 32768u + (wave & 3) * 3584u, qo, so);
    if (lane < 32) dma16_next<1024>(d, qo, so + 1024u);
  };

  if (__builtin_expect(wave >= 4 && wave < 8, 1)) {
    const int lw = wave - 4;
    const u32x4 dx = mk_desc(x, 2048u * 1024u);
    const unsigned xo0 = (unsigned)(256 * bi + 64 * lw + r8) * 1024u + (unsigned)(sl8 ^ (r8 >> 1)) * 16u;
    const unsigned xo1 = (xo0 ^ 64u) + 8192u - 1024u;
    const u32x4 d1 = mk_desc(w1, 512u * 3584u);
    const unsigned wo0 = (unsigned)(16 * lw + r8) * 3584u + (unsigned)cw * 128u + (unsigned)(sl8 ^ (r8 & 2)) * 16u;
    const unsigned wo1 = (wo0 ^ 64u) + 8u * 3584u - 1024u;
    unsigned buf = 0;
    STAMP(16);
#pragma unroll 1
    for (int n = 0; n < 8; ++n) {
      const unsigned ks = (unsigned)(n + rot) & 7u, d = sb + buf + lw * 8192u;
      if (n < 2) qkv_issue(n, buf);
#pragma unroll
      for (int u = 0; u < 2; ++u) {
        dma16(dx, d + u * 4096u, xo0, ks * 128u + u * 32768u);
        dma16_next<1024>(dx, xo1, ks * 128u + u * 32768u);
        dma16_next<2048>(dx, xo0, ks * 128u + u * 32768u + 16384u - 2048u);
        dma16_next<3072>(dx, xo1, ks * 128u + u * 32768u + 16384u - 2048u);
      }
      if (has_w1) {
        dma16(d1, sb + buf + 32768u + 1536u + lw * 3584u, wo0, ks * (64u * 3584u));
        dma16_next<1024>(d1, wo1, ks * (64u * 3584u));
      }
      if (n == 0 && lw == 0) {
        if (has_w1) { if (lane < 8) dma16(mk_desc(b1, 3584u), sb + L_B1, lane * 16u + (unsigned)cw * 128u, 0u); }
        else dma16(mk_desc(gw, 1024u), sb + L_G, lane * 16u, 0u);
      }
      if (n == (has_w1 ? 7 : 3)) {
        if (has_w1) {
          const u32x4 d2 = mk_desc(w2, 1792u * 1024u);
          const unsigned o2 = (unsigned)(16 * (r8 >> 2) + (r8 & 3)) * 1024u + (unsigned)bi * 128u + (unsigned)sl8 * 16u;
          const unsigned so = (unsigned)(64 * cw + 32 * (lw >> 1) + 8 * (lw & 1)) * 1024u;
          dma16(d2, sb + L_RT + lw * 2048u, o2, so); dma16_next<1024>(d2, o2, so + 3072u);
        } else {
          const u32x4 dO = mk_desc(wo, 512u * 1024u);
          const unsigned oo = (unsigned)r8 * 1024u + (unsigned)bi * 128u + (unsigned)sl8 * 16u;
          const unsigned so = (unsigned)((hd >> 3) * 8 + 2 * lw) * 16384u;
          dma16(dO, sb + L_RT + lw * 2048u, oo, so); dma16_next<1024>(dO, oo, so + 7168u);
          dma16(dO, sb + 2 * STG + 32768u + 1536u + lw * 3584u, oo, so + 16384u); dma16_next<1024>(dO, oo, so + 16384u + 7168u);
        }
      }
      NEXT_BUF(buf);
      if (n >= 1) {
        if (n == (has_w1 ? 7 : 3)) WAITV(12); else if (n == 1) { if (has_w1) WAITV(12); else WAITV(10); } else if (has_w1) WAITV(10); else WAITV(8);
        if (n == 1) STAMP(20);
        BARRIER();
        if (n == 1) STAMP(24);
      }
    }
    if (has_w1) WAITV(2); else WAITV(0);
    BARRIER();
    WAITV(0); BARRIER();
  } else if (wave < 4) {
    const int j = wave;
    __builtin_amdgcn_s_setprio(1);
    if (wave == 0) ((LAS unsigned*)(sm + L_FL))[lane] = (lane == 8 || lane == 0) ? 8u : 0u;
    f32x4 acc[4][3];
#pragma unroll
    for (int t = 0; t < 4; ++t)
#pragma unroll
      for (int c = 0; c < 3; ++c) acc[t][c] = (f32x4){0.f, 0.f, 0.f, 0.f};
    const unsigned wa0 = 32768u + (unsigned)(g4 >> 1) * 3584u + (unsigned)(12 * (g4 & 1) + q4) * 32u + p4 * 8u;
    const unsigned wa1 = wa0 ^ 128u;
    bf16x8 xA[4], wA[3], xB[4], wB[3];
#pragma unroll
    for (int t = 0; t < 4; ++t) xB[t] = __builtin_bit_cast(bf16x8, (u32x4){0u, 0u, 0u, 0u});
#pragma unroll
    for (int c = 0; c < 3; ++c) wB[c] = __builtin_bit_cast(bf16x8, (u32x4){0u, 0u, 0u, 0u});
#define QKV_RD(X, W, kk)                                                                                           \
    _Pragma("unroll") for (int c = 0; c < 3; ++c) W[c] = lds_tr2(sm, buf + wa0 + kk * 7168 + c * 512, buf + wa1 + kk * 7168 + c * 512); \
    _Pragma("unroll") for (int t = 0; t < 4; ++t) X[t] = lds_frag(sm, buf + (kk ? xa1 : xa0) + t * 2048);
#define QKV_MM(X, W)                                                                                               \
    _Pragma("unroll") for (int t = 0; t < 4; ++t) {                                                                \
      acc[t][0] = __builtin_amdgcn_mfma_f32_16x16x32_bf16(W[0], X[t], acc[t][0], 0, 0, 0);                         \
      acc[t][1] = __builtin_amdgcn_mfma_f32_16x16x32_bf16(W[1], X[t], acc[t][1], 0, 0, 0);                         \
      acc[t][2] = __builtin_amdgcn_mfma_f32_16x16x32_bf16(X[t], W[2], acc[t][2], 0, 0, 0);                         \
    }                                                                                                              \
    _Pragma("unroll") for (int u = 0; u < 10; ++u) { __builtin_amdgcn_sched_group_barrier(0x008, 1, 0); __builtin_amdgcn_sched_group_barrier(0x100, 1, 0); } \
    __builtin_amdgcn_sched_group_barrier(0x008, 2, 0);                                                             \
    asm volatile("" : "+v"(acc[0][0]), "+v"(acc[0][1]), "+v"(acc[0][2]), "+v"(acc[1][0]), "+v"(acc[1][1]), "+v"(acc[1][2]), \
                      "+v"(acc[2][0]), "+v"(acc[2][1]), "+v"(acc[2][2]), "+v"(acc[3][0]), "+v"(acc[3][1]), "+v"(acc[3][2]) :: "memory"); \
    __builtin_amdgcn_sched_barrier(0);
    unsigned buf = 0, nbuf = 2 * STG;
#pragma unroll 1
    for (int n = 0; n < 8; ++n) {
      BARRIER();
      STAMP(16 + n);
      QKV_RD(xA, wA, 0)
      QKV_MM(xB, wB)
      if (n < 6) qkv_issue(n + 2, nbuf);
      QKV_RD(xB, wB, 1)
      QKV_MM(xA, wA)
      NEXT_BUF(buf); NEXT_BUF(nbuf);
      if (n < 6) WAITV(2); else WAITV(0);
    }
    get_tag();
    QKV_MM(xB, wB)
    STAMP(1);
    const float qs = 0.25f * 1.4426950408889634f;
    const int rho = (l15 & 3) + 8 * ((l15 >> 2) & 1) + 4 * (l15 >> 3);
    const unsigned vo = 1024u + (unsigned)(g4 & 1) * 512u + (unsigned)((g4 >> 1) * 16 + rho) * 16u;
    const unsigned go = WS_KV + (unsigned)(hd * 64 + bi * 8 + 2 * j) * 2048u;
#pragma unroll
    for (int c = 2; c >= 0; --c)
#pragma unroll
      for (int tp = 0; tp < 2; ++tp) {
        const float sc = c ? 1.f : qs;
        const f32x4 a = acc[2 * tp][c] * sc, e = acc[2 * tp + 1][c] * sc;
        const auto r0 = __builtin_amdgcn_permlane16_swap(pk_bf16(a[0], a[1]), pk_bf16(e[0], e[1]), false, false);
        const auto r1 = __builtin_amdgcn_permlane16_swap(pk_bf16(a[2], a[3]), pk_bf16(e[2], e[3]), false, false);
        const u32x4 o = {r0[0], r1[0], r0[1], r1[1]};
        if (c == 0) *(LAS u32x4*)(sm + L_Q + (2 * j + tp) * 1024 + lane * 16) = o;
        else {
          ST128(o, (c == 1 ? lane * 16u : vo) + tp * 2048, go);
          *(LAS u32x4*)(sm + (2 * j + tp) * TILE + (c == 1 ? lane * 16u : vo + (g4 & 1) * 16u)) = o;
        }
      }
    if (lane < 4) *(LAS u32x4*)(sm + (2 * j + (lane >> 1)) * TILE + 1536 + (lane & 1) * 528) = ones;
    STAMP(10);
    BARRIER();
    WAITV(0);
    if (lane == 0) __builtin_amdgcn_raw_buffer_store_b64(tag, rws, WS_FKV + (unsigned)((hd * 8 + bi) * 4 + j) * 8u, 0, AUX_SC1);
    STAMP(11);
  } else {
    const int j = wave - 8;
    u32x2 ep = {0u, 0u};
    if (j == 0) ep = __builtin_amdgcn_raw_buffer_load_b64(rws, WS_EPOCH, 0, AUX_SC1);
    if (j == 1 && lane < 5)
      ep.x = __builtin_amdgcn_raw_buffer_load_b32(mk_rsrc((const void*)pc0, CODE_BYTES), (unsigned)(lane * 32 + idx) * 128u, 0, 0);
    auto put_tag = [&]() { if (j == 0) *(LAS u32x2*)(sm + L_FL + 256) = mk_tag(ep); else asm volatile("" :: "v"(ep)); };
    auto retile = [&](unsigned img, int ks) {
      const unsigned ta = img + (unsigned)(8 * hh + q4) * 128u + (unsigned)(4 * (g4 & 1) + 2 * (p4 & 1) + (p4 >> 1)) * 8u;
      ST128(__builtin_bit_cast(u32x4, lds_tr2(sm, ta, ta + 512)), WS_WT + lane * 16, (2 * bi * 144 + ks) * 1024);
      ST128(__builtin_bit_cast(u32x4, lds_tr2(sm, ta + 64, ta + 576)), WS_WT + lane * 16, ((2 * bi + 1) * 144 + ks) * 1024);
    };
    auto clear_o = [&]() {
      const u32x4 inv = {0xffffffffu, 0xffffffffu, 0xffffffffu, 0xffffffffu};
      const unsigned oo = WS_OF + (unsigned)((8 * bi + 2 * j) * 32 + hd) * 1024u + lane * 16u;
      ST128(inv, oo, 0); ST128(inv, oo + 32768, 0);
    };
    u32x4 kvd[3][4];
    auto kv_load = [&](int n0, int cnt) {
      glb_wait(rws, WS_FKV + (unsigned)((hd * 8 + ((bi + n0 + (lane < cnt ? lane : 0)) & 7)) * 4 + j) * 8u, tag);
#pragma unroll
      for (int u = 0; u < 3; ++u)
        if (u < cnt) {
          const unsigned go = WS_KV + (unsigned)(hd * 64 + ((bi + n0 + u) & 7) * 8 + 2 * j) * 2048u;
#pragma unroll
          for (int e = 0; e < 4; ++e) kvd[u][e] = LD128(lane * 16 + e * 1024, go);
        }
    };
    auto kv_store = [&](int n0, int cnt) {
#pragma unroll
      for (int u = 0; u < 3; ++u)
        if (u < cnt) {
          LAS char* dst = sm + ((n0 + u) * 8 + 2 * j) * TILE + lane * 16;
          *(LAS u32x4*)(dst) = kvd[u][0];
          *(LAS u32x4*)(dst + 1024 + hh * 16) = kvd[u][1];
          *(LAS u32x4*)(dst + TILE) = kvd[u][2];
          *(LAS u32x4*)(dst + TILE + 1024 + hh * 16) = kvd[u][3];
        }
      if (lane < 4 * cnt) *(LAS u32x4*)(sm + ((n0 + (lane >> 2)) * 8 + 2 * j + ((lane >> 1) & 1)) * TILE + 1536 + (lane & 1) * 528) = ones;
      WAITL0();
      if (lane < cnt) lds_add(sm, n0 + lane, 2u);
    };
    if (has_w1) {
      f32x4 acc[4][4];
      const unsigned wa = 32768u + 1536u + (unsigned)(g4 >> 1) * 3584u + (unsigned)(8 * (g4 & 1) + q4) * 128u +
                          (unsigned)((q4 >> 1) | ((g4 & 1) << 1)) * 32u + p4 * 8u;
      bf16x8 xA[4], wA[4], xB[4], wB[4];
#pragma unroll
      for (int t = 0; t < 4; ++t) { xB[t] = __builtin_bit_cast(bf16x8, (u32x4){0u, 0u, 0u, 0u}); wB[t] = xB[t]; }
#define W1_RD(X, W, kk)                                                                                            \
      _Pragma("unroll") for (int c = 0; c < 4; ++c) W[c] = lds_tr2(sm, buf + (wa ^ (unsigned)(c * 32)) + kk * 7168, buf + (wa ^ (unsigned)(c * 32)) + kk * 7168 + 512); \
      _Pragma("unroll") for (int t = 0; t < 4; ++t) X[t] = lds_frag(sm, buf + (kk ? xa1 : xa0) + t * 2048);
#define W1_MM(X, W)                                                                                                \
      _Pragma("unroll") for (int t = 0; t < 4; ++t)                                                                \
        _Pragma("unroll") for (int c = 0; c < 4; ++c) acc[t][c] = __builtin_amdgcn_mfma_f32_16x16x32_bf16(W[c], X[t], acc[t][c], 0, 0, 0); \
      _Pragma("unroll") for (int u = 0; u < 12; ++u) { __builtin_amdgcn_sched_group_barrier(0x008, 1, 0); __builtin_amdgcn_sched_group_barrier(0x100, 1, 0); } \
      __builtin_amdgcn_sched_group_barrier(0x008, 4, 0);                                                           \
      asm volatile("" : "+v"(acc[0][0]), "+v"(acc[0][1]), "+v"(acc[0][2]), "+v"(acc[0][3]), "+v"(acc[1][0]), "+v"(acc[1][1]), "+v"(acc[1][2]), "+v"(acc[1][3]), \
                        "+v"(acc[2][0]), "+v"(acc[2][1]), "+v"(acc[2][2]), "+v"(acc[2][3]), "+v"(acc[3][0]), "+v"(acc[3][1]), "+v"(acc[3][2]), "+v"(acc[3][3]) :: "memory"); \
      __builtin_amdgcn_sched_barrier(0);
      unsigned buf = 0;
      BARRIER();
#pragma unroll
      for (int c = 0; c < 4; ++c) {
        const u32x2 bv = *(LAS u32x2*)(sm + L_B1 + (16 * c + 4 * g4) * 2);
#pragma unroll
        for (int t = 0; t < 4; ++t) acc[t][c] = (f32x4){bf_lo(bv.x), bf_hi(bv.x), bf_lo(bv.y), bf_hi(bv.y)};
      }
#pragma unroll 1
      for (int n = 0; n < 8; ++n) {
        W1_RD(xA, wA, 0)
        W1_MM(xB, wB)
        W1_RD(xB, wB, 1)
        W1_MM(xA, wA)
        NEXT_BUF(buf);
        if (n == 2) put_tag();
        if (n < 7) BARRIER();
      }
      BARRIER();
      __builtin_amdgcn_s_setprio(3);
      get_tag();
      W1_MM(xB, wB)
      STAMP(1);
      const unsigned ao = WS_ACT + (unsigned)(8 * bi + 2 * j) * 114688u + (unsigned)cw * 4096u + (unsigned)g4 * 512u + l15 * 16u;
      auto mish2 = [&](int t0) {
#pragma unroll
        for (int t = t0; t < t0 + 2; ++t)
#pragma unroll
          for (int pc = 0; pc < 2; ++pc) {
            u32x4 o;
#pragma unroll
            for (int e = 0; e < 4; ++e) {
              f32x2 hv = {acc[t][2 * pc + (e >> 1)][2 * (e & 1)], acc[t][2 * pc + (e >> 1)][2 * (e & 1) + 1]};
              f32x2 a = hv * 1.4426950408889634f;
              f32x2 ex = {__builtin_amdgcn_exp2f(a.x), __builtin_amdgcn_exp2f(a.y)};
              f32x2 d = ex * (ex + 2.f) + 2.f;
              f32x2 r = {__builtin_amdgcn_rcpf(d.x), __builtin_amdgcn_rcpf(d.y)};
              f32x2 y = hv * (r * -2.f + 1.f);
              o[e] = pk_bf16(y.x, y.y);
            }
            ST128(o, ao + (t >> 1) * 114688 + pc * 2048 + (t & 1) * 256, 0);
          }
      };
      mish2(0);
      kv_load(1, 1);
      mish2(2);
      STAMP(2);
      kv_store(1, 1);
      clear_o();
      retile(L_RT + j * 2048u, 4 * cw + j);
      WAITV(0);
      if (lane == 0) __builtin_amdgcn_raw_buffer_store_b64(tag, rws, WS_FACT + (unsigned)((bi * 4 + j) * 32 + hd) * 8u, 0, AUX_SC1);
    } else {
      f32x4 acc[4];
#pragma unroll
      for (int t = 0; t < 4; ++t) acc[t] = (f32x4){0.f, 0.f, 0.f, 0.f};
      unsigned buf = 0;
#pragma unroll 1
      for (int n = 0; n < 8; ++n) {
        BARRIER();
        if (hd == 7) {
#pragma unroll
          for (int kk = 0; kk < 2; ++kk) {
            const bf16x8 gf = lds_frag(sm, L_G + ((n + rot) & 7) * 128 + kk * 64 + g4 * 16);
#pragma unroll
            for (int t = 0; t < 4; ++t) acc[t] = __builtin_amdgcn_mfma_f32_16x16x32_bf16(gf, lds_frag(sm, buf + (kk ? xa1 : xa0) + t * 2048), acc[t], 0, 0, 0);
          }
        }
        if (n == 2) { put_tag(); clear_o(); }
        if (n == 5 || n == 6) retile(n == 5 ? L_RT + j * 2048u : 2 * STG + 32768u + 1536u + j * 3584u, 112 + (hd >> 3) * 8 + 2 * j + (n - 5));
        NEXT_BUF(buf);
      }
      WAITV(0);
      get_tag();
      BARRIER();
      __builtin_amdgcn_s_setprio(3);
      STAMP(1);
      if (hd == 7) {
        const float lg = g4 == 0 ? acc[0][0] : g4 == 1 ? acc[1][0] : g4 == 2 ? acc[2][0] : acc[3][0];
        const float gv = __builtin_amdgcn_rcpf(1.f + __builtin_amdgcn_exp2f(lg * -1.4426950408889634f));
        __builtin_amdgcn_raw_buffer_store_b32(__float_as_uint(gv), rws, WS_G + (unsigned)(256 * bi + 64 * j + lane) * 4u, 0, AUX_SC1);
      }
      WAITV(0);
      if (lane == 0) __builtin_amdgcn_raw_buffer_store_b64(tag, rws, WS_FACT + (unsigned)((bi * 4 + j) * 32 + hd) * 8u, 0, AUX_SC1);
      kv_load(1, 1);
      STAMP(2);
      kv_store(1, 1);
    }
#pragma unroll 1
    for (int n0 = 2; n0 < 8; n0 += 3) {
      kv_load(n0, 3);
      kv_store(n0, 3);
    }
    STAMP(3);
    const int tm = hd >> 3, cp = hd & 7;
    if (j == 0) {
      glb_wait(rws, !hh ? WS_FACT + (unsigned)((4 * bi + tm) * 32 + l31) * 8u : WS_FACT + (unsigned)((4 * bi + p4) * 32 + 7) * 8u, tag);
#pragma unroll 1
      for (int u = 0; u < 2; ++u) glb_wait(rws, WS_FACT + (unsigned)(cp * 128 + u * 64 + lane) * 8u, tag);
      if (lane == 0) lds_add(sm, 18, 1u);
    } else lds_wait(sm, 18, 1u);
    STAMP(4);
    {
      *(LAS unsigned*)(sm + L_G + (64 * j + lane) * 4) = __builtin_amdgcn_raw_buffer_load_b32(rws, WS_G + (unsigned)(256 * bi + 64 * j + lane) * 4u, 0, AUX_SC1);
      f32x16 acc[2][2];
#pragma unroll
      for (int t = 0; t < 2; ++t)
#pragma unroll
        for (int c = 0; c < 2; ++c)
#pragma unroll
          for (int r = 0; r < 16; ++r) acc[t][c][r] = 0.f;
      const unsigned ab = WS_ACT + (unsigned)(8 * bi + 2 * tm + (j & 1)) * 114688u + (unsigned)(28 * j) * 1024u;
      const unsigned ab1 = WS_ACT + (unsigned)(8 * bi + 2 * tm + ((j & 1) ^ 1)) * 114688u + (unsigned)(28 * j) * 1024u;
      const unsigned wb = WS_WT + (unsigned)((2 * cp + (j >> 1)) * 144 + 28 * j) * 1024u;
      const unsigned wb1 = WS_WT + (unsigned)((2 * cp + ((j >> 1) ^ 1)) * 144 + 28 * j) * 1024u;
      u32x4 af[4][2], wf[4][2];
#pragma unroll
      for (int u = 0; u < 4; ++u) {
        af[u][0] = LD128(lane * 16 + u * 1024, ab); af[u][1] = LD128(lane * 16 + u * 1024, ab1);
        wf[u][0] = LD128(lane * 16 + u * 1024, wb); wf[u][1] = LD128(lane * 16 + u * 1024, wb1);
      }
      WAITL0();
      if (lane == 0) lds_add(sm, 17, 1u);
#pragma unroll 1
      for (int it = 1; it <= W2_N; ++it) {
        const unsigned nx = (it < 7) ? it * 4096u : 0u;
        const rsrc_t rws = mk_rsrc(ws, it < 7 ? WS_END : 0u);
#pragma unroll
        for (int u = 0; u < 4; ++u) {
#pragma unroll
          for (int t = 0; t < 2; ++t)
#pragma unroll
            for (int c = 0; c < 2; ++c)
              acc[t][c] = __builtin_amdgcn_mfma_f32_32x32x16_bf16(__builtin_bit_cast(bf16x8, wf[u][c]), __builtin_bit_cast(bf16x8, af[u][t]), acc[t][c], 0, 0, 0);
          af[u][0] = LD128(lane * 16 + u * 1024, ab + nx); af[u][1] = LD128(lane * 16 + u * 1024, ab1 + nx);
          wf[u][0] = LD128(lane * 16 + u * 1024, wb + nx); wf[u][1] = LD128(lane * 16 + u * 1024, wb1 + nx);
          __builtin_amdgcn_sched_barrier(0);
        }
      }
      lds_wait(sm, 17, 4u);
#pragma unroll
      for (int q = 0; q < 4; ++q) {
        LAS f32x4* bp = (LAS f32x4*)(sm + L_BP + (unsigned)(q ^ j) * 4096u + lane * 16);
        const f32x16& av = acc[q & 1][q >> 1];
        f32x4 v[4];
#pragma unroll
        for (int u = 0; u < 4; ++u) v[u] = (f32x4){av[4 * u], av[4 * u + 1], av[4 * u + 2], av[4 * u + 3]};
        if (q > 0) {
          lds_wait(sm, 32, 4u * q);
#pragma unroll
          for (int u = 0; u < 4; ++u) v[u] += bp[u * 64];
        }
#pragma unroll
        for (int u = 0; u < 4; ++u) bp[u * 64] = v[u];
        WAITL0();
        if (q < 3 && lane == 0) lds_add(sm, 32, 1u);
      }
    }
    STAMP(5);
    if (b == 0 && j == 0) {
#pragma unroll 1
      for (int u = 0; u < 4; ++u) glb_wait(rws, WS_FACT + (unsigned)((2 * u + hh) * 128 + l31) * 8u, tag);
      if (lane == 0) __builtin_amdgcn_raw_buffer_store_b64(tag, rws, WS_EPOCH, 0, AUX_SC1);
    }
    return;
  }

  STAMP(2);
  const int w = wave;
  const unsigned ka = lane * 16;
  const unsigned va = 1024u + (l31 < 16 ? (unsigned)(hh * 16 + l31) * 16u : ((l31 >= 20 && l31 < 28) ? (unsigned)hh * 256u : 512u));
  const int IDX = 0xDDDD;
  const bf16x8 qf = lds_frag(sm, L_Q + w * 1024 + lane * 16);
  f32x16 negm, O;
  {
    f32x16 z;
#pragma unroll
    for (int r = 0; r < 16; ++r) { z[r] = 0.f; O[r] = 0.f; }
    f32x16 s0 = __builtin_amdgcn_mfma_f32_32x32x16_bf16(lds_frag(sm, ka), qf, z, 0, 0, 0);
    float m = s0[0];
#pragma unroll
    for (int r = 1; r < 16; ++r) m = fmaxf(m, s0[r]);
    m = fmaxf(m, __shfl_xor(m, 32));
#pragma unroll
    for (int r = 0; r < 16; ++r) negm[r] = -m;
  }
  STAMP(7);
  const int tm = hd >> 3, cp = hd & 7;
  const int tt = w & 1, kq = w >> 1;
  u32x4 wf[2][8];
  {
    u32x4 kf, v0, v1;
    unsigned kb = sb + ka, vb = sb + va, fb = sb + L_FL + 4, ft;
    int cnt = SPLIT_AT, st, sp;
    if (w < 4) __builtin_amdgcn_s_setprio(2); else __builtin_amdgcn_s_setprio(1);
    asm volatile(ATTN_LOOP
                 : "+v"(O), "=&v"(kf), "=&v"(v0), "=&v"(v1), "+v"(kb), "+v"(vb), "+s"(cnt), "+v"(fb), "=&v"(ft), "=&s"(st), "=&s"(sp)
                 : "v"(qf), "v"(negm), "v"(IDX)
                 : ATTN_CLOBS);
    STAMP(12);
    if (w < 4) __builtin_amdgcn_s_setprio(0);
    lds_wait(sm, 17, 4u);
    const unsigned wb = WS_WT + (unsigned)(2 * cp * 144 + 112 + 8 * kq) * 1024u;
#pragma unroll
    for (int u = 0; u < 8; ++u) {
      wf[0][u] = LD128(lane * 16 + (u & 3) * 1024, wb + (u >> 2) * 4096);
      wf[1][u] = LD128(lane * 16 + (u & 3) * 1024, wb + 147456 + (u >> 2) * 4096);
    }
    STAMP(13);
    cnt = 8 - SPLIT_AT;
    asm volatile(ATTN_LOOP
                 : "+v"(O), "=&v"(kf), "=&v"(v0), "=&v"(v1), "+v"(kb), "+v"(vb), "+s"(cnt), "+v"(fb), "=&v"(ft), "=&s"(st), "=&s"(sp)
                 : "v"(qf), "v"(negm), "v"(IDX)
                 : ATTN_CLOBS);
  }
  STAMP(3);
  float lsum = hh ? O[12] : O[8];
  {
    float chk = lsum * 0.f;
#pragma unroll
    for (int r = 0; r < 8; ++r) chk = __builtin_fmaf(O[r], 0.f, chk);
    if (__any(chk != chk)) {
      float m = -3.0e38f;
      f32x16 z;
#pragma unroll
      for (int r = 0; r < 16; ++r) { z[r] = 0.f; O[r] = 0.f; }
#pragma unroll 1
      for (int t = 0; t < 64; ++t) {
        f32x16 S = __builtin_amdgcn_mfma_f32_32x32x16_bf16(lds_frag(sm, ka + t * TILE), qf, z, 0, 0, 0);
        float mx = m;
#pragma unroll
        for (int r = 0; r < 16; ++r) mx = fmaxf(mx, S[r]);
        mx = fmaxf(mx, __shfl_xor(mx, 32));
        const float al = __builtin_amdgcn_exp2f(m - mx);
        m = mx;
        f32x8 p0, p1;
#pragma unroll
        for (int r = 0; r < 16; ++r) O[r] *= al;
#pragma unroll
        for (int r = 0; r < 8; ++r) { p0[r] = __builtin_amdgcn_exp2f(S[r] - m); p1[r] = __builtin_amdgcn_exp2f(S[8 + r] - m); }
        O = __builtin_amdgcn_smfmac_f32_32x32x32_bf16(lds_frag(sm, va + t * TILE), __builtin_bit_cast(bf16x16, p0), O, IDX, 0, 0);
        O = __builtin_amdgcn_smfmac_f32_32x32x32_bf16(lds_frag(sm, va + t * TILE + 528), __builtin_bit_cast(bf16x16, p1), O, IDX, 0, 0);
      }
      lsum = hh ? O[12] : O[8];
    }
  }
  {
    const float sc = *(LAS float*)(sm + L_G + (32 * w + l31) * 4) * __builtin_amdgcn_rcpf(lsum);
    u32x4 o;
#pragma unroll
    for (int e = 0; e < 4; ++e) {
      unsigned u = pk_bf16(O[2 * e] * sc, O[2 * e + 1] * sc);
      o[e] = (u == 0xffffffffu) ? 0x7fc07fc0u : u;
    }
    ST128(o, WS_OF + (unsigned)((8 * bi + w) * 32 + hd) * 1024u + lane * 16u, 0);
  }
  {
    STAMP(4);
    BARRIER();
    u32x4 of[8];
    const unsigned ob = WS_OF + (unsigned)((8 * bi + 2 * tm + tt) * 32 + 8 * kq) * 1024u;
#pragma unroll
    for (int u = 0; u < 8; ++u) of[u] = LD128(lane * 16 + (u & 3) * 1024, ob + (u >> 2) * 4096);
    const float om = 1.f - *(LAS float*)(sm + L_G + (64 * tm + 32 * tt + l31) * 4);
    f32x4 a, c;
    {
      LAS f32x4* bp = (LAS f32x4*)(sm + L_BP + ((tt + 2 * (kq >> 1)) * 4 + 2 * (kq & 1)) * 1024 + lane * 16);
      a = bp[0] * om; c = bp[64] * om;
    }
    f32x16 acc[2];
#pragma unroll
    for (int r = 0; r < 16; ++r) { acc[0][r] = 0.f; acc[1][r] = 0.f; }
    unsigned todo = 0xffu, next = 0;
#pragma unroll 1
    for (unsigned s = 0; s < SPIN_CAP; ++s) {
#pragma unroll
      for (int u = 0; u < 8; ++u)
        if (todo & (1u << u)) {
          const unsigned mx = max(max(of[u][0], of[u][1]), max(of[u][2], of[u][3]));
          if (__all(mx != 0xffffffffu) || s == SPIN_CAP - 1) todo &= ~(1u << u);
        }
      CFENCE();
#pragma unroll
      for (int u = 0; u < 8; ++u)
        if (todo & (1u << u)) of[u] = LD128(lane * 16 + (u & 3) * 1024, ob + (u >> 2) * 4096);
#pragma unroll
      for (int u = 0; u < 8; ++u)
        if (next == (unsigned)u && !(todo & (1u << u))) {
          asm volatile("v_mfma_f32_32x32x16_bf16 %0, %2, %4, %0\n\tv_mfma_f32_32x32x16_bf16 %1, %3, %4, %1"
                       : "+v"(acc[0]), "+v"(acc[1]) : "v"(wf[0][u]), "v"(wf[1][u]), "v"(of[u]));
          next = u + 1;
        }
      if (next == 8) break;
    }
    asm volatile("s_nop 15" : "+v"(acc[0]), "+v"(acc[1]));
    STAMP(5);
#pragma unroll
    for (int p = 0; p < 4; ++p) {
      const f32x4 lo = {acc[p >> 1][8 * (p & 1)], acc[p >> 1][8 * (p & 1) + 1], acc[p >> 1][8 * (p & 1) + 2], acc[p >> 1][8 * (p & 1) + 3]};
      const f32x4 hi = {acc[p >> 1][8 * (p & 1) + 4], acc[p >> 1][8 * (p & 1) + 5], acc[p >> 1][8 * (p & 1) + 6], acc[p >> 1][8 * (p & 1) + 7]};
      if (p != kq) {
        LAS f32x4* d = (LAS f32x4*)(sm + ((tt * 4 + p) * 4 + kq) * 2048 + lane * 16);
        d[0] = lo; d[64] = hi;
      } else { a += lo; c += hi; }
    }
    BARRIER();
#pragma unroll
    for (int q = 1; q < 4; ++q) {
      LAS f32x4* d = (LAS f32x4*)(sm + ((tt * 4 + kq) * 4 + ((kq + q) & 3)) * 2048 + lane * 16);
      a += d[0]; c += d[64];
    }
    u32x4 o = {pk_bf16(a[0], a[1]), pk_bf16(a[2], a[3]), pk_bf16(c[0], c[1]), pk_bf16(c[2], c[3])};
    const rsrc_t ro = mk_rsrc(out, 2048u * 1024u);
    __builtin_amdgcn_raw_buffer_store_b128(o, ro, (unsigned)(256 * bi + 64 * tm + 32 * tt + l31) * 1024u + (unsigned)(64 * cp + 16 * kq + 8 * hh) * 2u, 0, OUT_AUX);
  }
  STAMP(6);
  STAMP_OUT();
}

extern "C" void kernel_launch(void* const* d_in, const int* in_sizes, int n_in, void* d_out, int out_size,
                              void* d_ws, size_t ws_size, hipStream_t stream) {
  fused_kernel<<<256, 768, 0, stream>>>((const u16*)d_in[0], (const u16*)d_in[1], (const u16*)d_in[2], (const u16*)d_in[4],
                                        (const u16*)d_in[5], (const u16*)d_in[6], (const u16*)d_in[7], (u16*)d_out,
                                        (unsigned char*)d_ws);
}
```

```cpp
#define ROTEXPR (((hd + bi) & 1) * 4)
#include <hip/hip_runtime.h>
#include <hip/hip_bf16.h>

typedef unsigned short u16;
typedef unsigned long long u64;
typedef __bf16 bf16x8 __attribute__((ext_vector_type(8)));
typedef __bf16 bf16x16 __attribute__((ext_vector_type(16)));
typedef __bf16 bf16x2 __attribute__((ext_vector_type(2)));
typedef short s16x4 __attribute__((ext_vector_type(4)));
typedef float f32x2 __attribute__((ext_vector_type(2)));
typedef float f32x4 __attribute__((ext_vector_type(4)));
typedef float f32x8 __attribute__((ext_vector_type(8)));
typedef float f32x16 __attribute__((ext_vector_type(16)));
typedef unsigned u32x2 __attribute__((ext_vector_type(2)));
typedef unsigned u32x4 __attribute__((ext_vector_type(4)));
#define LAS __attribute__((address_space(3)))
#define GAS __attribute__((address_space(1)))
typedef __amdgpu_buffer_rsrc_t rsrc_t;

constexpr unsigned WS_EPOCH = 0;
constexpr unsigned WS_FKV = 4096;
constexpr unsigned WS_FACT = WS_FKV + 8192;
constexpr unsigned WS_G = 32768;
constexpr unsigned WS_KV = 65536;
constexpr unsigned WS_ACT = WS_KV + 32u * 131072u;
constexpr unsigned WS_WT = WS_ACT + 64u * 114688u;
constexpr unsigned WS_OF = WS_WT + 16u * 144u * 1024u;
constexpr unsigned WS_END = WS_OF + 64u * 32u * 1024u;
constexpr unsigned WS_DIAG = WS_END;

constexpr unsigned STG = 47104;
constexpr unsigned TILE = 2080;
constexpr unsigned L_RT = 3 * STG;
constexpr unsigned L_BP = 64 * TILE;
constexpr unsigned L_Q = L_BP + 16384;
constexpr unsigned L_B1 = L_Q + 8192;
constexpr unsigned L_G = 158720;
constexpr unsigned L_FL = L_G + 1024;
constexpr unsigned L_END = L_FL + 512;

constexpr unsigned SPIN_CAP = 1u << 15;
constexpr int AUX_SC1 = 16;
#define POLL_SLEEP 4

__device__ __forceinline__ unsigned pk_bf16(float a, float b) {
  f32x2 v = {a, b};
  return __builtin_bit_cast(unsigned, __builtin_convertvector(v, bf16x2));
}
__device__ __forceinline__ float bf_lo(unsigned u) { return __uint_as_float(u << 16); }
__device__ __forceinline__ float bf_hi(unsigned u) { return __uint_as_float(u & 0xffff0000u); }
__device__ __forceinline__ rsrc_t mk_rsrc(const void* p, unsigned bytes) {
  return __builtin_amdgcn_make_buffer_rsrc((void*)p, 0, bytes, 0x00020000);
}
__device__ __forceinline__ u32x4 mk_desc(const void* p, unsigned bytes) {
  const u64 a = (u64)p;
  return (u32x4){(unsigned)a, (unsigned)(a >> 32) & 0xffffu, bytes, 0x00020000u};
}
__device__ __forceinline__ void dma16(u32x4 desc, unsigned lds_addr, unsigned voff, unsigned soff) {
  asm volatile("s_mov_b32 m0, %0\n\ts_nop 3\n\tbuffer_load_dwordx4 %1, %2, %3 offen lds"
               :: "s"(lds_addr), "v"(voff), "s"(desc), "s"(soff) : "memory");
}
template <int OFF>
__device__ __forceinline__ void dma16_next(u32x4 desc, unsigned voff, unsigned soff) {
  asm volatile("buffer_load_dwordx4 %0, %1, %2 offen offset:%3 lds" :: "v"(voff), "s"(desc), "s"(soff), "i"(OFF) : "memory");
}
#define BARRIER() asm volatile("s_waitcnt lgkmcnt(0)\n\ts_barrier" ::: "memory")
#define WAITV(n) asm volatile("s_waitcnt vmcnt(" #n ")" ::: "memory")
#define WAITL0() asm volatile("s_waitcnt lgkmcnt(0)" ::: "memory")
#define CFENCE() asm volatile("" ::: "memory")
#define LD128(off, soff) __builtin_amdgcn_raw_buffer_load_b128(rws, off, soff, AUX_SC1)
#define ST128(v, off, soff) __builtin_amdgcn_raw_buffer_store_b128(v, rws, off, soff, AUX_SC1)

#define STAMP(k) do {} while (0)
#define STAMP_OUT() do {} while (0)
#define ACC_BEG() do {} while (0)
#define ACC_END(a) do {} while (0)

#define STAMP2(k) do {} while (0)
__device__ __forceinline__ bf16x8 lds_frag(LAS char* sm, unsigned off) { return *(LAS bf16x8*)(sm + off); }
__device__ __forceinline__ bf16x8 lds_tr2(LAS char* sm, unsigned o0, unsigned o1) {
  s16x4 a = __builtin_amdgcn_ds_read_tr16_b64_v4i16((LAS s16x4*)(sm + o0));
  s16x4 b = __builtin_amdgcn_ds_read_tr16_b64_v4i16((LAS s16x4*)(sm + o1));
  return __builtin_bit_cast(bf16x8, __builtin_shufflevector(a, b, 0, 1, 2, 3, 4, 5, 6, 7));
}
__device__ __forceinline__ void lds_add(LAS char* sm, unsigned idx, unsigned v) {
  __hip_atomic_fetch_add((LAS unsigned*)(sm + L_FL) + idx, v, __ATOMIC_RELAXED, __HIP_MEMORY_SCOPE_WORKGROUP);
}
__device__ __forceinline__ void lds_wait(LAS char* sm, unsigned idx, unsigned want) {
#pragma unroll 1
  for (unsigned s = 0; s < SPIN_CAP * 4; ++s) {
    CFENCE();
    unsigned v = __hip_atomic_load((LAS unsigned*)(sm + L_FL) + idx, __ATOMIC_RELAXED, __HIP_MEMORY_SCOPE_WORKGROUP);
    if ((unsigned)__builtin_amdgcn_readfirstlane(v) >= want) break;
    __builtin_amdgcn_s_sleep(1);
  }
  CFENCE();
}
__device__ __forceinline__ void glb_wait(rsrc_t rs, unsigned off, u32x2 tag) {
#pragma unroll 1
  for (unsigned s = 0; s < SPIN_CAP; ++s) {
    CFENCE();
    u32x2 v = __builtin_amdgcn_raw_buffer_load_b64(rs, off, 0, AUX_SC1);
    if (__all(v.x == tag.x && v.y == tag.y)) break;
    __builtin_amdgcn_s_sleep(POLL_SLEEP);
  }
  CFENCE();
}

#define AE(b, i) "v_exp_f32 v[" #b "+" #i "], v[" #b "+" #i "]\n"
#define ATILE(cur, oth, t)                                                                                          \
  AE(cur, 0) AE(cur, 1)                                                                                              \
  "s_waitcnt lgkmcnt(2)\n v_smfmac_f32_32x32x32_bf16 %0, %3, v[" #oth "+8:" #oth "+15], %13\n"                       \
  "ds_read_b128 %3, %5 offset:2080*" #t "+528\n"                                                                     \
  AE(cur, 2) AE(cur, 3) AE(cur, 4) AE(cur, 5)                                                                        \
  "s_waitcnt lgkmcnt(2)\n v_mfma_f32_32x32x16_bf16 v[" #oth ":" #oth "+15], %1, %11, %12\n"                          \
  "ds_read_b128 %1, %4 offset:2080*(" #t "+2)\n"                                                                     \
  AE(cur, 6) AE(cur, 7) AE(cur, 8) AE(cur, 9) AE(cur, 10)                                                            \
  "s_waitcnt lgkmcnt(2)\n v_smfmac_f32_32x32x32_bf16 %0, %2, v[" #cur ":" #cur "+7], %13\n"                          \
  "ds_read_b128 %2, %5 offset:2080*(" #t "+1)\n"                                                                     \
  AE(cur, 11) AE(cur, 12) AE(cur, 13) AE(cur, 14) AE(cur, 15)
#define ATTN_LOOP                                                                                                   \
  "ds_read_b128 %1, %4\n ds_read_b128 %3, %5\n"                                                                      \
  "v_mov_b32 v120, 0\n v_mov_b32 v121, 0\n v_mov_b32 v122, 0\n v_mov_b32 v123, 0\n"                                  \
  "v_mov_b32 v124, 0\n v_mov_b32 v125, 0\n v_mov_b32 v126, 0\n v_mov_b32 v127, 0\n"                                  \
  "s_waitcnt lgkmcnt(0)\n v_mfma_f32_32x32x16_bf16 v[96:111], %1, %11, %12\n"                                        \
  "ds_read_b128 %1, %4 offset:2080\n ds_read_b128 %2, %5\n s_nop 11\n"                                               \
  "1:\n"                                                                                                             \
  ATILE(96, 112, 0) ATILE(112, 96, 1) ATILE(96, 112, 2) ATILE(112, 96, 3) ATILE(96, 112, 4)                          \
  "ds_read_b32 %8, %7\n"                                                                                             \
  ATILE(112, 96, 5)                                                                                                  \
  "v_readfirstlane_b32 %9, %8\n s_nop 3\n s_cmp_eq_u32 %9, 8\n s_cbranch_scc1 3f\n s_mov_b32 %10, 0x20000\n"         \
  "2:\n s_sleep 1\n ds_read_b32 %8, %7\n s_waitcnt lgkmcnt(0)\n v_readfirstlane_b32 %9, %8\n s_sub_u32 %10, %10, 1\n" \
  "s_cmp_eq_u32 %10, 0\n s_cbranch_scc1 3f\n s_cmp_eq_u32 %9, 8\n s_cbranch_scc0 2b\n"                               \
  "3:\n"                                                                                                             \
  ATILE(96, 112, 6) ATILE(112, 96, 7)                                                                                \
  "v_add_u32 %4, 8*2080, %4\n v_add_u32 %5, 8*2080, %5\n v_add_u32 %7, 4, %7\n s_sub_u32 %6, %6, 1\n"                \
  "s_cmp_lg_u32 %6, 0\n s_cbranch_scc1 1b\n"                                                                         \
  "s_waitcnt lgkmcnt(0)\n v_smfmac_f32_32x32x32_bf16 %0, %3, v[120:127], %13\n s_nop 15\n"
#define ATTN_CLOBS "v96","v97","v98","v99","v100","v101","v102","v103","v104","v105","v106","v107","v108","v109","v110","v111", \
  "v112","v113","v114","v115","v116","v117","v118","v119","v120","v121","v122","v123","v124","v125","v126","v127","scc","memory"
#define CODE_BYTES 17408u
#define W2_N 7
#define OUT_AUX 0
#define SPLIT_AT 6

__global__ void __launch_bounds__(768, 3)
fused_kernel(const u16* __restrict__ x, const u16* __restrict__ wqkv, const u16* __restrict__ wo,
             const u16* __restrict__ w1, const u16* __restrict__ b1, const u16* __restrict__ w2,
             const u16* __restrict__ gw, u16* __restrict__ out, unsigned char* __restrict__ ws) {
  __shared__ __attribute__((aligned(16))) char smem_[L_END];
  LAS char* sm = (LAS char*)smem_;
  const unsigned sb = (unsigned)(size_t)sm;
  u64 pc0;
  asm volatile("s_getpc_b64 %0" : "=s"(pc0));
  pc0 &= ~255ull;
  asm volatile("" :: "s"(x), "s"(wqkv), "s"(wo), "s"(w1), "s"(b1), "s"(w2), "s"(gw), "s"(out), "s"(ws));
  const int lane = threadIdx.x & 63;
  const int wave = __builtin_amdgcn_readfirstlane(threadIdx.x >> 6);
  const int b = blockIdx.x, xcd = b & 7, idx = b >> 3;
  const int bi = 4 * (xcd & 1) + (idx & 3), hd = 8 * (xcd >> 1) + (idx >> 2);
  const bool has_w1 = (hd & 7) != 7;
  const int cw = hd - (hd >> 3);
  const int rot = ROTEXPR;
  const int hh = lane >> 5, l31 = lane & 31, l15 = lane & 15, g4 = lane >> 4;
  const int q4 = l15 >> 2, p4 = lane & 3, r8 = lane >> 3, sl8 = lane & 7;
  const rsrc_t rws = mk_rsrc(ws, WS_END);
  const u32x4 ones = {0x3f803f80u, 0x3f803f80u, 0x3f803f80u, 0x3f803f80u};
  STAMP(0);

  auto mk_tag = [&](u32x2 e) {
    u64 z = ((u64)(unsigned)__builtin_amdgcn_readfirstlane(e.y) << 32 | (u64)(unsigned)__builtin_amdgcn_readfirstlane(e.x)) + 0x9E3779B97F4A7C15ull;
    z = (z ^ (z >> 30)) * 0xBF58476D1CE4E5B9ull;
    z = (z ^ (z >> 27)) * 0x94D049BB133111EBull;
    z ^= z >> 31;
    return (u32x2){(unsigned)z, (unsigned)(z >> 32)};
  };
  u32x2 tag;
  auto get_tag = [&]() {
    const u32x2 t = *(LAS u32x2*)(sm + L_FL + 256);
    tag = (u32x2){(unsigned)__builtin_amdgcn_readfirstlane(t.x), (unsigned)__builtin_amdgcn_readfirstlane(t.y)};
  };
  const unsigned xa0 = (unsigned)(64 * (wave & 3) + l15) * 128u + (unsigned)(g4 ^ (l15 >> 1)) * 16u;
  const unsigned xa1 = xa0 ^ 64u;
#define NEXT_BUF(v) v = (v == 2 * STG) ? 0 : v + STG
  const unsigned qo = (unsigned)(16 * (wave & 3) + (((lane >> 1) & 15) ^ ((g4 & 1) << 2))) * 3072u + (unsigned)hh * 1024u + (unsigned)hd * 32u + (lane & 1) * 16u;
  auto qkv_issue = [&](int n, unsigned buf) {
    const u32x4 d = mk_desc(wqkv, 512u * 3072u);
    const unsigned so = ((unsigned)(n + rot) & 7u) * (64u * 3072u);
    dma16(d, sb + buf + 32768u + (wave & 3) * 3584u, qo, so);
    if (lane < 32) dma16_next<1024>(d, qo, so + 1024u);
  };

  if (__builtin_expect(wave >= 4 && wave < 8, 1)) {
    const int lw = wave - 4;
    const u32x4 dx = mk_desc(x, 2048u * 1024u);
    const unsigned xo0 = (unsigned)(256 * bi + 64 * lw + r8) * 1024u + (unsigned)(sl8 ^ (r8 >> 1)) * 16u;
    const unsigned xo1 = (xo0 ^ 64u) + 8192u - 1024u;
    const u32x4 d1 = mk_desc(w1, 512u * 3584u);
    const unsigned wo0 = (unsigned)(16 * lw + r8) * 3584u + (unsigned)cw * 128u + (unsigned)(sl8 ^ (r8 & 2)) * 16u;
    const unsigned wo1 = (wo0 ^ 64u) + 8u * 3584u - 1024u;
    unsigned buf = 0;
    STAMP(16);
#pragma unroll 1
    for (int n = 0; n < 8; ++n) {
      const unsigned ks = (unsigned)(n + rot) & 7u, d = sb + buf + lw * 8192u;
      if (n < 2) qkv_issue(n, buf);
#pragma unroll
      for (int u = 0; u < 2; ++u) {
        dma16(dx, d + u * 4096u, xo0, ks * 128u + u * 32768u);
        dma16_next<1024>(dx, xo1, ks * 128u + u * 32768u);
        dma16_next<2048>(dx, xo0, ks * 128u + u * 32768u + 16384u - 2048u);
        dma16_next<3072>(dx, xo1, ks * 128u + u * 32768u + 16384u - 2048u);
      }
      if (has_w1) {
        dma16(d1, sb + buf + 32768u + 1536u + lw * 3584u, wo0, ks * (64u * 3584u));
        dma16_next<1024>(d1, wo1, ks * (64u * 3584u));
      }
      if (n == 0 && lw == 0) {
        if (has_w1) { if (lane < 8) dma16(mk_desc(b1, 3584u), sb + L_B1, lane * 16u + (unsigned)cw * 128u, 0u); }
        else dma16(mk_desc(gw, 1024u), sb + L_G, lane * 16u, 0u);
      }
      if (n == (has_w1 ? 7 : 3)) {
        if (has_w1) {
          const u32x4 d2 = mk_desc(w2, 1792u * 1024u);
          const unsigned o2 = (unsigned)(16 * (r8 >> 2) + (r8 & 3)) * 1024u + (unsigned)bi * 128u + (unsigned)sl8 * 16u;
          const unsigned so = (unsigned)(64 * cw + 32 * (lw >> 1) + 8 * (lw & 1)) * 1024u;
          dma16(d2, sb + L_RT + lw * 2048u, o2, so); dma16_next<1024>(d2, o2, so + 3072u);
        } else {
          const u32x4 dO = mk_desc(wo, 512u * 1024u);
          const unsigned oo = (unsigned)r8 * 1024u + (unsigned)bi * 128u + (unsigned)sl8 * 16u;
          const unsigned so = (unsigned)((hd >> 3) * 8 + 2 * lw) * 16384u;
          dma16(dO, sb + L_RT + lw * 2048u, oo, so); dma16_next<1024>(dO, oo, so + 7168u);
          dma16(dO, sb + 2 * STG + 32768u + 1536u + lw * 3584u, oo, so + 16384u); dma16_next<1024>(dO, oo, so + 16384u + 7168u);
        }
      }
      NEXT_BUF(buf);
      if (n >= 1) {
        if (n == (has_w1 ? 7 : 3)) WAITV(12); else if (n == 1) { if (has_w1) WAITV(12); else WAITV(10); } else if (has_w1) WAITV(10); else WAITV(8);
        if (n == 1) STAMP(20);
        BARRIER();
        if (n == 1) STAMP(24);
      }
    }
    if (has_w1) WAITV(2); else WAITV(0);
    BARRIER();
    WAITV(0); BARRIER();
  } else if (wave < 4) {
    const int j = wave;
    __builtin_amdgcn_s_setprio(1);
    if (wave == 0) ((LAS unsigned*)(sm + L_FL))[lane] = (lane == 8 || lane == 0) ? 8u : 0u;
    f32x4 acc[4][3];
#pragma unroll
    for (int t = 0; t < 4; ++t)
#pragma unroll
      for (int c = 0; c < 3; ++c) acc[t][c] = (f32x4){0.f, 0.f, 0.f, 0.f};
    const unsigned wa0 = 32768u + (unsigned)(g4 >> 1) * 3584u + (unsigned)(12 * (g4 & 1) + q4) * 32u + p4 * 8u;
    const unsigned wa1 = wa0 ^ 128u;
    bf16x8 xA[4], wA[3], xB[4], wB[3];
#pragma unroll
    for (int t = 0; t < 4; ++t) xB[t] = __builtin_bit_cast(bf16x8, (u32x4){0u, 0u, 0u, 0u});
#pragma unroll
    for (int c = 0; c < 3; ++c) wB[c] = __builtin_bit_cast(bf16x8, (u32x4){0u, 0u, 0u, 0u});
#define QKV_RD(X, W, kk)                                                                                           \
    _Pragma("unroll") for (int c = 0; c < 3; ++c) W[c] = lds_tr2(sm, buf + wa0 + kk * 7168 + c * 512, buf + wa1 + kk * 7168 + c * 512); \
    _Pragma("unroll") for (int t = 0; t < 4; ++t) X[t] = lds_frag(sm, buf + (kk ? xa1 : xa0) + t * 2048);
#define QKV_MM(X, W)                                                                                               \
    _Pragma("unroll") for (int t = 0; t < 4; ++t) {                                                                \
      acc[t][0] = __builtin_amdgcn_mfma_f32_16x16x32_bf16(W[0], X[t], acc[t][0], 0, 0, 0);                         \
      acc[t][1] = __builtin_amdgcn_mfma_f32_16x16x32_bf16(W[1], X[t], acc[t][1], 0, 0, 0);                         \
      acc[t][2] = __builtin_amdgcn_mfma_f32_16x16x32_bf16(X[t], W[2], acc[t][2], 0, 0, 0);                         \
    }                                                                                                              \
    _Pragma("unroll") for (int u = 0; u < 10; ++u) { __builtin_amdgcn_sched_group_barrier(0x008, 1, 0); __builtin_amdgcn_sched_group_barrier(0x100, 1, 0); } \
    __builtin_amdgcn_sched_group_barrier(0x008, 2, 0);                                                             \
    asm volatile("" : "+v"(acc[0][0]), "+v"(acc[0][1]), "+v"(acc[0][2]), "+v"(acc[1][0]), "+v"(acc[1][1]), "+v"(acc[1][2]), \
                      "+v"(acc[2][0]), "+v"(acc[2][1]), "+v"(acc[2][2]), "+v"(acc[3][0]), "+v"(acc[3][1]), "+v"(acc[3][2]) :: "memory"); \
    __builtin_amdgcn_sched_barrier(0);
    unsigned buf = 0, nbuf = 2 * STG;
#pragma unroll 1
    for (int n = 0; n < 8; ++n) {
      BARRIER();
      STAMP(16 + n);
      QKV_RD(xA, wA, 0)
      QKV_MM(xB, wB)
      if (n < 6) qkv_issue(n + 2, nbuf);
      QKV_RD(xB, wB, 1)
      QKV_MM(xA, wA)
      NEXT_BUF(buf); NEXT_BUF(nbuf);
      if (n < 6) WAITV(2); else WAITV(0);
    }
    get_tag();
    QKV_MM(xB, wB)
    STAMP(1);
    const float qs = 0.25f * 1.4426950408889634f;
    const int rho = (l15 & 3) + 8 * ((l15 >> 2) & 1) + 4 * (l15 >> 3);
    const unsigned vo = 1024u + (unsigned)(g4 & 1) * 512u + (unsigned)((g4 >> 1) * 16 + rho) * 16u;
    const unsigned go = WS_KV + (unsigned)(hd * 64 + bi * 8 + 2 * j) * 2048u;
#pragma unroll
    for (int c = 2; c >= 0; --c)
#pragma unroll
      for (int tp = 0; tp < 2; ++tp) {
        const float sc = c ? 1.f : qs;
        const f32x4 a = acc[2 * tp][c] * sc, e = acc[2 * tp + 1][c] * sc;
        const auto r0 = __builtin_amdgcn_permlane16_swap(pk_bf16(a[0], a[1]), pk_bf16(e[0], e[1]), false, false);
        const auto r1 = __builtin_amdgcn_permlane16_swap(pk_bf16(a[2], a[3]), pk_bf16(e[2], e[3]), false, false);
        const u32x4 o = {r0[0], r1[0], r0[1], r1[1]};
        if (c == 0) *(LAS u32x4*)(sm + L_Q + (2 * j + tp) * 1024 + lane * 16) = o;
        else {
          ST128(o, (c == 1 ? lane * 16u : vo) + tp * 2048, go);
          *(LAS u32x4*)(sm + (2 * j + tp) * TILE + (c == 1 ? lane * 16u : vo + (g4 & 1) * 16u)) = o;
        }
      }
    if (lane < 4) *(LAS u32x4*)(sm + (2 * j + (lane >> 1)) * TILE + 1536 + (lane & 1) * 528) = ones;
    STAMP(10);
    BARRIER();
    WAITV(0);
    if (lane == 0) __builtin_amdgcn_raw_buffer_store_b64(tag, rws, WS_FKV + (unsigned)((hd * 8 + bi) * 4 + j) * 8u, 0, AUX_SC1);
    STAMP(11);
  } else {
    const int j = wave - 8;
    u32x2 ep = {0u, 0u};
    if (j == 0) ep = __builtin_amdgcn_raw_buffer_load_b64(rws, WS_EPOCH, 0, AUX_SC1);
    if (j == 1 && lane < 5)
      ep.x = __builtin_amdgcn_raw_buffer_load_b32(mk_rsrc((const void*)pc0, CODE_BYTES), (unsigned)(lane * 32 + idx) * 128u, 0, 0);
    auto put_tag = [&]() { if (j == 0) *(LAS u32x2*)(sm + L_FL + 256) = mk_tag(ep); else asm volatile("" :: "v"(ep)); };
    auto retile = [&](unsigned img, int ks) {
      const unsigned ta = img + (unsigned)(8 * hh + q4) * 128u + (unsigned)(4 * (g4 & 1) + 2 * (p4 & 1) + (p4 >> 1)) * 8u;
      ST128(__builtin_bit_cast(u32x4, lds_tr2(sm, ta, ta + 512)), WS_WT + lane * 16, (2 * bi * 144 + ks) * 1024);
      ST128(__builtin_bit_cast(u32x4, lds_tr2(sm, ta + 64, ta + 576)), WS_WT + lane * 16, ((2 * bi + 1) * 144 + ks) * 1024);
    };
    auto clear_o = [&]() {
      const u32x4 inv = {0xffffffffu, 0xffffffffu, 0xffffffffu, 0xffffffffu};
      const unsigned oo = WS_OF + (unsigned)((8 * bi + 2 * j) * 32 + hd) * 1024u + lane * 16u;
      ST128(inv, oo, 0); ST128(inv, oo + 32768, 0);
    };
    u32x4 kvd[3][4];
    auto kv_load = [&](int n0, int cnt) {
      glb_wait(rws, WS_FKV + (unsigned)((hd * 8 + ((bi + n0 + (lane < cnt ? lane : 0)) & 7)) * 4 + j) * 8u, tag);
#pragma unroll
      for (int u = 0; u < 3; ++u)
        if (u < cnt) {
          const unsigned go = WS_KV + (unsigned)(hd * 64 + ((bi + n0 + u) & 7) * 8 + 2 * j) * 2048u;
#pragma unroll
          for (int e = 0; e < 4; ++e) kvd[u][e] = LD128(lane * 16 + e * 1024, go);
        }
    };
    auto kv_store = [&](int n0, int cnt) {
#pragma unroll
      for (int u = 0; u < 3; ++u)
        if (u < cnt) {
          LAS char* dst = sm + ((n0 + u) * 8 + 2 * j) * TILE + lane * 16;
          *(LAS u32x4*)(dst) = kvd[u][0];
          *(LAS u32x4*)(dst + 1024 + hh * 16) = kvd[u][1];
          *(LAS u32x4*)(dst + TILE) = kvd[u][2];
          *(LAS u32x4*)(dst + TILE + 1024 + hh * 16) = kvd[u][3];
        }
      if (lane < 4 * cnt) *(LAS u32x4*)(sm + ((n0 + (lane >> 2)) * 8 + 2 * j + ((lane >> 1) & 1)) * TILE + 1536 + (lane & 1) * 528) = ones;
      WAITL0();
      if (lane < cnt) lds_add(sm, n0 + lane, 2u);
    };
    if (has_w1) {
      f32x4 acc[4][4];
      const unsigned wa = 32768u + 1536u + (unsigned)(g4 >> 1) * 3584u + (unsigned)(8 * (g4 & 1) + q4) * 128u +
                          (unsigned)((q4 >> 1) | ((g4 & 1) << 1)) * 32u + p4 * 8u;
      bf16x8 xA[4], wA[4], xB[4], wB[4];
#pragma unroll
      for (int t = 0; t < 4; ++t) { xB[t] = __builtin_bit_cast(bf16x8, (u32x4){0u, 0u, 0u, 0u}); wB[t] = xB[t]; }
#define W1_RD(X, W, kk)                                                                                            \
      _Pragma("unroll") for (int c = 0; c < 4; ++c) W[c] = lds_tr2(sm, buf + (wa ^ (unsigned)(c * 32)) + kk * 7168, buf + (wa ^ (unsigned)(c * 32)) + kk * 7168 + 512); \
      _Pragma("unroll") for (int t = 0; t < 4; ++t) X[t] = lds_frag(sm, buf + (kk ? xa1 : xa0) + t * 2048);
#define W1_MM(X, W)                                                                                                \
      _Pragma("unroll") for (int t = 0; t < 4; ++t)                                                                \
        _Pragma("unroll") for (int c = 0; c < 4; ++c) acc[t][c] = __builtin_amdgcn_mfma_f32_16x16x32_bf16(W[c], X[t], acc[t][c], 0, 0, 0); \
      _Pragma("unroll") for (int u = 0; u < 12; ++u) { __builtin_amdgcn_sched_group_barrier(0x008, 1, 0); __builtin_amdgcn_sched_group_barrier(0x100, 1, 0); } \
      __builtin_amdgcn_sched_group_barrier(0x008, 4, 0);                                                           \
      asm volatile("" : "+v"(acc[0][0]), "+v"(acc[0][1]), "+v"(acc[0][2]), "+v"(acc[0][3]), "+v"(acc[1][0]), "+v"(acc[1][1]), "+v"(acc[1][2]), "+v"(acc[1][3]), \
                        "+v"(acc[2][0]), "+v"(acc[2][1]), "+v"(acc[2][2]), "+v"(acc[2][3]), "+v"(acc[3][0]), "+v"(acc[3][1]), "+v"(acc[3][2]), "+v"(acc[3][3]) :: "memory"); \
      __builtin_amdgcn_sched_barrier(0);
      unsigned buf = 0;
      BARRIER();
#pragma unroll
      for (int c = 0; c < 4; ++c) {
        const u32x2 bv = *(LAS u32x2*)(sm + L_B1 + (16 * c + 4 * g4) * 2);
#pragma unroll
        for (int t = 0; t < 4; ++t) acc[t][c] = (f32x4){bf_lo(bv.x), bf_hi(bv.x), bf_lo(bv.y), bf_hi(bv.y)};
      }
#pragma unroll 1
      for (int n = 0; n < 8; ++n) {
        W1_RD(xA, wA, 0)
        W1_MM(xB, wB)
        W1_RD(xB, wB, 1)
        W1_MM(xA, wA)
        NEXT_BUF(buf);
        if (n == 2) put_tag();
        if (n < 7) BARRIER();
      }
      BARRIER();
      __builtin_amdgcn_s_setprio(3);
      get_tag();
      W1_MM(xB, wB)
      STAMP(1);
      const unsigned ao = WS_ACT + (unsigned)(8 * bi + 2 * j) * 114688u + (unsigned)cw * 4096u + (unsigned)g4 * 512u + l15 * 16u;
      auto mish2 = [&](int t0) {
#pragma unroll
        for (int t = t0; t < t0 + 2; ++t)
#pragma unroll
          for (int pc = 0; pc < 2; ++pc) {
            u32x4 o;
#pragma unroll
            for (int e = 0; e < 4; ++e) {
              f32x2 hv = {acc[t][2 * pc + (e >> 1)][2 * (e & 1)], acc[t][2 * pc + (e >> 1)][2 * (e & 1) + 1]};
              f32x2 a = hv * 1.4426950408889634f;
              f32x2 ex = {__builtin_amdgcn_exp2f(a.x), __builtin_amdgcn_exp2f(a.y)};
              f32x2 d = ex * (ex + 2.f) + 2.f;
              f32x2 r = {__builtin_amdgcn_rcpf(d.x), __builtin_amdgcn_rcpf(d.y)};
              f32x2 y = hv * (r * -2.f + 1.f);
              o[e] = pk_bf16(y.x, y.y);
            }
            ST128(o, ao + (t >> 1) * 114688 + pc * 2048 + (t & 1) * 256, 0);
          }
      };
      mish2(0);
      kv_load(1, 1);
      mish2(2);
      STAMP(2);
      kv_store(1, 1);
      clear_o();
      retile(L_RT + j * 2048u, 4 * cw + j);
      WAITV(0);
      if (lane == 0) __builtin_amdgcn_raw_buffer_store_b64(tag, rws, WS_FACT + (unsigned)((bi * 4 + j) * 32 + hd) * 8u, 0, AUX_SC1);
    } else {
      f32x4 acc[4];
#pragma unroll
      for (int t = 0; t < 4; ++t) acc[t] = (f32x4){0.f, 0.f, 0.f, 0.f};
      unsigned buf = 0;
#pragma unroll 1
      for (int n = 0; n < 8; ++n) {
        BARRIER();
        if (hd == 7) {
#pragma unroll
          for (int kk = 0; kk < 2; ++kk) {
            const bf16x8 gf = lds_frag(sm, L_G + ((n + rot) & 7) * 128 + kk * 64 + g4 * 16);
#pragma unroll
            for (int t = 0; t < 4; ++t) acc[t] = __builtin_amdgcn_mfma_f32_16x16x32_bf16(gf, lds_frag(sm, buf + (kk ? xa1 : xa0) + t * 2048), acc[t], 0, 0, 0);
          }
        }
        if (n == 2) { put_tag(); clear_o(); }
        if (n == 5 || n == 6) retile(n == 5 ? L_RT + j * 2048u : 2 * STG + 32768u + 1536u + j * 3584u, 112 + (hd >> 3) * 8 + 2 * j + (n - 5));
        NEXT_BUF(buf);
      }
      WAITV(0);
      get_tag();
      BARRIER();
      __builtin_amdgcn_s_setprio(3);
      STAMP(1);
      if (hd == 7) {
        const float lg = g4 == 0 ? acc[0][0] : g4 == 1 ? acc[1][0] : g4 == 2 ? acc[2][0] : acc[3][0];
        const float gv = __builtin_amdgcn_rcpf(1.f + __builtin_amdgcn_exp2f(lg * -1.4426950408889634f));
        __builtin_amdgcn_raw_buffer_store_b32(__float_as_uint(gv), rws, WS_G + (unsigned)(256 * bi + 64 * j + lane) * 4u, 0, AUX_SC1);
      }
      WAITV(0);
      if (lane == 0) __builtin_amdgcn_raw_buffer_store_b64(tag, rws, WS_FACT + (unsigned)((bi * 4 + j) * 32 + hd) * 8u, 0, AUX_SC1);
      kv_load(1, 1);
      STAMP(2);
      kv_store(1, 1);
    }
#pragma unroll 1
    for (int n0 = 2; n0 < 8; n0 += 3) {
      kv_load(n0, 3);
      kv_store(n0, 3);
    }
    STAMP(3);
    const int tm = hd >> 3, cp = hd & 7;
    if (j == 0) {
      glb_wait(rws, !hh ? WS_FACT + (unsigned)((4 * bi + tm) * 32 + l31) * 8u : WS_FACT + (unsigned)((4 * bi + p4) * 32 + 7) * 8u, tag);
#pragma unroll 1
      for (int u = 0; u < 2; ++u) glb_wait(rws, WS_FACT + (unsigned)(cp * 128 + u * 64 + lane) * 8u, tag);
      if (lane == 0) lds_add(sm, 18, 1u);
    } else lds_wait(sm, 18, 1u);
    STAMP(4);
    {
      *(LAS unsigned*)(sm + L_G + (64 * j + lane) * 4) = __builtin_amdgcn_raw_buffer_load_b32(rws, WS_G + (unsigned)(256 * bi + 64 * j + lane) * 4u, 0, AUX_SC1);
      f32x16 acc[2][2];
#pragma unroll
      for (int t = 0; t < 2; ++t)
#pragma unroll
        for (int c = 0; c < 2; ++c)
#pragma unroll
          for (int r = 0; r < 16; ++r) acc[t][c][r] = 0.f;
      const unsigned ab = WS_ACT + (unsigned)(8 * bi + 2 * tm + (j & 1)) * 114688u + (unsigned)(28 * j) * 1024u;
      const unsigned ab1 = WS_ACT + (unsigned)(8 * bi + 2 * tm + ((j & 1) ^ 1)) * 114688u + (unsigned)(28 * j) * 1024u;
      const unsigned wb = WS_WT + (unsigned)((2 * cp + (j >> 1)) * 144 + 28 * j) * 1024u;
      const unsigned wb1 = WS_WT + (unsigned)((2 * cp + ((j >> 1) ^ 1)) * 144 + 28 * j) * 1024u;
      u32x4 af[4][2], wf[4][2];
#pragma unroll
      for (int u = 0; u < 4; ++u) {
        af[u][0] = LD128(lane * 16 + u * 1024, ab); af[u][1] = LD128(lane * 16 + u * 1024, ab1);
        wf[u][0] = LD128(lane * 16 + u * 1024, wb); wf[u][1] = LD128(lane * 16 + u * 1024, wb1);
      }
      WAITL0();
      if (lane == 0) lds_add(sm, 17, 1u);
#pragma unroll 1
      for (int it = 1; it <= W2_N; ++it) {
        const unsigned nx = (it < 7) ? it * 4096u : 0u;
        const rsrc_t rws = mk_rsrc(ws, it < 7 ? WS_END : 0u);
#pragma unroll
        for (int u = 0; u < 4; ++u) {
#pragma unroll
          for (int t = 0; t < 2; ++t)
#pragma unroll
            for (int c = 0; c < 2; ++c)
              acc[t][c] = __builtin_amdgcn_mfma_f32_32x32x16_bf16(__builtin_bit_cast(bf16x8, wf[u][c]), __builtin_bit_cast(bf16x8, af[u][t]), acc[t][c], 0, 0, 0);
          af[u][0] = LD128(lane * 16 + u * 1024, ab + nx); af[u][1] = LD128(lane * 16 + u * 1024, ab1 + nx);
          wf[u][0] = LD128(lane * 16 + u * 1024, wb + nx); wf[u][1] = LD128(lane * 16 + u * 1024, wb1 + nx);
          __builtin_amdgcn_sched_barrier(0);
        }
      }
      lds_wait(sm, 17, 4u);
#pragma unroll
      for (int q = 0; q < 4; ++q) {
        LAS f32x4* bp = (LAS f32x4*)(sm + L_BP + (unsigned)(q ^ j) * 4096u + lane * 16);
        const f32x16& av = acc[q & 1][q >> 1];
        f32x4 v[4];
#pragma unroll
        for (int u = 0; u < 4; ++u) v[u] = (f32x4){av[4 * u], av[4 * u + 1], av[4 * u + 2], av[4 * u + 3]};
        if (q > 0) {
          lds_wait(sm, 32, 4u * q);
#pragma unroll
          for (int u = 0; u < 4; ++u) v[u] += bp[u * 64];
        }
#pragma unroll
        for (int u = 0; u < 4; ++u) bp[u * 64] = v[u];
        WAITL0();
        if (q < 3 && lane == 0) lds_add(sm, 32, 1u);
      }
    }
    STAMP(5);
    STAMP_OUT();
    if (b == 0 && j == 0) {
#pragma unroll 1
      for (int u = 0; u < 4; ++u) glb_wait(rws, WS_FACT + (unsigned)((2 * u + hh) * 128 + l31) * 8u, tag);
      if (lane == 0) __builtin_amdgcn_raw_buffer_store_b64(tag, rws, WS_EPOCH, 0, AUX_SC1);
    }
    return;
  }

  STAMP(2);
  const int w = wave;
  const unsigned ka = lane * 16;
  const unsigned va = 1024u + (l31 < 16 ? (unsigned)(hh * 16 + l31) * 16u : ((l31 >= 20 && l31 < 28) ? (unsigned)hh * 256u : 512u));
  const int IDX = 0xDDDD;
  const bf16x8 qf = lds_frag(sm, L_Q + w * 1024 + lane * 16);
  f32x16 negm, O;
  {
    f32x16 z;
#pragma unroll
    for (int r = 0; r < 16; ++r) { z[r] = 0.f; O[r] = 0.f; }
    f32x16 s0 = __builtin_amdgcn_mfma_f32_32x32x16_bf16(lds_frag(sm, ka), qf, z, 0, 0, 0);
    float m = s0[0];
#pragma unroll
    for (int r = 1; r < 16; ++r) m = fmaxf(m, s0[r]);
    m = fmaxf(m, __shfl_xor(m, 32));
#pragma unroll
    for (int r = 0; r < 16; ++r) negm[r] = -m;
  }
  STAMP(7);
  const int tm = hd >> 3, cp = hd & 7;
  const int tt = w & 1, kq = w >> 1;
  u32x4 wf[2][8];
  {
    u32x4 kf, v0, v1;
    unsigned kb = sb + ka, vb = sb + va, fb = sb + L_FL + 4, ft;
    int cnt = SPLIT_AT, st, sp;
    if (w < 4) __builtin_amdgcn_s_setprio(2); else __builtin_amdgcn_s_setprio(1);
    asm volatile(ATTN_LOOP
                 : "+v"(O), "=&v"(kf), "=&v"(v0), "=&v"(v1), "+v"(kb), "+v"(vb), "+s"(cnt), "+v"(fb), "=&v"(ft), "=&s"(st), "=&s"(sp)
                 : "v"(qf), "v"(negm), "v"(IDX)
                 : ATTN_CLOBS);
    STAMP(12);
    if (w < 4) __builtin_amdgcn_s_setprio(0);
    lds_wait(sm, 17, 4u);
    const unsigned wb = WS_WT + (unsigned)(2 * cp * 144 + 112 + 8 * kq) * 1024u;
#pragma unroll
    for (int u = 0; u < 8; ++u) {
      wf[0][u] = LD128(lane * 16 + (u & 3) * 1024, wb + (u >> 2) * 4096);
      wf[1][u] = LD128(lane * 16 + (u & 3) * 1024, wb + 147456 + (u >> 2) * 4096);
    }
    STAMP(13);
    cnt = 8 - SPLIT_AT;
    asm volatile(ATTN_LOOP
                 : "+v"(O), "=&v"(kf), "=&v"(v0), "=&v"(v1), "+v"(kb), "+v"(vb), "+s"(cnt), "+v"(fb), "=&v"(ft), "=&s"(st), "=&s"(sp)
                 : "v"(qf), "v"(negm), "v"(IDX)
                 : ATTN_CLOBS);
  }
  STAMP(3);
  float lsum = hh ? O[12] : O[8];
  {
    float chk = lsum * 0.f;
#pragma unroll
    for (int r = 0; r < 8; ++r) chk = __builtin_fmaf(O[r], 0.f, chk);
    if (__any(chk != chk)) {
      float m = -3.0e38f;
      f32x16 z;
#pragma unroll
      for (int r = 0; r < 16; ++r) { z[r] = 0.f; O[r] = 0.f; }
#pragma unroll 1
      for (int t = 0; t < 64; ++t) {
        f32x16 S = __builtin_amdgcn_mfma_f32_32x32x16_bf16(lds_frag(sm, ka + t * TILE), qf, z, 0, 0, 0);
        float mx = m;
#pragma unroll
        for (int r = 0; r < 16; ++r) mx = fmaxf(mx, S[r]);
        mx = fmaxf(mx, __shfl_xor(mx, 32));
        const float al = __builtin_amdgcn_exp2f(m - mx);
        m = mx;
        f32x8 p0, p1;
#pragma unroll
        for (int r = 0; r < 16; ++r) O[r] *= al;
#pragma unroll
        for (int r = 0; r < 8; ++r) { p0[r] = __builtin_amdgcn_exp2f(S[r] - m); p1[r] = __builtin_amdgcn_exp2f(S[8 + r] - m); }
        O = __builtin_amdgcn_smfmac_f32_32x32x32_bf16(lds_frag(sm, va + t * TILE), __builtin_bit_cast(bf16x16, p0), O, IDX, 0, 0);
        O = __builtin_amdgcn_smfmac_f32_32x32x32_bf16(lds_frag(sm, va + t * TILE + 528), __builtin_bit_cast(bf16x16, p1), O, IDX, 0, 0);
      }
      lsum = hh ? O[12] : O[8];
    }
  }
  {
    const float sc = *(LAS float*)(sm + L_G + (32 * w + l31) * 4) * __builtin_amdgcn_rcpf(lsum);
    u32x4 o;
#pragma unroll
    for (int e = 0; e < 4; ++e) {
      unsigned u = pk_bf16(O[2 * e] * sc, O[2 * e + 1] * sc);
      o[e] = (u == 0xffffffffu) ? 0x7fc07fc0u : u;
    }
    ST128(o, WS_OF + (unsigned)((8 * bi + w) * 32 + hd) * 1024u + lane * 16u, 0);
  }
  {
    STAMP(4);
    BARRIER();
    u32x4 of[8];
    const unsigned ob = WS_OF + (unsigned)((8 * bi + 2 * tm + tt) * 32 + 8 * kq) * 1024u;
#pragma unroll
    for (int u = 0; u < 8; ++u) of[u] = LD128(lane * 16 + (u & 3) * 1024, ob + (u >> 2) * 4096);
    const float om = 1.f - *(LAS float*)(sm + L_G + (64 * tm + 32 * tt + l31) * 4);
    f32x4 a, c;
    {
      LAS f32x4* bp = (LAS f32x4*)(sm + L_BP + ((tt + 2 * (kq >> 1)) * 4 + 2 * (kq & 1)) * 1024 + lane * 16);
      a = bp[0] * om; c = bp[64] * om;
    }
    f32x16 acc[2];
#pragma unroll
    for (int r = 0; r < 16; ++r) { acc[0][r] = 0.f; acc[1][r] = 0.f; }
    unsigned todo = 0xffu, next = 0;
#pragma unroll 1
    for (unsigned s = 0; s < SPIN_CAP; ++s) {
#pragma unroll
      for (int u = 0; u < 8; ++u)
        if (todo & (1u << u)) {
          const unsigned mx = max(max(of[u][0], of[u][1]), max(of[u][2], of[u][3]));
          if (__all(mx != 0xffffffffu) || s == SPIN_CAP - 1) todo &= ~(1u << u);
        }
      CFENCE();
#pragma unroll
      for (int u = 0; u < 8; ++u)
        if (todo & (1u << u)) of[u] = LD128(lane * 16 + (u & 3) * 1024, ob + (u >> 2) * 4096);
#pragma unroll
      for (int u = 0; u < 8; ++u)
        if (next == (unsigned)u && !(todo & (1u << u))) {
          asm volatile("v_mfma_f32_32x32x16_bf16 %0, %2, %4, %0\n\tv_mfma_f32_32x32x16_bf16 %1, %3, %4, %1"
                       : "+v"(acc[0]), "+v"(acc[1]) : "v"(wf[0][u]), "v"(wf[1][u]), "v"(of[u]));
          next = u + 1;
        }
      if (next == 8) break;
    }
    asm volatile("s_nop 15" : "+v"(acc[0]), "+v"(acc[1]));
    STAMP(5);
#pragma unroll
    for (int p = 0; p < 4; ++p) {
      const f32x4 lo = {acc[p >> 1][8 * (p & 1)], acc[p >> 1][8 * (p & 1) + 1], acc[p >> 1][8 * (p & 1) + 2], acc[p >> 1][8 * (p & 1) + 3]};
      const f32x4 hi = {acc[p >> 1][8 * (p & 1) + 4], acc[p >> 1][8 * (p & 1) + 5], acc[p >> 1][8 * (p & 1) + 6], acc[p >> 1][8 * (p & 1) + 7]};
      if (p != kq) {
        LAS f32x4* d = (LAS f32x4*)(sm + ((tt * 4 + p) * 4 + kq) * 2048 + lane * 16);
        d[0] = lo; d[64] = hi;
      } else { a += lo; c += hi; }
    }
    BARRIER();
#pragma unroll
    for (int q = 1; q < 4; ++q) {
      LAS f32x4* d = (LAS f32x4*)(sm + ((tt * 4 + kq) * 4 + ((kq + q) & 3)) * 2048 + lane * 16);
      a += d[0]; c += d[64];
    }
    u32x4 o = {pk_bf16(a[0], a[1]), pk_bf16(a[2], a[3]), pk_bf16(c[0], c[1]), pk_bf16(c[2], c[3])};
    const rsrc_t ro = mk_rsrc(out, 2048u * 1024u);
    __builtin_amdgcn_raw_buffer_store_b128(o, ro, (unsigned)(256 * bi + 64 * tm + 32 * tt + l31) * 1024u + (unsigned)(64 * cp + 16 * kq + 8 * hh) * 2u, 0, OUT_AUX);
  }
  STAMP(6);
  STAMP_OUT();
}

extern "C" void kernel_launch(void* const* d_in, const int* in_sizes, int n_in, void* d_out, int out_size,
                              void* d_ws, size_t ws_size, hipStream_t stream) {
  fused_kernel<<<256, 768, 0, stream>>>((const u16*)d_in[0], (const u16*)d_in[1], (const u16*)d_in[2], (const u16*)d_in[4],
                                        (const u16*)d_in[5], (const u16*)d_in[6], (const u16*)d_in[7], (u16*)d_out,
                                        (unsigned char*)d_ws);
}
```

```cpp
#define KVL 2
#include <hip/hip_runtime.h>
#include <hip/hip_bf16.h>

typedef unsigned short u16;
typedef unsigned long long u64;
typedef __bf16 bf16x8 __attribute__((ext_vector_type(8)));
typedef __bf16 bf16x16 __attribute__((ext_vector_type(16)));
typedef __bf16 bf16x2 __attribute__((ext_vector_type(2)));
typedef short s16x4 __attribute__((ext_vector_type(4)));
typedef float f32x2 __attribute__((ext_vector_type(2)));
typedef float f32x4 __attribute__((ext_vector_type(4)));
typedef float f32x8 __attribute__((ext_vector_type(8)));
typedef float f32x16 __attribute__((ext_vector_type(16)));
typedef unsigned u32x2 __attribute__((ext_vector_type(2)));
typedef unsigned u32x4 __attribute__((ext_vector_type(4)));
#define LAS __attribute__((address_space(3)))
#define GAS __attribute__((address_space(1)))
typedef __amdgpu_buffer_rsrc_t rsrc_t;

constexpr unsigned WS_EPOCH = 0;
constexpr unsigned WS_FKV = 4096;
constexpr unsigned WS_FACT = WS_FKV + 8192;
constexpr unsigned WS_FCL = WS_FACT + 8192;
constexpr unsigned WS_G = 32768;
constexpr unsigned WS_KV = 65536;
constexpr unsigned WS_ACT = WS_KV + 32u * 131072u;
constexpr unsigned WS_WT = WS_ACT + 64u * 114688u;
constexpr unsigned WS_OF = WS_WT + 16u * 144u * 1024u;
constexpr unsigned WS_END = WS_OF + 64u * 32u * 1024u;
constexpr unsigned WS_DIAG = WS_END;

constexpr unsigned STG = 47104;
constexpr unsigned TILE = 2064;
constexpr unsigned L_RT = 3 * STG;
constexpr unsigned L_BP = 133120;
constexpr unsigned L_Q = L_BP + 16384;
constexpr unsigned L_B1 = L_Q + 8192;
constexpr unsigned L_G = 158720;
constexpr unsigned L_FL = L_G + 1024;
constexpr unsigned L_END = L_FL + 512;

constexpr unsigned SPIN_CAP = 1u << 15;
constexpr int AUX_SC1 = 16;
#define POLL_SLEEP 4

__device__ __forceinline__ unsigned pk_bf16(float a, float b) {
  f32x2 v = {a, b};
  return __builtin_bit_cast(unsigned, __builtin_convertvector(v, bf16x2));
}
__device__ __forceinline__ float bf_lo(unsigned u) { return __uint_as_float(u << 16); }
__device__ __forceinline__ float bf_hi(unsigned u) { return __uint_as_float(u & 0xffff0000u); }
__device__ __forceinline__ rsrc_t mk_rsrc(const void* p, unsigned bytes) {
  return __builtin_amdgcn_make_buffer_rsrc((void*)p, 0, bytes, 0x00020000);
}
__device__ __forceinline__ u32x4 mk_desc(const void* p, unsigned bytes) {
  const u64 a = (u64)p;
  return (u32x4){(unsigned)a, (unsigned)(a >> 32) & 0xffffu, bytes, 0x00020000u};
}
__device__ __forceinline__ void dma16(u32x4 desc, unsigned lds_addr, unsigned voff, unsigned soff) {
  asm volatile("s_mov_b32 m0, %0\n\ts_nop 3\n\tbuffer_load_dwordx4 %1, %2, %3 offen lds"
               :: "s"(lds_addr), "v"(voff), "s"(desc), "s"(soff) : "memory");
}
__device__ __forceinline__ void dma16x2_sc1(u32x4 desc, unsigned lds_addr, unsigned voff, unsigned soff) {
  asm volatile("s_mov_b32 m0, %0\n\ts_nop 3\n\tbuffer_load_dwordx4 %1, %2, %3 offen sc1 lds\n\tbuffer_load_dwordx4 %1, %2, %3 offen offset:1024 sc1 lds"
               :: "s"(lds_addr), "v"(voff), "s"(desc), "s"(soff) : "memory");
}
template <int OFF>
__device__ __forceinline__ void dma16_next(u32x4 desc, unsigned voff, unsigned soff) {
  asm volatile("buffer_load_dwordx4 %0, %1, %2 offen offset:%3 lds" :: "v"(voff), "s"(desc), "s"(soff), "i"(OFF) : "memory");
}
#define BARRIER() asm volatile("s_waitcnt lgkmcnt(0)\n\ts_barrier" ::: "memory")
#define WAITV(n) asm volatile("s_waitcnt vmcnt(" #n ")" ::: "memory")
#define WAITL0() asm volatile("s_waitcnt lgkmcnt(0)" ::: "memory")
#define CFENCE() asm volatile("" ::: "memory")
#define LD128(off, soff) __builtin_amdgcn_raw_buffer_load_b128(rws, off, soff, AUX_SC1)
#define ST128(v, off, soff) __builtin_amdgcn_raw_buffer_store_b128(v, rws, off, soff, AUX_SC1)

#define STAMP(k) do {} while (0)
#define STAMP_OUT() do {} while (0)
#define ACC_BEG() do {} while (0)
#define ACC_END(a) do {} while (0)

#define STAMP2(k) do {} while (0)
__device__ __forceinline__ bf16x8 lds_frag(LAS char* sm, unsigned off) { return *(LAS bf16x8*)(sm + off); }
__device__ __forceinline__ bf16x8 lds_tr2(LAS char* sm, unsigned o0, unsigned o1) {
  s16x4 a = __builtin_amdgcn_ds_read_tr16_b64_v4i16((LAS s16x4*)(sm + o0));
  s16x4 b = __builtin_amdgcn_ds_read_tr16_b64_v4i16((LAS s16x4*)(sm + o1));
  return __builtin_bit_cast(bf16x8, __builtin_shufflevector(a, b, 0, 1, 2, 3, 4, 5, 6, 7));
}
__device__ __forceinline__ void lds_add(LAS char* sm, unsigned idx, unsigned v) {
  __hip_atomic_fetch_add((LAS unsigned*)(sm + L_FL) + idx, v, __ATOMIC_RELAXED, __HIP_MEMORY_SCOPE_WORKGROUP);
}
__device__ __forceinline__ void lds_wait(LAS char* sm, unsigned idx, unsigned want) {
#pragma unroll 1
  for (unsigned s = 0; s < SPIN_CAP * 4; ++s) {
    CFENCE();
    unsigned v = __hip_atomic_load((LAS unsigned*)(sm + L_FL) + idx, __ATOMIC_RELAXED, __HIP_MEMORY_SCOPE_WORKGROUP);
    if ((unsigned)__builtin_amdgcn_readfirstlane(v) >= want) break;
    __builtin_amdgcn_s_sleep(1);
  }
  CFENCE();
}
__device__ __forceinline__ void glb_wait(rsrc_t rs, unsigned off, u32x2 tag) {
#pragma unroll 1
  for (unsigned s = 0; s < SPIN_CAP; ++s) {
    CFENCE();
    u32x2 v = __builtin_amdgcn_raw_buffer_load_b64(rs, off, 0, AUX_SC1);
    if (__all(v.x == tag.x && v.y == tag.y)) break;
    __builtin_amdgcn_s_sleep(POLL_SLEEP);
  }
  CFENCE();
}

#define AE(b, i) "v_exp_f32 v[" #b "+" #i "], v[" #b "+" #i "]\n"
#define ATILE(cur, oth, t)                                                                                          \
  AE(cur, 0) AE(cur, 1)                                                                                              \
  "s_waitcnt lgkmcnt(2)\n v_smfmac_f32_32x32x32_bf16 %0, %3, v[" #oth "+8:" #oth "+15], %14\n"                       \
  "ds_read_b128 %3, %11 offset:2064*" #t "\n"                                                                        \
  AE(cur, 2) AE(cur, 3) AE(cur, 4) AE(cur, 5)                                                                        \
  "s_waitcnt lgkmcnt(2)\n v_mfma_f32_32x32x16_bf16 v[" #oth ":" #oth "+15], %1, %12, %13\n"                          \
  "ds_read_b128 %1, %4 offset:2064*(" #t "+2)\n"                                                                     \
  AE(cur, 6) AE(cur, 7) AE(cur, 8) AE(cur, 9) AE(cur, 10)                                                            \
  "s_waitcnt lgkmcnt(2)\n v_smfmac_f32_32x32x32_bf16 %0, %2, v[" #cur ":" #cur "+7], %14\n"                          \
  "ds_read_b128 %2, %5 offset:2064*(" #t "+1)\n"                                                                     \
  AE(cur, 11) AE(cur, 12) AE(cur, 13) AE(cur, 14) AE(cur, 15)
#define ATTN_LOOP                                                                                                   \
  "ds_read_b128 %1, %4\n ds_read_b128 %3, %5\n"                                                                      \
  "v_mov_b32 v120, 0\n v_mov_b32 v121, 0\n v_mov_b32 v122, 0\n v_mov_b32 v123, 0\n"                                  \
  "v_mov_b32 v124, 0\n v_mov_b32 v125, 0\n v_mov_b32 v126, 0\n v_mov_b32 v127, 0\n"                                  \
  "s_waitcnt lgkmcnt(0)\n v_mfma_f32_32x32x16_bf16 v[96:111], %1, %12, %13\n"                                        \
  "ds_read_b128 %1, %4 offset:2064\n ds_read_b128 %2, %5\n s_nop 11\n"                                               \
  "1:\n"                                                                                                             \
  ATILE(96, 112, 0) ATILE(112, 96, 1) ATILE(96, 112, 2) ATILE(112, 96, 3) ATILE(96, 112, 4)                          \
  "ds_read_b32 %8, %7\n"                                                                                             \
  ATILE(112, 96, 5)                                                                                                  \
  "v_readfirstlane_b32 %9, %8\n s_nop 3\n s_cmp_eq_u32 %9, 8\n s_cbranch_scc1 3f\n"         \
  "2:\n s_sleep 1\n ds_read_b32 %8, %7\n s_waitcnt lgkmcnt(0)\n v_readfirstlane_b32 %9, %8\n s_sub_u32 %10, %10, 1\n" \
  "s_cmp_le_i32 %10, 0\n s_cbranch_scc1 3f\n s_cmp_eq_u32 %9, 8\n s_cbranch_scc0 2b\n"                               \
  "3:\n"                                                                                                             \
  ATILE(96, 112, 6) ATILE(112, 96, 7)                                                                                \
  "v_add_u32 %4, 8*2064, %4\n v_add_u32 %5, 8*2064, %5\n v_add_u32 %11, 8*2064, %11\n v_add_u32 %7, 4, %7\n s_sub_u32 %6, %6, 1\n"                \
  "s_cmp_lg_u32 %6, 0\n s_cbranch_scc1 1b\n"                                                                         \
  "s_waitcnt lgkmcnt(0)\n v_smfmac_f32_32x32x32_bf16 %0, %3, v[120:127], %14\n s_nop 15\n"
#define ATTN_CLOBS "v96","v97","v98","v99","v100","v101","v102","v103","v104","v105","v106","v107","v108","v109","v110","v111", \
  "v112","v113","v114","v115","v116","v117","v118","v119","v120","v121","v122","v123","v124","v125","v126","v127","scc","memory"
#define CODE_BYTES 17408u
#define W2_N 7
#define OUT_AUX 0
#define ROTEXPR (((hd + bi) & 1) * 4)
#define P3_MODE 2
#define WO_PRIO 3
#define KVOFF(h, g) (WS_KV + (unsigned)(((((h) & 7) * 16 + ((g) & 15)) * 8) + ((h) >> 3) * 2 + ((g) >> 4)) * 4096u)
#define FIRST_MAX 2
#define RT_AT 5
#define P3_LEFT 0
#define P3_SLEEP 4
#define P3_HOFF (r8 * 1024 + 1020)
#define SPLIT_AT 6

__global__ void __launch_bounds__(768, 3)
fused_kernel(const u16* __restrict__ x, const u16* __restrict__ wqkv, const u16* __restrict__ wo,
             const u16* __restrict__ w1, const u16* __restrict__ b1, const u16* __restrict__ w2,
             const u16* __restrict__ gw, u16* __restrict__ out, unsigned char* __restrict__ ws) {
  __shared__ __attribute__((aligned(16))) char smem_[L_END];
  LAS char* sm = (LAS char*)smem_;
  const unsigned sb = (unsigned)(size_t)sm;
  u64 pc0;
  asm volatile("s_getpc_b64 %0" : "=s"(pc0));
  pc0 &= ~255ull;
  asm volatile("" :: "s"(x), "s"(wqkv), "s"(wo), "s"(w1), "s"(b1), "s"(w2), "s"(gw), "s"(out), "s"(ws));
  const int lane = threadIdx.x & 63;
  const int wave = __builtin_amdgcn_readfirstlane(threadIdx.x >> 6);
  const int b = blockIdx.x, xcd = b & 7, idx = b >> 3;
  const int bi = 4 * (xcd & 1) + (idx & 3), hd = 8 * (xcd >> 1) + (idx >> 2);
  const bool has_w1 = (hd & 7) != 7;
  const int cw = hd - (hd >> 3);
  const int rot = ROTEXPR;
  const int hh = lane >> 5, l31 = lane & 31, l15 = lane & 15, g4 = lane >> 4;
  const int q4 = l15 >> 2, p4 = lane & 3, r8 = lane >> 3, sl8 = lane & 7;
  const rsrc_t rws = mk_rsrc(ws, WS_END);
  const u32x4 ones = {0x3f803f80u, 0x3f803f80u, 0x3f803f80u, 0x3f803f80u};
  STAMP(0);

  auto mk_tag = [&](u32x2 e) {
    u64 z = ((u64)(unsigned)__builtin_amdgcn_readfirstlane(e.y) << 32 | (u64)(unsigned)__builtin_amdgcn_readfirstlane(e.x)) + 0x9E3779B97F4A7C15ull;
    z = (z ^ (z >> 30)) * 0xBF58476D1CE4E5B9ull;
    z = (z ^ (z >> 27)) * 0x94D049BB133111EBull;
    z ^= z >> 31;
    return (u32x2){(unsigned)z, (unsigned)(z >> 32)};
  };
  u32x2 tag;
  auto get_tag = [&]() {
    const u32x2 t = *(LAS u32x2*)(sm + L_FL + 256);
    tag = (u32x2){(unsigned)__builtin_amdgcn_readfirstlane(t.x), (unsigned)__builtin_amdgcn_readfirstlane(t.y)};
  };
  const unsigned xa0 = (unsigned)(64 * (wave & 3) + l15) * 128u + (unsigned)(g4 ^ (l15 >> 1)) * 16u;
  const unsigned xa1 = xa0 ^ 64u;
#define NEXT_BUF(v) v = (v == 2 * STG) ? 0 : v + STG
  const unsigned qo = (unsigned)(16 * (wave & 3) + (((lane >> 1) & 15) ^ ((g4 & 1) << 2))) * 3072u + (unsigned)hh * 1024u + (unsigned)hd * 32u + (lane & 1) * 16u;
  auto qkv_issue = [&](int n, unsigned buf) {
    const u32x4 d = mk_desc(wqkv, 512u * 3072u);
    const unsigned so = ((unsigned)(n + rot) & 7u) * (64u * 3072u);
    dma16(d, sb + buf + 32768u + (wave & 3) * 3584u, qo, so);
    if (lane < 32) dma16_next<1024>(d, qo, so + 1024u);
  };

  if (__builtin_expect(wave >= 4 && wave < 8, 1)) {
    const int lw = wave - 4;
    const u32x4 dx = mk_desc(x, 2048u * 1024u);
    const unsigned xo0 = (unsigned)(256 * bi + 64 * lw + r8) * 1024u + (unsigned)(sl8 ^ (r8 >> 1)) * 16u;
    const unsigned xo1 = (xo0 ^ 64u) + 8192u - 1024u;
    const u32x4 d1 = mk_desc(w1, 512u * 3584u);
    const unsigned wo0 = (unsigned)(16 * lw + r8) * 3584u + (unsigned)cw * 128u + (unsigned)(sl8 ^ (r8 & 2)) * 16u;
    const unsigned wo1 = (wo0 ^ 64u) + 8u * 3584u - 1024u;
    unsigned buf = 0;
    STAMP(16);
#pragma unroll 1
    for (int n = 0; n < 8; ++n) {
      const unsigned ks = (unsigned)(n + rot) & 7u, d = sb + buf + lw * 8192u;
      if (n < 2) qkv_issue(n, buf);
#pragma unroll
      for (int u = 0; u < 2; ++u) {
        dma16(dx, d + u * 4096u, xo0, ks * 128u + u * 32768u);
        dma16_next<1024>(dx, xo1, ks * 128u + u * 32768u);
        dma16_next<2048>(dx, xo0, ks * 128u + u * 32768u + 16384u - 2048u);
        dma16_next<3072>(dx, xo1, ks * 128u + u * 32768u + 16384u - 2048u);
      }
      if (has_w1) {
        dma16(d1, sb + buf + 32768u + 1536u + lw * 3584u, wo0, ks * (64u * 3584u));
        dma16_next<1024>(d1, wo1, ks * (64u * 3584u));
      }
      if (n == 0 && lw == 0) {
        if (has_w1) { if (lane < 8) dma16(mk_desc(b1, 3584u), sb + L_B1, lane * 16u + (unsigned)cw * 128u, 0u); }
        else dma16(mk_desc(gw, 1024u), sb + L_G, lane * 16u, 0u);
      }
      if (n == RT_AT) {
        if (has_w1) {
          const u32x4 d2 = mk_desc(w2, 1792u * 1024u);
          const unsigned o2 = (unsigned)(16 * (r8 >> 2) + (r8 & 3)) * 1024u + (unsigned)bi * 128u + (unsigned)sl8 * 16u;
          const unsigned so = (unsigned)(64 * cw + 32 * (lw >> 1) + 8 * (lw & 1)) * 1024u;
          dma16(d2, sb + L_RT + lw * 2048u, o2, so); dma16_next<1024>(d2, o2, so + 3072u);
        } else {
          const u32x4 dO = mk_desc(wo, 512u * 1024u);
          const unsigned oo = (unsigned)r8 * 1024u + (unsigned)bi * 128u + (unsigned)sl8 * 16u;
          const unsigned so = (unsigned)((hd >> 3) * 8 + 2 * lw) * 16384u;
          dma16(dO, sb + L_RT + lw * 2048u, oo, so); dma16_next<1024>(dO, oo, so + 7168u);
          dma16(dO, sb + 2 * STG + 32768u + 1536u + lw * 3584u, oo, so + 16384u); dma16_next<1024>(dO, oo, so + 16384u + 7168u);
        }
      }
      NEXT_BUF(buf);
      if (n >= 1) {
        if (n == RT_AT) WAITV(12); else if (n == 1) { if (has_w1) WAITV(12); else WAITV(10); } else if (has_w1) WAITV(10); else WAITV(8);
        if (n == 1) STAMP(20);
        BARRIER();
        if (n == 1) STAMP(24);
      }
    }
    if (RT_AT == 7) WAITV(2); else WAITV(0);
    BARRIER();
    if (lane < 2) *(LAS u32x4*)(sm + (2 * lw + lane) * TILE + 2048) = ones;
    WAITV(0); BARRIER();
  } else if (wave < 4) {
    const int j = wave;
    __builtin_amdgcn_s_setprio(3);
    if (wave == 0) ((LAS unsigned*)(sm + L_FL))[lane] = (lane == 8 || lane == 0) ? 8u : 0u;
    f32x4 acc[4][3];
#pragma unroll
    for (int t = 0; t < 4; ++t)
#pragma unroll
      for (int c = 0; c < 3; ++c) acc[t][c] = (f32x4){0.f, 0.f, 0.f, 0.f};
    const unsigned wa0 = 32768u + (unsigned)(g4 >> 1) * 3584u + (unsigned)(12 * (g4 & 1) + q4) * 32u + p4 * 8u;
    const unsigned wa1 = wa0 ^ 128u;
    bf16x8 xA[4], wA[3], xB[4], wB[3];
#pragma unroll
    for (int t = 0; t < 4; ++t) xB[t] = __builtin_bit_cast(bf16x8, (u32x4){0u, 0u, 0u, 0u});
#pragma unroll
    for (int c = 0; c < 3; ++c) wB[c] = __builtin_bit_cast(bf16x8, (u32x4){0u, 0u, 0u, 0u});
#define QKV_RD(X, W, kk)                                                                                           \
    _Pragma("unroll") for (int c = 0; c < 3; ++c) W[c] = lds_tr2(sm, buf + wa0 + kk * 7168 + c * 512, buf + wa1 + kk * 7168 + c * 512); \
    _Pragma("unroll") for (int t = 0; t < 4; ++t) X[t] = lds_frag(sm, buf + (kk ? xa1 : xa0) + t * 2048);
#define QKV_MM(X, W)                                                                                               \
    _Pragma("unroll") for (int t = 0; t < 4; ++t) {                                                                \
      acc[t][0] = __builtin_amdgcn_mfma_f32_16x16x32_bf16(W[0], X[t], acc[t][0], 0, 0, 0);                         \
      acc[t][1] = __builtin_amdgcn_mfma_f32_16x16x32_bf16(W[1], X[t], acc[t][1], 0, 0, 0);                         \
      acc[t][2] = __builtin_amdgcn_mfma_f32_16x16x32_bf16(X[t], W[2], acc[t][2], 0, 0, 0);                         \
    }                                                                                                              \
    _Pragma("unroll") for (int u = 0; u < 10; ++u) { __builtin_amdgcn_sched_group_barrier(0x008, 1, 0); __builtin_amdgcn_sched_group_barrier(0x100, 1, 0); } \
    __builtin_amdgcn_sched_group_barrier(0x008, 2, 0);                                                             \
    asm volatile("" : "+v"(acc[0][0]), "+v"(acc[0][1]), "+v"(acc[0][2]), "+v"(acc[1][0]), "+v"(acc[1][1]), "+v"(acc[1][2]), \
                      "+v"(acc[2][0]), "+v"(acc[2][1]), "+v"(acc[2][2]), "+v"(acc[3][0]), "+v"(acc[3][1]), "+v"(acc[3][2]) :: "memory"); \
    __builtin_amdgcn_sched_barrier(0);
    unsigned vo = 1024u + (unsigned)(g4 & 1) * 512u + (unsigned)((g4 >> 1) * 16 + (l15 & 3) + 8 * ((l15 >> 2) & 1) + 4 * (l15 >> 3)) * 16u;
    unsigned eq = L_Q + 2 * j * 1024 + lane * 16, ek = 2 * j * TILE + lane * 16, ev = 2 * j * TILE + vo;
    unsigned go = KVOFF(hd, bi * 4 + j);
    asm volatile("" : "+v"(vo), "+v"(eq), "+v"(ek), "+v"(ev), "+s"(go));
    unsigned buf = 0, nbuf = 2 * STG;
#pragma unroll 1
    for (int n = 0; n < 8; ++n) {
      BARRIER();
      STAMP(16 + n);
      QKV_RD(xA, wA, 0)
      QKV_MM(xB, wB)
      if (n < 6) qkv_issue(n + 2, nbuf);
      QKV_RD(xB, wB, 1)
      QKV_MM(xA, wA)
      NEXT_BUF(buf); NEXT_BUF(nbuf);
      if (n < 6) WAITV(2); else WAITV(0);
    }
    QKV_MM(xB, wB)
    STAMP(1);
    u32x4 pub[3][2];
#pragma unroll
    for (int c = 2; c >= 0; --c)
#pragma unroll
      for (int tp = 0; tp < 2; ++tp) {
        const float sc = c ? 1.f : 0.25f * 1.4426950408889634f;
        const f32x4 a = acc[2 * tp][c] * sc, e = acc[2 * tp + 1][c] * sc;
        const auto r0 = __builtin_amdgcn_permlane16_swap(pk_bf16(a[0], a[1]), pk_bf16(e[0], e[1]), false, false);
        const auto r1 = __builtin_amdgcn_permlane16_swap(pk_bf16(a[2], a[3]), pk_bf16(e[2], e[3]), false, false);
        pub[c][tp] = (u32x4){r0[0], r1[0], r0[1], r1[1]};
        if (c) ST128(pub[c][tp], (c == 1 ? lane * 16u : vo) + tp * 2048, go);
        *(LAS u32x4*)(sm + (c == 0 ? eq + tp * 1024 : (c == 1 ? ek : ev) + tp * TILE)) = pub[c][tp];
      }
    asm volatile("" :: "v"(pub[1][0]), "v"(pub[1][1]), "v"(pub[2][0]), "v"(pub[2][1]));
    STAMP(10);
    BARRIER();
    get_tag();
    WAITV(0);
    if (lane == 0) __builtin_amdgcn_raw_buffer_store_b64(tag, rws, WS_FKV + (unsigned)((hd * 8 + bi) * 4 + j) * 8u, 0, AUX_SC1);
    STAMP(11);
  } else {
    const int j = wave - 8;
    u32x2 ep = {0u, 0u};
    if (j == 0) ep = __builtin_amdgcn_raw_buffer_load_b64(rws, WS_EPOCH, 0, AUX_SC1);
    if (j == 1 && lane < 5)
      ep.x = __builtin_amdgcn_raw_buffer_load_b32(mk_rsrc((const void*)pc0, CODE_BYTES), (unsigned)(lane * 32 + idx) * 128u, 0, 0);
    auto put_tag = [&]() { if (j == 0) *(LAS u32x2*)(sm + L_FL + 256) = mk_tag(ep); else asm volatile("" :: "v"(ep)); };
    auto retile = [&](unsigned img, int ks) {
      const unsigned ta = img + (unsigned)(8 * hh + q4) * 128u + (unsigned)(4 * (g4 & 1) + 2 * (p4 & 1) + (p4 >> 1)) * 8u;
      ST128(__builtin_bit_cast(u32x4, lds_tr2(sm, ta, ta + 512)), WS_WT + lane * 16, (2 * bi * 144 + ks) * 1024);
      ST128(__builtin_bit_cast(u32x4, lds_tr2(sm, ta + 64, ta + 576)), WS_WT + lane * 16, ((2 * bi + 1) * 144 + ks) * 1024);
    };
    auto clear_o = [&]() {
      const u32x4 inv = {0xffffffffu, 0xffffffffu, 0xffffffffu, 0xffffffffu};
      const unsigned oo = WS_OF + (unsigned)((8 * bi + 2 * j) * 32 + hd) * 1024u + lane * 16u;
      ST128(inv, oo, 0); ST128(inv, oo + 32768, 0);
    };
    const unsigned fkv = WS_FKV + (unsigned)((hd * 8 + ((bi + lane) & 7)) * 4 + j) * 8u;
    auto gather = [&](u32x2 fv, auto pre) __attribute__((always_inline)) {
      const u32x4 dws = mk_desc(ws, WS_END);
      unsigned spins = 0;
#pragma unroll 1
      for (int n0 = 1; n0 < 8;) {
        const unsigned rdy = ((unsigned)__builtin_amdgcn_ballot_w64(fv.x == tag.x && fv.y == tag.y) & 0xffu) >> n0;
        int cnt = __builtin_ctz(~rdy);
        if (++spins > SPIN_CAP) cnt = 8 - n0;
        CFENCE();
        if (n0 == 1 && cnt > FIRST_MAX) cnt = FIRST_MAX;
#pragma unroll 1
        for (int u = n0; u < n0 + cnt; ++u) {
          const unsigned go = KVOFF(hd, ((bi + u) & 7) * 4 + j), d = sb + (unsigned)(u * 8 + 2 * j) * TILE;
          const unsigned gu = __builtin_amdgcn_readfirstlane(go), du = __builtin_amdgcn_readfirstlane(d);
          dma16x2_sc1(dws, du, lane * 16u, gu);
          dma16x2_sc1(dws, du + TILE, lane * 16u, gu + 2048u);
        }
        if (n0 + cnt == 8) {
          const int ns = pre();
          if (ns) WAITV(10); else WAITV(0);
        } else {
          if (!cnt) __builtin_amdgcn_s_sleep(POLL_SLEEP);
          fv = __builtin_amdgcn_raw_buffer_load_b64(rws, fkv, 0, AUX_SC1);
          WAITV(0);
        }
        if (lane < cnt) lds_add(sm, n0 + lane, 2u);
        if (cnt && n0 == 1) STAMP(7);
        n0 += cnt;
      }
      WAITV(0);
      if (lane == 0) __builtin_amdgcn_raw_buffer_store_b64(tag, rws, WS_FACT + (unsigned)((bi * 4 + j) * 32 + hd) * 8u, 0, AUX_SC1);
    };
    auto put_ones = [&]() { if (lane < 14) *(LAS u32x4*)(sm + ((1 + (lane >> 1)) * 8 + 2 * j + (lane & 1)) * TILE + 2048) = ones; };
    if (has_w1) {
      f32x4 acc[4][4];
      const unsigned wa = 32768u + 1536u + (unsigned)(g4 >> 1) * 3584u + (unsigned)(8 * (g4 & 1) + q4) * 128u +
                          (unsigned)((q4 >> 1) | ((g4 & 1) << 1)) * 32u + p4 * 8u;
      bf16x8 xA[4], wA[4], xB[4], wB[4];
#pragma unroll
      for (int t = 0; t < 4; ++t) { xB[t] = __builtin_bit_cast(bf16x8, (u32x4){0u, 0u, 0u, 0u}); wB[t] = xB[t]; }
#define W1_RD(X, W, kk)                                                                                            \
      _Pragma("unroll") for (int c = 0; c < 4; ++c) W[c] = lds_tr2(sm, buf + (wa ^ (unsigned)(c * 32)) + kk * 7168, buf + (wa ^ (unsigned)(c * 32)) + kk * 7168 + 512); \
      _Pragma("unroll") for (int t = 0; t < 4; ++t) X[t] = lds_frag(sm, buf + (kk ? xa1 : xa0) + t * 2048);
#define W1_MM(X, W)                                                                                                \
      _Pragma("unroll") for (int t = 0; t < 4; ++t)                                                                \
        _Pragma("unroll") for (int c = 0; c < 4; ++c) acc[t][c] = __builtin_amdgcn_mfma_f32_16x16x32_bf16(W[c], X[t], acc[t][c], 0, 0, 0); \
      _Pragma("unroll") for (int u = 0; u < 12; ++u) { __builtin_amdgcn_sched_group_barrier(0x008, 1, 0); __builtin_amdgcn_sched_group_barrier(0x100, 1, 0); } \
      __builtin_amdgcn_sched_group_barrier(0x008, 4, 0);                                                           \
      asm volatile("" : "+v"(acc[0][0]), "+v"(acc[0][1]), "+v"(acc[0][2]), "+v"(acc[0][3]), "+v"(acc[1][0]), "+v"(acc[1][1]), "+v"(acc[1][2]), "+v"(acc[1][3]), \
                        "+v"(acc[2][0]), "+v"(acc[2][1]), "+v"(acc[2][2]), "+v"(acc[2][3]), "+v"(acc[3][0]), "+v"(acc[3][1]), "+v"(acc[3][2]), "+v"(acc[3][3]) :: "memory"); \
      __builtin_amdgcn_sched_barrier(0);
      unsigned buf = 0;
      BARRIER();
#pragma unroll
      for (int c = 0; c < 4; ++c) {
        const u32x2 bv = *(LAS u32x2*)(sm + L_B1 + (16 * c + 4 * g4) * 2);
#pragma unroll
        for (int t = 0; t < 4; ++t) acc[t][c] = (f32x4){bf_lo(bv.x), bf_hi(bv.x), bf_lo(bv.y), bf_hi(bv.y)};
      }
#pragma unroll 1
      for (int n = 0; n < 8; ++n) {
        W1_RD(xA, wA, 0)
        W1_MM(xB, wB)
        W1_RD(xB, wB, 1)
        W1_MM(xA, wA)
        NEXT_BUF(buf);
        if (n == 2) put_tag();
        if (n < 7) BARRIER();
      }
      BARRIER();
      __builtin_amdgcn_s_setprio(3);
      get_tag();
      put_ones();
      W1_MM(xB, wB)
      STAMP(1);
      u32x4 actv[4][2];
      auto mish1 = [&](int t) __attribute__((always_inline)) {
#pragma unroll
          for (int pc = 0; pc < 2; ++pc) {
            u32x4 o;
#pragma unroll
            for (int e = 0; e < 4; ++e) {
              f32x2 hv = {acc[t][2 * pc + (e >> 1)][2 * (e & 1)], acc[t][2 * pc + (e >> 1)][2 * (e & 1) + 1]};
              f32x2 a = hv * 1.4426950408889634f;
              f32x2 ex = {__builtin_amdgcn_exp2f(a.x), __builtin_amdgcn_exp2f(a.y)};
              f32x2 d = ex * (ex + 2.f) + 2.f;
              f32x2 r = {__builtin_amdgcn_rcpf(d.x), __builtin_amdgcn_rcpf(d.y)};
              f32x2 y = hv * (r * -2.f + 1.f);
              o[e] = pk_bf16(y.x, y.y);
            }
            asm volatile("" : "+v"(o));
            actv[t][pc] = o;
          }
      };
      mish1(0); mish1(1);
      mish1(2);
      const u32x2 fv = __builtin_amdgcn_raw_buffer_load_b64(rws, fkv, 0, AUX_SC1);
      mish1(3);
      STAMP(2);
      gather(fv, [&]() __attribute__((always_inline)) {
        const unsigned ao = WS_ACT + (unsigned)(8 * bi + 2 * j) * 114688u + (unsigned)cw * 4096u + (unsigned)g4 * 512u + l15 * 16u;
#pragma unroll
        for (int t = 0; t < 4; ++t)
#pragma unroll
          for (int pc = 0; pc < 2; ++pc) ST128(actv[t][pc], ao + (t >> 1) * 114688 + pc * 2048 + (t & 1) * 256, 0);
        retile(L_RT + j * 2048u, 4 * cw + j);
        return 10;
      });
    } else {
      f32x4 acc[4];
#pragma unroll
      for (int t = 0; t < 4; ++t) acc[t] = (f32x4){0.f, 0.f, 0.f, 0.f};
      unsigned buf = 0;
#pragma unroll 1
      for (int n = 0; n < 8; ++n) {
        BARRIER();
        if (hd == 7) {
#pragma unroll
          for (int kk = 0; kk < 2; ++kk) {
            const bf16x8 gf = lds_frag(sm, L_G + ((n + rot) & 7) * 128 + kk * 64 + g4 * 16);
#pragma unroll
            for (int t = 0; t < 4; ++t) acc[t] = __builtin_amdgcn_mfma_f32_16x16x32_bf16(gf, lds_frag(sm, buf + (kk ? xa1 : xa0) + t * 2048), acc[t], 0, 0, 0);
          }
        }
        if (n == 2) put_tag();
        if (n == 5 || n == 6) retile(n == 5 ? L_RT + j * 2048u : 2 * STG + 32768u + 1536u + j * 3584u, 112 + (hd >> 3) * 8 + 2 * j + (n - 5));
        NEXT_BUF(buf);
      }
      WAITV(0);
      get_tag();
      BARRIER();
      __builtin_amdgcn_s_setprio(3);
      put_ones();
      STAMP(1);
      if (hd == 7) {
        const float lg = g4 == 0 ? acc[0][0] : g4 == 1 ? acc[1][0] : g4 == 2 ? acc[2][0] : acc[3][0];
        const float gv = __builtin_amdgcn_rcpf(1.f + __builtin_amdgcn_exp2f(lg * -1.4426950408889634f));
        __builtin_amdgcn_raw_buffer_store_b32(__float_as_uint(gv), rws, WS_G + (unsigned)(256 * bi + 64 * j + lane) * 4u, 0, AUX_SC1);
      }
      STAMP(2);
      gather(__builtin_amdgcn_raw_buffer_load_b64(rws, fkv, 0, AUX_SC1), [&]() { return 0; });
    }
    STAMP(3);
    const int tm = hd >> 3, cp = hd & 7;
    if (j == 0) {
      const unsigned o1 = !hh ? WS_FACT + (unsigned)((4 * bi + tm) * 32 + l31) * 8u : WS_FACT + (unsigned)((4 * bi + p4) * 32 + 7) * 8u;
#pragma unroll 1
      for (unsigned s = 0; s < SPIN_CAP; ++s) {
        CFENCE();
        const u32x2 v = __builtin_amdgcn_raw_buffer_load_b64(rws, o1, 0, AUX_SC1);
        const u32x4 y = LD128(WS_FACT + (unsigned)cp * 1024u + lane * 16u, 0);
        if (__all(v.x == tag.x && v.y == tag.y && y[0] == tag.x && y[1] == tag.y && y[2] == tag.x && y[3] == tag.y)) break;
        __builtin_amdgcn_s_sleep(POLL_SLEEP);
      }
      CFENCE();
      if (lane == 0) lds_add(sm, 18, 1u);
    } else lds_wait(sm, 18, 1u);
    STAMP(4);
    {
      *(LAS unsigned*)(sm + L_G + (64 * j + lane) * 4) = __builtin_amdgcn_raw_buffer_load_b32(rws, WS_G + (unsigned)(256 * bi + 64 * j + lane) * 4u, 0, AUX_SC1);
      f32x16 acc[2][2];
#pragma unroll
      for (int t = 0; t < 2; ++t)
#pragma unroll
        for (int c = 0; c < 2; ++c)
#pragma unroll
          for (int r = 0; r < 16; ++r) acc[t][c][r] = 0.f;
      const unsigned ab = WS_ACT + (unsigned)(8 * bi + 2 * tm + (j & 1)) * 114688u + (unsigned)(28 * j) * 1024u;
      const unsigned ab1 = WS_ACT + (unsigned)(8 * bi + 2 * tm + ((j & 1) ^ 1)) * 114688u + (unsigned)(28 * j) * 1024u;
      const unsigned wb = WS_WT + (unsigned)((2 * cp + (j >> 1)) * 144 + 28 * j) * 1024u;
      const unsigned wb1 = WS_WT + (unsigned)((2 * cp + ((j >> 1) ^ 1)) * 144 + 28 * j) * 1024u;
      u32x4 af[4][2], wf[4][2];
#pragma unroll
      for (int u = 0; u < 4; ++u) {
        af[u][0] = LD128(lane * 16 + u * 1024, ab); af[u][1] = LD128(lane * 16 + u * 1024, ab1);
        wf[u][0] = LD128(lane * 16 + u * 1024, wb); wf[u][1] = LD128(lane * 16 + u * 1024, wb1);
      }
      WAITL0();
      if (lane == 0) lds_add(sm, 17, 1u);
#pragma unroll 1
      for (int it = 1; it <= W2_N; ++it) {
        const unsigned nx = (it < 7) ? it * 4096u : 0u;
        if (it == 2) clear_o();
        if (it == 4) {
          WAITV(16);
          if (lane == 0) { __builtin_amdgcn_raw_buffer_store_b64(tag, rws, WS_FCL + (unsigned)((bi * 4 + j) * 32 + hd) * 8u, 0, AUX_SC1); lds_add(sm, 22, 1u); }
        }
        const rsrc_t rws = mk_rsrc(ws, it < 7 ? WS_END : 0u);
#pragma unroll
        for (int u = 0; u < 4; ++u) {
#pragma unroll
          for (int t = 0; t < 2; ++t)
#pragma unroll
            for (int c = 0; c < 2; ++c)
              acc[t][c] = __builtin_amdgcn_mfma_f32_32x32x16_bf16(__builtin_bit_cast(bf16x8, wf[u][c]), __builtin_bit_cast(bf16x8, af[u][t]), acc[t][c], 0, 0, 0);
          af[u][0] = LD128(lane * 16 + u * 1024, ab + nx); af[u][1] = LD128(lane * 16 + u * 1024, ab1 + nx);
          wf[u][0] = LD128(lane * 16 + u * 1024, wb + nx); wf[u][1] = LD128(lane * 16 + u * 1024, wb1 + nx);
          __builtin_amdgcn_sched_barrier(0);
        }
      }
      lds_wait(sm, 17, 4u);
#pragma unroll
      for (int q = 0; q < 4; ++q) {
        LAS f32x4* bp = (LAS f32x4*)(sm + L_BP + (unsigned)(q ^ j) * 4096u + lane * 16);
        const f32x16& av = acc[q & 1][q >> 1];
        f32x4 v[4];
#pragma unroll
        for (int u = 0; u < 4; ++u) v[u] = (f32x4){av[4 * u], av[4 * u + 1], av[4 * u + 2], av[4 * u + 3]};
        if (q > 0) {
          lds_wait(sm, 32, 4u * q);
#pragma unroll
          for (int u = 0; u < 4; ++u) v[u] += bp[u * 64];
        }
#pragma unroll
        for (int u = 0; u < 4; ++u) bp[u * 64] = v[u];
        WAITL0();
        if (q < 3 && lane == 0) lds_add(sm, 32, 1u);
      }
    }
    if (j == 1) glb_wait(rws, WS_FCL + (unsigned)((4 * bi + tm) * 32 + l31) * 8u, tag);
    STAMP(5);
    STAMP_OUT();
    if (b == 0 && j == 0) {
#pragma unroll 1
      for (int u = 0; u < 4; ++u) glb_wait(rws, WS_FACT + (unsigned)((2 * u + hh) * 128 + l31) * 8u, tag);
      if (lane == 0) __builtin_amdgcn_raw_buffer_store_b64(tag, rws, WS_EPOCH, 0, AUX_SC1);
    }
    return;
  }

  STAMP(2);
  const int w = wave;
  const unsigned ka = lane * 16;
  const bool vone = l31 >= 28 || (l31 >= 16 && l31 < 20);
  const unsigned va = vone ? 2048u : 1024u + (unsigned)(hh * 16 + (l31 < 16 ? l31 : 0)) * 16u, va1 = vone ? 2048u : va + 512u;
  const int IDX = 0xDDDD;
  const bf16x8 qf = lds_frag(sm, L_Q + w * 1024 + lane * 16);
  f32x16 negm, O;
  {
    f32x16 z;
#pragma unroll
    for (int r = 0; r < 16; ++r) { z[r] = 0.f; O[r] = 0.f; }
    f32x16 s0 = __builtin_amdgcn_mfma_f32_32x32x16_bf16(lds_frag(sm, ka), qf, z, 0, 0, 0);
    float m = s0[0];
#pragma unroll
    for (int r = 1; r < 16; ++r) m = fmaxf(m, s0[r]);
    const auto sw = __builtin_amdgcn_permlane32_swap(__float_as_uint(m), __float_as_uint(m), false, false);
    m = fmaxf(__uint_as_float(sw[0]), __uint_as_float(sw[1]));
#pragma unroll
    for (int r = 0; r < 16; ++r) negm[r] = -m;
  }
  STAMP(7);
  const int tm = hd >> 3, cp = hd & 7;
  const int tt = w & 1, kq = w >> 1;
  u32x4 wf[2][8];
  {
    u32x4 kf, v0, v1;
    unsigned kb = sb + ka, vb = sb + va, vb1 = sb + va1, fb = sb + L_FL + 4, ft;
    int cnt = SPLIT_AT, st, sp = 0x40000;
    if (w < 4) __builtin_amdgcn_s_setprio(2); else __builtin_amdgcn_s_setprio(1);
    asm volatile(ATTN_LOOP
                 : "+v"(O), "=&v"(kf), "=&v"(v0), "=&v"(v1), "+v"(kb), "+v"(vb), "+s"(cnt), "+v"(fb), "=&v"(ft), "=&s"(st), "+s"(sp), "+v"(vb1)
                 : "v"(qf), "v"(negm), "v"(IDX)
                 : ATTN_CLOBS);
    STAMP(12);
    __builtin_amdgcn_s_setprio(WO_PRIO);
    lds_wait(sm, 17, 4u);
    const unsigned wb = WS_WT + (unsigned)(2 * cp * 144 + 112 + 8 * kq) * 1024u;
#pragma unroll
    for (int u = 0; u < 8; ++u) {
      wf[0][u] = LD128(lane * 16 + (u & 3) * 1024, wb + (u >> 2) * 4096);
      wf[1][u] = LD128(lane * 16 + (u & 3) * 1024, wb + 147456 + (u >> 2) * 4096);
    }
    STAMP(13);
    if (w < 4) __builtin_amdgcn_s_setprio(0); else __builtin_amdgcn_s_setprio(1);
    cnt = 8 - SPLIT_AT;
    asm volatile(ATTN_LOOP
                 : "+v"(O), "=&v"(kf), "=&v"(v0), "=&v"(v1), "+v"(kb), "+v"(vb), "+s"(cnt), "+v"(fb), "=&v"(ft), "=&s"(st), "+s"(sp), "+v"(vb1)
                 : "v"(qf), "v"(negm), "v"(IDX)
                 : ATTN_CLOBS);
  }
  STAMP(3);
  float lsum = hh ? O[12] : O[8];
  {
    float chk = lsum * 0.f;
#pragma unroll
    for (int r = 0; r < 8; ++r) chk = __builtin_fmaf(O[r], 0.f, chk);
    if (__any(chk != chk)) {
      float m = -3.0e38f;
      f32x16 z;
#pragma unroll
      for (int r = 0; r < 16; ++r) { z[r] = 0.f; O[r] = 0.f; }
#pragma unroll 1
      for (int t = 0; t < 64; ++t) {
        f32x16 S = __builtin_amdgcn_mfma_f32_32x32x16_bf16(lds_frag(sm, ka + t * TILE), qf, z, 0, 0, 0);
        float mx = m;
#pragma unroll
        for (int r = 0; r < 16; ++r) mx = fmaxf(mx, S[r]);
        mx = fmaxf(mx, __shfl_xor(mx, 32));
        const float al = __builtin_amdgcn_exp2f(m - mx);
        m = mx;
        f32x8 p0, p1;
#pragma unroll
        for (int r = 0; r < 16; ++r) O[r] *= al;
#pragma unroll
        for (int r = 0; r < 8; ++r) { p0[r] = __builtin_amdgcn_exp2f(S[r] - m); p1[r] = __builtin_amdgcn_exp2f(S[8 + r] - m); }
        O = __builtin_amdgcn_smfmac_f32_32x32x32_bf16(lds_frag(sm, va + t * TILE), __builtin_bit_cast(bf16x16, p0), O, IDX, 0, 0);
        O = __builtin_amdgcn_smfmac_f32_32x32x32_bf16(lds_frag(sm, va1 + t * TILE), __builtin_bit_cast(bf16x16, p1), O, IDX, 0, 0);
      }
      lsum = hh ? O[12] : O[8];
    }
  }
  lds_wait(sm, 22, 4u);
  {
    const float sc = *(LAS float*)(sm + L_G + (32 * w + l31) * 4) * __builtin_amdgcn_rcpf(lsum);
    u32x4 o;
#pragma unroll
    for (int e = 0; e < 4; ++e) {
      unsigned u = pk_bf16(O[2 * e] * sc, O[2 * e + 1] * sc);
      o[e] = (u == 0xffffffffu) ? 0x7fc07fc0u : u;
    }
    ST128(o, WS_OF + (unsigned)((8 * bi + w) * 32 + hd) * 1024u + lane * 16u, 0);
  }
  {
    STAMP(4);
    unsigned xwo = (unsigned)(tt * 16 + kq) * 2048u + lane * 16u, xro = (unsigned)(tt * 16 + kq * 4) * 2048u + lane * 16u;
    unsigned oo = (unsigned)(256 * bi + 64 * tm + 32 * tt + l31) * 1024u + (unsigned)(64 * cp + 16 * kq + 8 * hh) * 2u;
    asm volatile("" : "+v"(xwo), "+v"(xro), "+v"(oo));
    LAS f32x4* xw = (LAS f32x4*)(sm + xwo);
    LAS f32x4* xr = (LAS f32x4*)(sm + xro);
    BARRIER();
    const unsigned ob = WS_OF + (unsigned)((8 * bi + 2 * tm + tt) * 32 + 8 * kq) * 1024u;
    f32x16 acc[2];
    f32x4 a, c;
#pragma unroll 1
    for (unsigned s = 0; s < SPIN_CAP; ++s) {
      CFENCE();
      u32x4 of[8];
#pragma unroll
      for (int u = 0; u < 8; ++u) { of[u] = LD128(lane * 16 + (u & 3) * 1024, ob + (u >> 2) * 4096); __builtin_amdgcn_sched_barrier(0); }
      const float om = 1.f - *(LAS float*)(sm + L_G + (64 * tm + 32 * tt + l31) * 4);
      LAS f32x4* bp = (LAS f32x4*)(sm + L_BP + ((tt + 2 * (kq >> 1)) * 4 + 2 * (kq & 1)) * 1024 + lane * 16);
      a = bp[0] * om; c = bp[64] * om;
      __builtin_amdgcn_sched_barrier(0);
#pragma unroll
      for (int r = 0; r < 16; ++r) { acc[0][r] = 0.f; acc[1][r] = 0.f; }
      unsigned mx = 0;
#pragma unroll
      for (int u = 0; u < 8; ++u) {
        acc[0] = __builtin_amdgcn_mfma_f32_32x32x16_bf16(__builtin_bit_cast(bf16x8, wf[0][u]), __builtin_bit_cast(bf16x8, of[u]), acc[0], 0, 0, 0);
        acc[1] = __builtin_amdgcn_mfma_f32_32x32x16_bf16(__builtin_bit_cast(bf16x8, wf[1][u]), __builtin_bit_cast(bf16x8, of[u]), acc[1], 0, 0, 0);
        mx = max(max(mx, of[u][0]), of[u][1]);
        mx = max(max(mx, of[u][2]), of[u][3]);
        asm volatile("" : "+v"(acc[0]), "+v"(acc[1]), "+v"(mx));
      }
      if (__all(mx != 0xffffffffu)) break;
#pragma unroll 1
      for (unsigned z = 0; z < 4096; ++z) {
        CFENCE();
        const unsigned v = __builtin_amdgcn_raw_buffer_load_b32(rws, sl8 * 1024 + 1020, ob, AUX_SC1);
        if (__builtin_popcountll(__builtin_amdgcn_ballot_w64(v == 0xffffffffu)) <= P3_LEFT * 8) break;
      }
    }
    STAMP(5);
#pragma unroll
    for (int p = 0; p < 4; ++p) {
      xw[p * 512] = (f32x4){acc[p >> 1][8 * (p & 1)], acc[p >> 1][8 * (p & 1) + 1], acc[p >> 1][8 * (p & 1) + 2], acc[p >> 1][8 * (p & 1) + 3]};
      xw[p * 512 + 64] = (f32x4){acc[p >> 1][8 * (p & 1) + 4], acc[p >> 1][8 * (p & 1) + 5], acc[p >> 1][8 * (p & 1) + 6], acc[p >> 1][8 * (p & 1) + 7]};
    }
    BARRIER();
    f32x4 pv[8];
#pragma unroll
    for (int q = 0; q < 8; ++q) pv[q] = xr[q * 64];
#pragma unroll
    for (int q = 0; q < 4; ++q) { a += pv[2 * q]; c += pv[2 * q + 1]; }
    const u32x4 o = {pk_bf16(a[0], a[1]), pk_bf16(a[2], a[3]), pk_bf16(c[0], c[1]), pk_bf16(c[2], c[3])};
    __builtin_amdgcn_raw_buffer_store_b128(o, mk_rsrc(out, 2048u * 1024u), oo, 0, OUT_AUX);
  }
  STAMP(6);
  STAMP_OUT();
}

extern "C" void kernel_launch(void* const* d_in, const int* in_sizes, int n_in, void* d_out, int out_size,
                              void* d_ws, size_t ws_size, hipStream_t stream) {
  fused_kernel<<<256, 768, 0, stream>>>((const u16*)d_in[0], (const u16*)d_in[1], (const u16*)d_in[2], (const u16*)d_in[4],
                                        (const u16*)d_in[5], (const u16*)d_in[6], (const u16*)d_in[7], (u16*)d_out,
                                        (unsigned char*)d_ws);
}
```

```cpp
#define DRAIN_FIRST
#define P3_MODE 0
#include <hip/hip_runtime.h>
#include <hip/hip_bf16.h>

typedef unsigned short u16;
typedef unsigned long long u64;
typedef __bf16 bf16x8 __attribute__((ext_vector_type(8)));
typedef __bf16 bf16x16 __attribute__((ext_vector_type(16)));
typedef __bf16 bf16x2 __attribute__((ext_vector_type(2)));
typedef short s16x4 __attribute__((ext_vector_type(4)));
typedef float f32x2 __attribute__((ext_vector_type(2)));
typedef float f32x4 __attribute__((ext_vector_type(4)));
typedef float f32x8 __attribute__((ext_vector_type(8)));
typedef float f32x16 __attribute__((ext_vector_type(16)));
typedef unsigned u32x2 __attribute__((ext_vector_type(2)));
typedef unsigned u32x4 __attribute__((ext_vector_type(4)));
#define LAS __attribute__((address_space(3)))
#define GAS __attribute__((address_space(1)))
typedef __amdgpu_buffer_rsrc_t rsrc_t;

constexpr unsigned WS_EPOCH = 0;
constexpr unsigned WS_FKV = 4096;
constexpr unsigned WS_FACT = WS_FKV + 8192;
constexpr unsigned WS_FCL = WS_FACT + 8192;
constexpr unsigned WS_G = 32768;
constexpr unsigned WS_KV = 65536;
constexpr unsigned WS_ACT = WS_KV + 32u * 131072u;
constexpr unsigned WS_WT = WS_ACT + 64u * 114688u;
constexpr unsigned WS_OF = WS_WT + 16u * 144u * 1024u;
constexpr unsigned WS_CK = WS_OF + 64u * 32u * 1024u;
constexpr unsigned WS_END = WS_CK + 32u * 32u * 512u;
constexpr unsigned WS_DIAG = WS_END;

constexpr unsigned STG = 47104;
constexpr unsigned TILE = 2064;
constexpr unsigned L_RT = 3 * STG;
constexpr unsigned L_BP = 133120;
constexpr unsigned L_Q = L_BP + 16384;
constexpr unsigned L_B1 = L_Q + 8192;
constexpr unsigned L_G = 158720;
constexpr unsigned L_FL = L_G + 1024;
constexpr unsigned L_END = L_FL + 512;

constexpr unsigned SPIN_CAP = 1u << 15;
constexpr int AUX_SC1 = 16;
#define POLL_SLEEP 4

__device__ __forceinline__ unsigned pk_bf16(float a, float b) {
  f32x2 v = {a, b};
  return __builtin_bit_cast(unsigned, __builtin_convertvector(v, bf16x2));
}
__device__ __forceinline__ unsigned ck_mix(u32x4 a, u32x4 b, unsigned seed) {
  unsigned s = seed + a[0] + (a[1] ^ 0x9E3779B9u);
  s = ((s << 7) | (s >> 25)) ^ a[2] ^ b[0];
  s = s + a[3] + b[1];
  s = ((s << 11) | (s >> 21)) ^ b[2];
  return s * 0x85EBCA6Bu + b[3];
}
__device__ __forceinline__ float bf_lo(unsigned u) { return __uint_as_float(u << 16); }
__device__ __forceinline__ float bf_hi(unsigned u) { return __uint_as_float(u & 0xffff0000u); }
__device__ __forceinline__ rsrc_t mk_rsrc(const void* p, unsigned bytes) {
  return __builtin_amdgcn_make_buffer_rsrc((void*)p, 0, bytes, 0x00020000);
}
__device__ __forceinline__ u32x4 mk_desc(const void* p, unsigned bytes) {
  const u64 a = (u64)p;
  return (u32x4){(unsigned)a, (unsigned)(a >> 32) & 0xffffu, bytes, 0x00020000u};
}
__device__ __forceinline__ void dma16(u32x4 desc, unsigned lds_addr, unsigned voff, unsigned soff) {
  asm volatile("s_mov_b32 m0, %0\n\ts_nop 3\n\tbuffer_load_dwordx4 %1, %2, %3 offen lds"
               :: "s"(lds_addr), "v"(voff), "s"(desc), "s"(soff) : "memory");
}
__device__ __forceinline__ void dma16x2_sc1(u32x4 desc, unsigned lds_addr, unsigned voff, unsigned soff) {
  asm volatile("s_mov_b32 m0, %0\n\ts_nop 3\n\tbuffer_load_dwordx4 %1, %2, %3 offen sc1 lds\n\tbuffer_load_dwordx4 %1, %2, %3 offen offset:1024 sc1 lds"
               :: "s"(lds_addr), "v"(voff), "s"(desc), "s"(soff) : "memory");
}
template <int OFF>
__device__ __forceinline__ void dma16_next(u32x4 desc, unsigned voff, unsigned soff) {
  asm volatile("buffer_load_dwordx4 %0, %1, %2 offen offset:%3 lds" :: "v"(voff), "s"(desc), "s"(soff), "i"(OFF) : "memory");
}
#define BARRIER() asm volatile("s_waitcnt lgkmcnt(0)\n\ts_barrier" ::: "memory")
#define WAITV(n) asm volatile("s_waitcnt vmcnt(" #n ")" ::: "memory")
#define WAITL0() asm volatile("s_waitcnt lgkmcnt(0)" ::: "memory")
#define CFENCE() asm volatile("" ::: "memory")
#define LD128(off, soff) __builtin_amdgcn_raw_buffer_load_b128(rws, off, soff, AUX_SC1)
#define ST128(v, off, soff) __builtin_amdgcn_raw_buffer_store_b128(v, rws, off, soff, AUX_SC1)

#define STAMP(k) do {} while (0)
#define STAMP_OUT() do {} while (0)
#define ACC_BEG() do {} while (0)
#define ACC_END(a) do {} while (0)

#define STAMP2(k) do {} while (0)
__device__ __forceinline__ bf16x8 lds_frag(LAS char* sm, unsigned off) { return *(LAS bf16x8*)(sm + off); }
__device__ __forceinline__ bf16x8 lds_tr2(LAS char* sm, unsigned o0, unsigned o1) {
  s16x4 a = __builtin_amdgcn_ds_read_tr16_b64_v4i16((LAS s16x4*)(sm + o0));
  s16x4 b = __builtin_amdgcn_ds_read_tr16_b64_v4i16((LAS s16x4*)(sm + o1));
  return __builtin_bit_cast(bf16x8, __builtin_shufflevector(a, b, 0, 1, 2, 3, 4, 5, 6, 7));
}
__device__ __forceinline__ void lds_add(LAS char* sm, unsigned idx, unsigned v) {
  __hip_atomic_fetch_add((LAS unsigned*)(sm + L_FL) + idx, v, __ATOMIC_RELAXED, __HIP_MEMORY_SCOPE_WORKGROUP);
}
__device__ __forceinline__ void lds_wait(LAS char* sm, unsigned idx, unsigned want) {
#pragma unroll 1
  for (unsigned s = 0; s < SPIN_CAP * 4; ++s) {
    CFENCE();
    unsigned v = __hip_atomic_load((LAS unsigned*)(sm + L_FL) + idx, __ATOMIC_RELAXED, __HIP_MEMORY_SCOPE_WORKGROUP);
    if ((unsigned)__builtin_amdgcn_readfirstlane(v) >= want) break;
    __builtin_amdgcn_s_sleep(1);
  }
  CFENCE();
}
__device__ __forceinline__ void glb_wait(rsrc_t rs, unsigned off, u32x2 tag) {
#pragma unroll 1
  for (unsigned s = 0; s < SPIN_CAP; ++s) {
    CFENCE();
    u32x2 v = __builtin_amdgcn_raw_buffer_load_b64(rs, off, 0, AUX_SC1);
    if (__all(v.x == tag.x && v.y == tag.y)) break;
    __builtin_amdgcn_s_sleep(POLL_SLEEP);
  }
  CFENCE();
}

#define AE(b, i) "v_exp_f32 v[" #b "+" #i "], v[" #b "+" #i "]\n"
#define ATILE(cur, oth, t)                                                                                          \
  AE(cur, 0) AE(cur, 1)                                                                                              \
  "s_waitcnt lgkmcnt(2)\n v_smfmac_f32_32x32x32_bf16 %0, %3, v[" #oth "+8:" #oth "+15], %14\n"                       \
  "ds_read_b128 %3, %11 offset:2064*" #t "\n"                                                                        \
  AE(cur, 2) AE(cur, 3) AE(cur, 4) AE(cur, 5)                                                                        \
  "s_waitcnt lgkmcnt(2)\n v_mfma_f32_32x32x16_bf16 v[" #oth ":" #oth "+15], %1, %12, %13\n"                          \
  "ds_read_b128 %1, %4 offset:2064*(" #t "+2)\n"                                                                     \
  AE(cur, 6) AE(cur, 7) AE(cur, 8) AE(cur, 9) AE(cur, 10)                                                            \
  "s_waitcnt lgkmcnt(2)\n v_smfmac_f32_32x32x32_bf16 %0, %2, v[" #cur ":" #cur "+7], %14\n"                          \
  "ds_read_b128 %2, %5 offset:2064*(" #t "+1)\n"                                                                     \
  AE(cur, 11) AE(cur, 12) AE(cur, 13) AE(cur, 14) AE(cur, 15)
#define ATTN_LOOP                                                                                                   \
  "ds_read_b128 %1, %4\n ds_read_b128 %3, %5\n"                                                                      \
  "v_mov_b32 v120, 0\n v_mov_b32 v121, 0\n v_mov_b32 v122, 0\n v_mov_b32 v123, 0\n"                                  \
  "v_mov_b32 v124, 0\n v_mov_b32 v125, 0\n v_mov_b32 v126, 0\n v_mov_b32 v127, 0\n"                                  \
  "s_waitcnt lgkmcnt(0)\n v_mfma_f32_32x32x16_bf16 v[96:111], %1, %12, %13\n"                                        \
  "ds_read_b128 %1, %4 offset:2064\n ds_read_b128 %2, %5\n s_nop 11\n"                                               \
  "1:\n"                                                                                                             \
  ATILE(96, 112, 0) ATILE(112, 96, 1) ATILE(96, 112, 2) ATILE(112, 96, 3) ATILE(96, 112, 4)                          \
  "ds_read_b32 %8, %7\n"                                                                                             \
  ATILE(112, 96, 5)                                                                                                  \
  "v_readfirstlane_b32 %9, %8\n s_nop 3\n s_cmp_eq_u32 %9, 8\n s_cbranch_scc1 3f\n"         \
  "2:\n s_sleep 1\n ds_read_b32 %8, %7\n s_waitcnt lgkmcnt(0)\n v_readfirstlane_b32 %9, %8\n s_sub_u32 %10, %10, 1\n" \
  "s_cmp_le_i32 %10, 0\n s_cbranch_scc1 3f\n s_cmp_eq_u32 %9, 8\n s_cbranch_scc0 2b\n"                               \
  "3:\n"                                                                                                             \
  ATILE(96, 112, 6) ATILE(112, 96, 7)                                                                                \
  "v_add_u32 %4, 8*2064, %4\n v_add_u32 %5, 8*2064, %5\n v_add_u32 %11, 8*2064, %11\n v_add_u32 %7, 4, %7\n s_sub_u32 %6, %6, 1\n"                \
  "s_cmp_lg_u32 %6, 4\n s_cbranch_scc1 4f\n v_mov_b32 %8, 1\n s_mov_b64 exec, 1\n ds_add_u32 %7, %8 offset:80\n s_mov_b64 exec, -1\n 4:\n" \
  "s_cmp_lg_u32 %6, %15\n s_cbranch_scc1 5f\n s_setprio 0\n 5:\n"                                                    \
  "s_cmp_lg_u32 %6, 0\n s_cbranch_scc1 1b\n"                                                                         \
  "s_waitcnt lgkmcnt(0)\n v_smfmac_f32_32x32x32_bf16 %0, %3, v[120:127], %14\n s_nop 15\n"
#define ATTN_CLOBS "v96","v97","v98","v99","v100","v101","v102","v103","v104","v105","v106","v107","v108","v109","v110","v111", \
  "v112","v113","v114","v115","v116","v117","v118","v119","v120","v121","v122","v123","v124","v125","v126","v127","scc","memory"
#define CODE_BYTES 17408u
#define W2_N 7
#define OUT_AUX 0
#define ROTEXPR (((hd + bi) & 1) * 4)
#define WO_PRIO 3
#define KVOFF(h, g) (WS_KV + (unsigned)((h) * 32 + (g)) * 4096u)
#define FAST_N 2
#define ACT_AT 4
#define PACE_N 15
#define PACE2(n) WAITV(n)
#define PACE1(n) PACE2(n)
#define PACE() PACE1(PACE_N)
#define W2_LOW_AT 1
#define W2_LOW 1
#define W2_HOLD 99
#define W2_HOLD_N 9u
#define PA1 2
#define PB1 1
#define PA2 0
#define PB2 1
#define WO_AT 7
#define PRIO_LEFT 2
#define GATE_WAIT 16
#define RT_AT 5
#define P3_LEFT 0
#define P3_SLEEP 4
#define P3_HOFF (r8 * 1024 + 1020)
#define SPLIT_AT 6

__global__ void __launch_bounds__(768, 3)
fused_kernel(const u16* __restrict__ x, const u16* __restrict__ wqkv, const u16* __restrict__ wo,
             const u16* __restrict__ w1, const u16* __restrict__ b1, const u16* __restrict__ w2,
             const u16* __restrict__ gw, u16* __restrict__ out, unsigned char* __restrict__ ws) {
  __shared__ __attribute__((aligned(16))) char smem_[L_END];
  LAS char* sm = (LAS char*)smem_;
  const unsigned sb = (unsigned)(size_t)sm;
  u64 pc0;
  asm volatile("s_getpc_b64 %0" : "=s"(pc0));
  pc0 &= ~255ull;
  asm volatile("" :: "s"(x), "s"(wqkv), "s"(wo), "s"(w1), "s"(b1), "s"(w2), "s"(gw), "s"(out), "s"(ws));
  const int lane = threadIdx.x & 63;
  const int wave = __builtin_amdgcn_readfirstlane(threadIdx.x >> 6);
  const int b = blockIdx.x, xcd = b & 7, idx = b >> 3;
  const int bi = 4 * (xcd & 1) + (idx & 3), hd = 8 * (xcd >> 1) + (idx >> 2);
  const bool has_w1 = (hd & 7) != 7;
  const int cw = hd - (hd >> 3);
  const int rot = ROTEXPR;
  const int hh = lane >> 5, l31 = lane & 31, l15 = lane & 15, g4 = lane >> 4;
  const int q4 = l15 >> 2, p4 = lane & 3, r8 = lane >> 3, sl8 = lane & 7;
  if (wave < 8) {
    const u32x4 dx = mk_desc(x, 2048u * 1024u);
    const unsigned xo0 = (unsigned)(256 * bi + 32 * wave + r8) * 1024u + (unsigned)(sl8 ^ (r8 >> 1)) * 16u, xo1 = (xo0 ^ 64u) + 7168u;
    dma16(dx, sb + wave * 4096u, xo0, rot * 128u);
    dma16_next<1024>(dx, xo1, rot * 128u);
    dma16_next<2048>(dx, xo0, rot * 128u + 14336u);
    dma16_next<3072>(dx, xo1, rot * 128u + 14336u);
  }
  __builtin_amdgcn_sched_barrier(0);
  const rsrc_t rws = mk_rsrc(ws, WS_END);
  const u32x4 ones = {0x3f803f80u, 0x3f803f80u, 0x3f803f80u, 0x3f803f80u};
  STAMP(0);

  auto mk_tag = [&](u32x2 e) {
    u64 z = ((u64)(unsigned)__builtin_amdgcn_readfirstlane(e.y) << 32 | (u64)(unsigned)__builtin_amdgcn_readfirstlane(e.x)) + 0x9E3779B97F4A7C15ull;
    z = (z ^ (z >> 30)) * 0xBF58476D1CE4E5B9ull;
    z = (z ^ (z >> 27)) * 0x94D049BB133111EBull;
    z ^= z >> 31;
    return (u32x2){(unsigned)z, (unsigned)(z >> 32)};
  };
  u32x2 tag;
  auto get_tag = [&]() {
    const u32x2 t = *(LAS u32x2*)(sm + L_FL + 256);
    tag = (u32x2){(unsigned)__builtin_amdgcn_readfirstlane(t.x), (unsigned)__builtin_amdgcn_readfirstlane(t.y)};
  };
  const unsigned xa0 = (unsigned)(64 * (wave & 3) + l15) * 128u + (unsigned)(g4 ^ (l15 >> 1)) * 16u;
  const unsigned xa1 = xa0 ^ 64u;
#define NEXT_BUF(v) v = (v == 2 * STG) ? 0 : v + STG
  const unsigned qo = (unsigned)(16 * (wave & 3) + (((lane >> 1) & 15) ^ ((g4 & 1) << 2))) * 3072u + (unsigned)hh * 1024u + (unsigned)hd * 32u + (lane & 1) * 16u;
  auto qkv_issue = [&](int n, unsigned buf) {
    const u32x4 d = mk_desc(wqkv, 512u * 3072u);
    const unsigned so = ((unsigned)(n + rot) & 7u) * (64u * 3072u);
    dma16(d, sb + buf + 32768u + (wave & 3) * 3584u, qo, so);
    if (lane < 32) dma16_next<1024>(d, qo, so + 1024u);
  };

  if (__builtin_expect(wave >= 4 && wave < 8, 1)) {
    const int lw = wave - 4;
    const u32x4 dx = mk_desc(x, 2048u * 1024u);
    const unsigned xo0 = (unsigned)(256 * bi + 64 * lw + r8) * 1024u + (unsigned)(sl8 ^ (r8 >> 1)) * 16u;
    const unsigned xo1 = (xo0 ^ 64u) + 8192u - 1024u;
    const u32x4 d1 = mk_desc(w1, 512u * 3584u);
    const unsigned wo0 = (unsigned)(16 * lw + r8) * 3584u + (unsigned)cw * 128u + (unsigned)(sl8 ^ (r8 & 2)) * 16u;
    const unsigned wo1 = (wo0 ^ 64u) + 8u * 3584u - 1024u;
    unsigned buf = 0;
    STAMP(16);
#pragma unroll 1
    for (int n = 0; n < 8; ++n) {
      const unsigned ks = (unsigned)(n + rot) & 7u, d = sb + buf + lw * 8192u;
      if (n < 2) qkv_issue(n, buf);
      if (n)
#pragma unroll
      for (int u = 0; u < 2; ++u) {
        dma16(dx, d + u * 4096u, xo0, ks * 128u + u * 32768u);
        dma16_next<1024>(dx, xo1, ks * 128u + u * 32768u);
        dma16_next<2048>(dx, xo0, ks * 128u + u * 32768u + 16384u - 2048u);
        dma16_next<3072>(dx, xo1, ks * 128u + u * 32768u + 16384u - 2048u);
      }
      if (has_w1) {
        dma16(d1, sb + buf + 32768u + 1536u + lw * 3584u, wo0, ks * (64u * 3584u));
        dma16_next<1024>(d1, wo1, ks * (64u * 3584u));
      }
      if (n == 0 && lw == 0) {
        if (has_w1) { if (lane < 8) dma16(mk_desc(b1, 3584u), sb + L_B1, lane * 16u + (unsigned)cw * 128u, 0u); }
        else dma16(mk_desc(gw, 1024u), sb + L_G, lane * 16u, 0u);
      }
      if (n == RT_AT) {
        if (has_w1) {
          const u32x4 d2 = mk_desc(w2, 1792u * 1024u);
          const unsigned o2 = (unsigned)(16 * (r8 >> 2) + (r8 & 3)) * 1024u + (unsigned)bi * 128u + (unsigned)sl8 * 16u;
          const unsigned so = (unsigned)(64 * cw + 32 * (lw >> 1) + 8 * (lw & 1)) * 1024u;
          dma16(d2, sb + L_RT + lw * 2048u, o2, so); dma16_next<1024>(d2, o2, so + 3072u);
        } else {
          const u32x4 dO = mk_desc(wo, 512u * 1024u);
          const unsigned oo = (unsigned)r8 * 1024u + (unsigned)bi * 128u + (unsigned)sl8 * 16u;
          const unsigned so = (unsigned)((hd >> 3) * 8 + 2 * lw) * 16384u;
          dma16(dO, sb + L_RT + lw * 2048u, oo, so); dma16_next<1024>(dO, oo, so + 7168u);
          dma16(dO, sb + 2 * STG + 32768u + 1536u + lw * 3584u, oo, so + 16384u); dma16_next<1024>(dO, oo, so + 16384u + 7168u);
        }
      }
      NEXT_BUF(buf);
      if (n >= 1) {
        if (n == RT_AT) WAITV(12); else if (n == 1) { if (has_w1) WAITV(12); else WAITV(10); } else if (has_w1) WAITV(10); else WAITV(8);
        if (n == 1) STAMP(20);
        BARRIER();
        if (n == 1) STAMP(24);
      }
    }
    if (RT_AT == 7) WAITV(2); else WAITV(0);
    BARRIER();
    if (lane < 2) *(LAS u32x4*)(sm + (2 * lw + lane) * TILE + 2048) = ones;
    WAITV(0); BARRIER();
  } else if (wave < 4) {
    const int j = wave;
    __builtin_amdgcn_s_setprio(3);
    if (wave == 0) ((LAS unsigned*)(sm + L_FL))[lane] = 0u;
    f32x4 acc[4][3];
#pragma unroll
    for (int t = 0; t < 4; ++t)
#pragma unroll
      for (int c = 0; c < 3; ++c) acc[t][c] = (f32x4){0.f, 0.f, 0.f, 0.f};
    const unsigned wa0 = 32768u + (unsigned)(g4 >> 1) * 3584u + (unsigned)(12 * (g4 & 1) + q4) * 32u + p4 * 8u;
    const unsigned wa1 = wa0 ^ 128u;
    bf16x8 xA[4], wA[3], xB[4], wB[3];
#pragma unroll
    for (int t = 0; t < 4; ++t) xB[t] = __builtin_bit_cast(bf16x8, (u32x4){0u, 0u, 0u, 0u});
#pragma unroll
    for (int c = 0; c < 3; ++c) wB[c] = __builtin_bit_cast(bf16x8, (u32x4){0u, 0u, 0u, 0u});
#define QKV_RD(X, W, kk)                                                                                           \
    _Pragma("unroll") for (int c = 0; c < 3; ++c) W[c] = lds_tr2(sm, buf + wa0 + kk * 7168 + c * 512, buf + wa1 + kk * 7168 + c * 512); \
    _Pragma("unroll") for (int t = 0; t < 4; ++t) X[t] = lds_frag(sm, buf + (kk ? xa1 : xa0) + t * 2048);
#define QKV_MM(X, W)                                                                                               \
    _Pragma("unroll") for (int t = 0; t < 4; ++t) {                                                                \
      acc[t][0] = __builtin_amdgcn_mfma_f32_16x16x32_bf16(W[0], X[t], acc[t][0], 0, 0, 0);                         \
      acc[t][1] = __builtin_amdgcn_mfma_f32_16x16x32_bf16(W[1], X[t], acc[t][1], 0, 0, 0);                         \
      acc[t][2] = __builtin_amdgcn_mfma_f32_16x16x32_bf16(X[t], W[2], acc[t][2], 0, 0, 0);                         \
    }                                                                                                              \
    _Pragma("unroll") for (int u = 0; u < 10; ++u) { __builtin_amdgcn_sched_group_barrier(0x008, 1, 0); __builtin_amdgcn_sched_group_barrier(0x100, 1, 0); } \
    __builtin_amdgcn_sched_group_barrier(0x008, 2, 0);                                                             \
    asm volatile("" : "+v"(acc[0][0]), "+v"(acc[0][1]), "+v"(acc[0][2]), "+v"(acc[1][0]), "+v"(acc[1][1]), "+v"(acc[1][2]), \
                      "+v"(acc[2][0]), "+v"(acc[2][1]), "+v"(acc[2][2]), "+v"(acc[3][0]), "+v"(acc[3][1]), "+v"(acc[3][2]) :: "memory"); \
    __builtin_amdgcn_sched_barrier(0);
    unsigned vo = 1024u + (unsigned)(g4 & 1) * 512u + (unsigned)((g4 >> 1) * 16 + (l15 & 3) + 8 * ((l15 >> 2) & 1) + 4 * (l15 >> 3)) * 16u;
    unsigned eq = L_Q + 2 * j * 1024 + lane * 16, ek = 2 * j * TILE + lane * 16, ev = 2 * j * TILE + vo;
    unsigned go = KVOFF(hd, bi * 4 + j);
    asm volatile("" : "+v"(vo), "+v"(eq), "+v"(ek), "+v"(ev), "+s"(go));
    unsigned buf = 0, nbuf = 2 * STG;
    WAITV(0);
#pragma unroll 1
    for (int n = 0; n < 8; ++n) {
      BARRIER();
      STAMP(16 + n);
      QKV_RD(xA, wA, 0)
      QKV_MM(xB, wB)
      if (n < 6) qkv_issue(n + 2, nbuf);
      QKV_RD(xB, wB, 1)
      QKV_MM(xA, wA)
      NEXT_BUF(buf); NEXT_BUF(nbuf);
      if (n < 6) WAITV(2); else WAITV(0);
    }
    QKV_MM(xB, wB)
    STAMP(1);
    u32x4 pub[3][2];
#pragma unroll
    for (int c = 2; c >= 0; --c)
#pragma unroll
      for (int tp = 0; tp < 2; ++tp) {
        const float sc = c ? 1.f : 0.25f * 1.4426950408889634f;
        const f32x4 a = acc[2 * tp][c] * sc, e = acc[2 * tp + 1][c] * sc;
        const auto r0 = __builtin_amdgcn_permlane16_swap(pk_bf16(a[0], a[1]), pk_bf16(e[0], e[1]), false, false);
        const auto r1 = __builtin_amdgcn_permlane16_swap(pk_bf16(a[2], a[3]), pk_bf16(e[2], e[3]), false, false);
        pub[c][tp] = (u32x4){r0[0], r1[0], r0[1], r1[1]};
        if (c) ST128(pub[c][tp], (c == 1 ? lane * 16u : vo) + tp * 2048, go);
        *(LAS u32x4*)(sm + (c == 0 ? eq + tp * 1024 : (c == 1 ? ek : ev) + tp * TILE)) = pub[c][tp];
      }
    asm volatile("" :: "v"(pub[1][0]), "v"(pub[1][1]), "v"(pub[2][0]), "v"(pub[2][1]));
    STAMP(10);
    BARRIER();
    get_tag();
    {
      const unsigned co = WS_CK + (unsigned)(hd * 32 + bi * 4 + j) * 512u;
      __builtin_amdgcn_raw_buffer_store_b32(ck_mix(pub[1][0], pub[1][1], tag.x), rws, lane * 8u, co, AUX_SC1);
      __builtin_amdgcn_raw_buffer_store_b32(ck_mix(pub[2][0], pub[2][1], tag.y), rws, ((vo - 1024u) >> 1) + 4u, co, AUX_SC1);
    }
    WAITV(0);
    if (lane == 0) __builtin_amdgcn_raw_buffer_store_b64(tag, rws, WS_FKV + (unsigned)((hd * 8 + bi) * 4 + j) * 8u, 0, AUX_SC1);
    STAMP(11);
  } else {
    const int j = wave - 8;
    u32x2 ep = {0u, 0u};
    if (j == 0) ep = __builtin_amdgcn_raw_buffer_load_b64(rws, WS_EPOCH, 0, AUX_SC1);
    if (j == 1 && lane < 5)
      ep.x = __builtin_amdgcn_raw_buffer_load_b32(mk_rsrc((const void*)pc0, CODE_BYTES), (unsigned)(lane * 32 + idx) * 128u, 0, 0);
    auto put_tag = [&]() { if (j == 0) *(LAS u32x2*)(sm + L_FL + 256) = mk_tag(ep); else asm volatile("" :: "v"(ep)); };
    auto retile = [&](unsigned img, int ks) {
      const unsigned ta = img + (unsigned)(8 * hh + q4) * 128u + (unsigned)(4 * (g4 & 1) + 2 * (p4 & 1) + (p4 >> 1)) * 8u;
      ST128(__builtin_bit_cast(u32x4, lds_tr2(sm, ta, ta + 512)), WS_WT + lane * 16, (2 * bi * 144 + ks) * 1024);
      ST128(__builtin_bit_cast(u32x4, lds_tr2(sm, ta + 64, ta + 576)), WS_WT + lane * 16, ((2 * bi + 1) * 144 + ks) * 1024);
    };
    auto clear_o = [&]() {
      const u32x4 inv = {0xffffffffu, 0xffffffffu, 0xffffffffu, 0xffffffffu};
      const unsigned oo = WS_OF + (unsigned)((8 * bi + 2 * j) * 32 + hd) * 1024u + lane * 16u;
      ST128(inv, oo, 0); ST128(inv, oo + 32768, 0);
    };
    const unsigned fkv = WS_FKV + (unsigned)((hd * 8 + ((bi + lane) & 7)) * 4 + j) * 8u;
    u32x4 fd[FAST_N][4];
    u32x2 fc[FAST_N];
    auto fast_load = [&](int u) __attribute__((always_inline)) {
      const unsigned g = (unsigned)(((bi + 1 + u) & 7) * 4 + j);
      fc[u] = __builtin_amdgcn_raw_buffer_load_b64(rws, lane * 8u, WS_CK + (unsigned)(hd * 32 + g) * 512u, AUX_SC1);
#pragma unroll
      for (int e = 0; e < 4; ++e) fd[u][e] = LD128(lane * 16 + e * 1024, KVOFF(hd, g));
    };
    auto fast_take = [&]() __attribute__((always_inline)) {
#pragma unroll
      for (int u = 0; u < FAST_N; ++u) {
#pragma unroll 1
        for (unsigned s = 0; s < SPIN_CAP; ++s) {
          if (__all(ck_mix(fd[u][0], fd[u][2], tag.x) == fc[u].x && ck_mix(fd[u][1], fd[u][3], tag.y) == fc[u].y)) break;
          CFENCE();
#pragma unroll
          for (int v = u; v < FAST_N; ++v) fast_load(v);
        }
        LAS char* dst = sm + ((1 + u) * 8 + 2 * j) * TILE + lane * 16;
#pragma unroll
        for (int e = 0; e < 4; ++e) *(LAS u32x4*)(dst + (e >> 1) * TILE + (e & 1) * 1024) = fd[u][e];
        WAITL0();
        if (lane == 0) lds_add(sm, 1 + u, 2u);
        if (u == 0) STAMP(7);
      }
    };
    auto gather = [&](auto pre) __attribute__((always_inline)) {
      u32x2 fv = __builtin_amdgcn_raw_buffer_load_b64(rws, fkv, 0, AUX_SC1);
      fast_take();
      const u32x4 dws = mk_desc(ws, WS_END);
      unsigned spins = 0;
      bool done = false;
#pragma unroll 1
      for (int n0 = 1 + FAST_N; n0 < 8;) {
        const unsigned rdy = ((unsigned)__builtin_amdgcn_ballot_w64(fv.x == tag.x && fv.y == tag.y) & 0xffu) >> n0;
        int cnt = __builtin_ctz(~rdy);
        if (++spins > SPIN_CAP) cnt = 8 - n0;
        CFENCE();
#pragma unroll 1
        for (int u = n0; u < n0 + cnt; ++u) {
          const unsigned go = KVOFF(hd, ((bi + u) & 7) * 4 + j), d = sb + (unsigned)(u * 8 + 2 * j) * TILE;
          const unsigned gu = __builtin_amdgcn_readfirstlane(go), du = __builtin_amdgcn_readfirstlane(d);
          dma16x2_sc1(dws, du, lane * 16u, gu);
          dma16x2_sc1(dws, du + TILE, lane * 16u, gu + 2048u);
        }
        if (n0 + cnt < 8) {
          if (!cnt) __builtin_amdgcn_s_sleep(POLL_SLEEP);
          fv = __builtin_amdgcn_raw_buffer_load_b64(rws, fkv, 0, AUX_SC1);
        }
        WAITV(0);
        if (lane < cnt) lds_add(sm, n0 + lane, 2u);
        n0 += cnt;
        if (!done && n0 > ACT_AT) {
          pre();
          WAITV(0);
          if (lane == 0) __builtin_amdgcn_raw_buffer_store_b64(tag, rws, WS_FACT + (unsigned)((bi * 4 + j) * 32 + hd) * 8u, 0, AUX_SC1);
          STAMP(6);
          done = true;
        }
      }
    };
    auto put_ones = [&]() { if (lane < 14) *(LAS u32x4*)(sm + ((1 + (lane >> 1)) * 8 + 2 * j + (lane & 1)) * TILE + 2048) = ones; };
    if (has_w1) {
      f32x4 acc[4][4];
      const unsigned wa = 32768u + 1536u + (unsigned)(g4 >> 1) * 3584u + (unsigned)(8 * (g4 & 1) + q4) * 128u +
                          (unsigned)((q4 >> 1) | ((g4 & 1) << 1)) * 32u + p4 * 8u;
      bf16x8 xA[4], wA[4], xB[4], wB[4];
#pragma unroll
      for (int t = 0; t < 4; ++t) { xB[t] = __builtin_bit_cast(bf16x8, (u32x4){0u, 0u, 0u, 0u}); wB[t] = xB[t]; }
#define W1_RD(X, W, kk)                                                                                            \
      _Pragma("unroll") for (int c = 0; c < 4; ++c) W[c] = lds_tr2(sm, buf + (wa ^ (unsigned)(c * 32)) + kk * 7168, buf + (wa ^ (unsigned)(c * 32)) + kk * 7168 + 512); \
      _Pragma("unroll") for (int t = 0; t < 4; ++t) X[t] = lds_frag(sm, buf + (kk ? xa1 : xa0) + t * 2048);
#define W1_MM(X, W)                                                                                                \
      _Pragma("unroll") for (int t = 0; t < 4; ++t)                                                                \
        _Pragma("unroll") for (int c = 0; c < 4; ++c) acc[t][c] = __builtin_amdgcn_mfma_f32_16x16x32_bf16(W[c], X[t], acc[t][c], 0, 0, 0); \
      _Pragma("unroll") for (int u = 0; u < 12; ++u) { __builtin_amdgcn_sched_group_barrier(0x008, 1, 0); __builtin_amdgcn_sched_group_barrier(0x100, 1, 0); } \
      __builtin_amdgcn_sched_group_barrier(0x008, 4, 0);                                                           \
      asm volatile("" : "+v"(acc[0][0]), "+v"(acc[0][1]), "+v"(acc[0][2]), "+v"(acc[0][3]), "+v"(acc[1][0]), "+v"(acc[1][1]), "+v"(acc[1][2]), "+v"(acc[1][3]), \
                        "+v"(acc[2][0]), "+v"(acc[2][1]), "+v"(acc[2][2]), "+v"(acc[2][3]), "+v"(acc[3][0]), "+v"(acc[3][1]), "+v"(acc[3][2]), "+v"(acc[3][3]) :: "memory"); \
      __builtin_amdgcn_sched_barrier(0);
      unsigned buf = 0;
      BARRIER();
#pragma unroll
      for (int c = 0; c < 4; ++c) {
        const u32x2 bv = *(LAS u32x2*)(sm + L_B1 + (16 * c + 4 * g4) * 2);
#pragma unroll
        for (int t = 0; t < 4; ++t) acc[t][c] = (f32x4){bf_lo(bv.x), bf_hi(bv.x), bf_lo(bv.y), bf_hi(bv.y)};
      }
#pragma unroll 1
      for (int n = 0; n < 8; ++n) {
        W1_RD(xA, wA, 0)
        W1_MM(xB, wB)
        W1_RD(xB, wB, 1)
        W1_MM(xA, wA)
        NEXT_BUF(buf);
        if (n == 2) put_tag();
        if (n < 7) BARRIER();
      }
      BARRIER();
      __builtin_amdgcn_s_setprio(3);
      get_tag();
      put_ones();
      W1_MM(xB, wB)
      STAMP(1);
      u32x4 actv[4][2];
      auto mish1 = [&](int t) __attribute__((always_inline)) {
#pragma unroll
          for (int pc = 0; pc < 2; ++pc) {
            u32x4 o;
#pragma unroll
            for (int e = 0; e < 4; ++e) {
              f32x2 hv = {acc[t][2 * pc + (e >> 1)][2 * (e & 1)], acc[t][2 * pc + (e >> 1)][2 * (e & 1) + 1]};
              f32x2 a = hv * 1.4426950408889634f;
              f32x2 ex = {__builtin_amdgcn_exp2f(a.x), __builtin_amdgcn_exp2f(a.y)};
              f32x2 d = ex * (ex + 2.f) + 2.f;
              f32x2 r = {__builtin_amdgcn_rcpf(d.x), __builtin_amdgcn_rcpf(d.y)};
              f32x2 y = hv * (r * -2.f + 1.f);
              o[e] = pk_bf16(y.x, y.y);
            }
            asm volatile("" : "+v"(o));
            actv[t][pc] = o;
          }
      };
      mish1(0); mish1(1);
      mish1(2);
#pragma unroll
      for (int u = 0; u < FAST_N; ++u) fast_load(u);
      mish1(3);
      STAMP(2);
      gather([&]() __attribute__((always_inline)) {
        const unsigned ao = WS_ACT + (unsigned)(8 * bi + 2 * j) * 114688u + (unsigned)cw * 4096u + (unsigned)g4 * 512u + l15 * 16u;
#pragma unroll
        for (int t = 0; t < 4; ++t)
#pragma unroll
          for (int pc = 0; pc < 2; ++pc) { ST128(actv[t][pc], ao + (t >> 1) * 114688 + pc * 2048 + (t & 1) * 256, 0); PACE(); }
        retile(L_RT + j * 2048u, 4 * cw + j);
        return 10;
      });
    } else {
      f32x4 acc[4];
#pragma unroll
      for (int t = 0; t < 4; ++t) acc[t] = (f32x4){0.f, 0.f, 0.f, 0.f};
      unsigned buf = 0;
#pragma unroll 1
      for (int n = 0; n < 8; ++n) {
        BARRIER();
        if (hd == 7) {
#pragma unroll
          for (int kk = 0; kk < 2; ++kk) {
            const bf16x8 gf = lds_frag(sm, L_G + ((n + rot) & 7) * 128 + kk * 64 + g4 * 16);
#pragma unroll
            for (int t = 0; t < 4; ++t) acc[t] = __builtin_amdgcn_mfma_f32_16x16x32_bf16(gf, lds_frag(sm, buf + (kk ? xa1 : xa0) + t * 2048), acc[t], 0, 0, 0);
          }
        }
        if (n == 2) put_tag();
        if (n == 5 || n == 6) retile(n == 5 ? L_RT + j * 2048u : 2 * STG + 32768u + 1536u + j * 3584u, 112 + (hd >> 3) * 8 + 2 * j + (n - 5));
        NEXT_BUF(buf);
      }
      WAITV(0);
      get_tag();
      BARRIER();
      __builtin_amdgcn_s_setprio(3);
      put_ones();
      STAMP(1);
      if (hd == 7) {
        const float lg = g4 == 0 ? acc[0][0] : g4 == 1 ? acc[1][0] : g4 == 2 ? acc[2][0] : acc[3][0];
        const float gv = __builtin_amdgcn_rcpf(1.f + __builtin_amdgcn_exp2f(lg * -1.4426950408889634f));
        __builtin_amdgcn_raw_buffer_store_b32(__float_as_uint(gv), rws, WS_G + (unsigned)(256 * bi + 64 * j + lane) * 4u, 0, AUX_SC1);
      }
      STAMP(2);
      __builtin_amdgcn_s_sleep(GATE_WAIT);
#pragma unroll
      for (int u = 0; u < FAST_N; ++u) fast_load(u);
      gather([&]() { return 0; });
    }
    STAMP(3);
    clear_o();
    WAITV(0);
    if (lane == 0) { __builtin_amdgcn_raw_buffer_store_b64(tag, rws, WS_FCL + (unsigned)((bi * 4 + j) * 32 + hd) * 8u, 0, AUX_SC1); lds_add(sm, 8, 2u); }
    const int tm = hd >> 3, cp = hd & 7;
    if (j == 0) {
      const unsigned o1 = !hh ? WS_FACT + (unsigned)((4 * bi + tm) * 32 + l31) * 8u : WS_FACT + (unsigned)((4 * bi + p4) * 32 + 7) * 8u;
#pragma unroll 1
      for (unsigned s = 0; s < SPIN_CAP; ++s) {
        CFENCE();
        const u32x2 v = __builtin_amdgcn_raw_buffer_load_b64(rws, o1, 0, AUX_SC1);
        const u32x4 y = LD128(WS_FACT + (unsigned)cp * 1024u + lane * 16u, 0);
        if (__all(v.x == tag.x && v.y == tag.y && y[0] == tag.x && y[1] == tag.y && y[2] == tag.x && y[3] == tag.y)) break;
        __builtin_amdgcn_s_sleep(POLL_SLEEP);
      }
      CFENCE();
      if (lane == 0) lds_add(sm, 18, 1u);
    } else {
      if (b == 0 && j == 2) {
#pragma unroll 1
        for (int u = 0; u < 4; ++u) glb_wait(rws, WS_FKV + (unsigned)(64 * u + lane) * 32u, tag);
        if (lane == 0) __builtin_amdgcn_raw_buffer_store_b64(tag, rws, WS_EPOCH, 0, AUX_SC1);
      }
      lds_wait(sm, 18, 1u);
    }
    STAMP(4);
    {
      *(LAS unsigned*)(sm + L_G + (64 * j + lane) * 4) = __builtin_amdgcn_raw_buffer_load_b32(rws, WS_G + (unsigned)(256 * bi + 64 * j + lane) * 4u, 0, AUX_SC1);
      f32x16 acc[2][2];
#pragma unroll
      for (int t = 0; t < 2; ++t)
#pragma unroll
        for (int c = 0; c < 2; ++c)
#pragma unroll
          for (int r = 0; r < 16; ++r) acc[t][c][r] = 0.f;
      const unsigned ab = WS_ACT + (unsigned)(8 * bi + 2 * tm) * 114688u + (unsigned)(28 * j) * 1024u;
      const unsigned ab1 = WS_ACT + (unsigned)(8 * bi + 2 * tm + 1) * 114688u + (unsigned)(28 * j) * 1024u;
      const unsigned wb = WS_WT + (unsigned)((2 * cp) * 144 + 28 * j) * 1024u;
      const unsigned wb1 = WS_WT + (unsigned)((2 * cp + 1) * 144 + 28 * j) * 1024u;
      u32x4 af[4][2], wf[4][2];
#pragma unroll
      for (int u = 0; u < 4; ++u) {
        af[u][0] = LD128(lane * 16 + u * 1024, ab); af[u][1] = LD128(lane * 16 + u * 1024, ab1);
        wf[u][0] = LD128(lane * 16 + u * 1024, wb); wf[u][1] = LD128(lane * 16 + u * 1024, wb1);
      }
      WAITL0();
      if (lane == 0) lds_add(sm, 17, 1u);
#pragma unroll 1
      for (int it = 1; it <= W2_N; ++it) {
        const unsigned nx = (it < 7) ? it * 4096u : 0u;
        if (it == W2_LOW_AT) __builtin_amdgcn_s_setprio(W2_LOW);
        if (it == W2_HOLD) { __builtin_amdgcn_s_setprio(0); lds_wait(sm, 21, W2_HOLD_N); __builtin_amdgcn_s_setprio(2); }
        if (it == WO_AT) {
          lds_wait(sm, 25, 8u);
          const u32x4 dws = mk_desc(ws, WS_END);
#pragma unroll 1
          for (int u = 0; u < 8; ++u)
            dma16x2_sc1(dws, sb + (unsigned)(j * 16 + 2 * u) * 1024u, lane * 16u, WS_WT + (unsigned)((2 * cp + (u >> 2)) * 144 + 112 + 8 * j + 2 * (u & 3)) * 1024u);
        }
        if (it == 4 && j == 1) {
          glb_wait(rws, WS_FCL + (unsigned)((4 * bi + tm) * 32 + l31) * 8u, tag);
          if (lane == 0) lds_add(sm, 24, 1u);
        }
        const rsrc_t rws = mk_rsrc(ws, it < 7 ? WS_END : 0u);
#pragma unroll
        for (int u = 0; u < 4; ++u) {
#pragma unroll
          for (int t = 0; t < 2; ++t)
#pragma unroll
            for (int c = 0; c < 2; ++c)
              acc[t][c] = __builtin_amdgcn_mfma_f32_32x32x16_bf16(__builtin_bit_cast(bf16x8, wf[u][c]), __builtin_bit_cast(bf16x8, af[u][t]), acc[t][c], 0, 0, 0);
          af[u][0] = LD128(lane * 16 + u * 1024, ab + nx); af[u][1] = LD128(lane * 16 + u * 1024, ab1 + nx);
          wf[u][0] = LD128(lane * 16 + u * 1024, wb + nx); wf[u][1] = LD128(lane * 16 + u * 1024, wb1 + nx);
          __builtin_amdgcn_sched_barrier(0);
        }
      }
      STAMP(8);
      WAITV(0);
      if (lane == 0) lds_add(sm, 24, 1u);
      __builtin_amdgcn_s_setprio(0);
      lds_wait(sm, 21, 8u);
      __builtin_amdgcn_s_setprio(3);
      LAS f32x4* xw = (LAS f32x4*)(sm + 65536 + j * 2048 + lane * 16);
#pragma unroll
      for (int u = 0; u < 16; ++u) {
        const f32x16& av = acc[u >> 3][(u >> 2) & 1];
        xw[(u >> 1) * 512 + (u & 1) * 64] = (f32x4){av[4 * (u & 3)], av[4 * (u & 3) + 1], av[4 * (u & 3) + 2], av[4 * (u & 3) + 3]};
      }
    }
    STAMP(5);
    STAMP_OUT();
    BARRIER();
    return;
  }

  STAMP(2);
  const int w = wave;
  const unsigned ka = lane * 16;
  const bool vone = l31 >= 16;
  const unsigned va = vone ? 2048u : 1024u + (unsigned)(hh * 16 + (l31 < 16 ? l31 : 0)) * 16u, va1 = vone ? 2048u : va + 512u;
  const int IDX = 0xDDDD;
  const bf16x8 qf = lds_frag(sm, L_Q + w * 1024 + lane * 16);
  f32x16 negm, O;
  {
    f32x16 z;
#pragma unroll
    for (int r = 0; r < 16; ++r) { z[r] = 0.f; O[r] = 0.f; }
    f32x16 s0 = __builtin_amdgcn_mfma_f32_32x32x16_bf16(lds_frag(sm, ka), qf, z, 0, 0, 0);
    float m = s0[0];
#pragma unroll
    for (int r = 1; r < 16; ++r) m = fmaxf(m, s0[r]);
    const auto sw = __builtin_amdgcn_permlane32_swap(__float_as_uint(m), __float_as_uint(m), false, false);
    m = fmaxf(__uint_as_float(sw[0]), __uint_as_float(sw[1]));
#pragma unroll
    for (int r = 0; r < 16; ++r) negm[r] = -m;
  }
  STAMP(7);
  const int tm = hd >> 3, cp = hd & 7;
  const int tt = w & 1, kq = w >> 1;
  unsigned xwo = (unsigned)(tt * 16 + kq) * 2048u + lane * 16u, xro = (unsigned)(tt * 16 + kq * 4) * 2048u + lane * 16u;
  unsigned oo = (unsigned)(256 * bi + 64 * tm + 32 * tt + l31) * 1024u + (unsigned)(64 * cp + 16 * kq + 8 * hh) * 2u;
  unsigned wfo = (unsigned)kq * 16384u + lane * 16u, gao = L_G + (unsigned)(64 * tm + 32 * tt + l31) * 4u;
  unsigned osb = WS_OF + (unsigned)((8 * bi + w) * 32 + hd) * 1024u, ob = WS_OF + (unsigned)((8 * bi + 2 * tm + tt) * 32 + 8 * kq) * 1024u;
  asm volatile("" : "+v"(xwo), "+v"(xro), "+v"(oo), "+v"(wfo), "+v"(gao), "+s"(osb), "+s"(ob));
  {
    u32x4 kf, v0, v1;
    unsigned kb = sb + ka, vb = sb + va, vb1 = sb + va1, fb = sb + L_FL + 4, ft;
    int cnt = 8, st, sp = 0x40000, sw = (w < 4) ? PRIO_LEFT : -1;
    if (w < 4) __builtin_amdgcn_s_setprio(2); else __builtin_amdgcn_s_setprio(1);
    asm volatile(ATTN_LOOP
                 : "+v"(O), "=&v"(kf), "=&v"(v0), "=&v"(v1), "+v"(kb), "+v"(vb), "+s"(cnt), "+v"(fb), "=&v"(ft), "=&s"(st), "+s"(sp), "+v"(vb1)
                 : "v"(qf), "v"(negm), "v"(IDX), "s"(sw)
                 : ATTN_CLOBS);
  }
  __builtin_amdgcn_s_setprio(3);
  STAMP(3);
  u32x4 wf[2][8];
  const unsigned rdy = __hip_atomic_load((LAS unsigned*)(sm + L_FL) + 24, __ATOMIC_RELAXED, __HIP_MEMORY_SCOPE_WORKGROUP);
  CFENCE();
  float gv = *(LAS float*)(sm + gao);
#pragma unroll
  for (int u = 0; u < 16; ++u) wf[u >> 3][u & 7] = *(LAS u32x4*)(sm + wfo + u * 1024);
  CFENCE();
  float lsum = O[8];
  {
    float chk = lsum * 0.f;
#pragma unroll
    for (int r = 0; r < 8; ++r) chk = __builtin_fmaf(O[r], 0.f, chk);
    if (__any(chk != chk)) {
      get_tag();
      glb_wait(rws, WS_FKV + (unsigned)(hd * 32 + l31) * 8u, tag);
      float m = -3.0e38f;
      f32x16 z;
#pragma unroll
      for (int r = 0; r < 16; ++r) { z[r] = 0.f; O[r] = 0.f; }
#pragma unroll 1
      for (int t = 0; t < 64; ++t) {
        const unsigned go = KVOFF(hd, t >> 1) + (t & 1) * 2048u, vg = 1024u + (unsigned)(hh * 16 + l15) * 16u;
        const u32x4 kg = LD128(lane * 16, go);
        u32x4 va0 = LD128(vg, go), va1 = LD128(vg + 512, go);
        if (l31 >= 16) { va0 = ones; va1 = ones; }
        f32x16 S = __builtin_amdgcn_mfma_f32_32x32x16_bf16(__builtin_bit_cast(bf16x8, kg), qf, z, 0, 0, 0);
        float mx = m;
#pragma unroll
        for (int r = 0; r < 16; ++r) mx = fmaxf(mx, S[r]);
        mx = fmaxf(mx, __shfl_xor(mx, 32));
        const float al = __builtin_amdgcn_exp2f(m - mx);
        m = mx;
        f32x8 p0, p1;
#pragma unroll
        for (int r = 0; r < 16; ++r) O[r] *= al;
#pragma unroll
        for (int r = 0; r < 8; ++r) { p0[r] = __builtin_amdgcn_exp2f(S[r] - m); p1[r] = __builtin_amdgcn_exp2f(S[8 + r] - m); }
        O = __builtin_amdgcn_smfmac_f32_32x32x32_bf16(__builtin_bit_cast(bf16x8, va0), __builtin_bit_cast(bf16x16, p0), O, IDX, 0, 0);
        O = __builtin_amdgcn_smfmac_f32_32x32x32_bf16(__builtin_bit_cast(bf16x8, va1), __builtin_bit_cast(bf16x16, p1), O, IDX, 0, 0);
      }
      lsum = O[8];
    }
  }
  {
    const float sc = __builtin_amdgcn_rcpf(lsum);
    u32x4 o;
#pragma unroll
    for (int e = 0; e < 4; ++e) o[e] = min(pk_bf16(O[2 * e] * sc, O[2 * e + 1] * sc), 0xfffffffeu);
    ST128(o, lane * 16u, osb);
  }
  {
    STAMP(4);
    LAS f32x4* xw = (LAS f32x4*)(sm + xwo);
    LAS f32x4* xr = (LAS f32x4*)(sm + xro);
    if ((unsigned)__builtin_amdgcn_readfirstlane(rdy) < 5u) {
      __builtin_amdgcn_s_setprio(0);
      lds_wait(sm, 24, 5u);
      __builtin_amdgcn_s_setprio(3);
      gv = *(LAS float*)(sm + gao);
#pragma unroll
      for (int u = 0; u < 16; ++u) wf[u >> 3][u & 7] = *(LAS u32x4*)(sm + wfo + u * 1024);
    }
    WAITL0();
    if (lane == 0) lds_add(sm, 21, 1u);
    __builtin_amdgcn_s_setprio(0); lds_wait(sm, 21, 8u); __builtin_amdgcn_s_setprio(3);
    WAITV(0);
    STAMP(14);
    f32x16 acc[2];
#pragma unroll 1
    for (unsigned s = 0; s < SPIN_CAP; ++s) {
      CFENCE();
      u32x4 of[8];
#pragma unroll
      for (int u = 0; u < 8; ++u) { of[u] = LD128(lane * 16 + (u & 3) * 1024, ob + (u >> 2) * 4096); __builtin_amdgcn_sched_barrier(0); }
      __builtin_amdgcn_sched_barrier(0);
#pragma unroll
      for (int r = 0; r < 16; ++r) { acc[0][r] = 0.f; acc[1][r] = 0.f; }
      unsigned mx = 0;
#pragma unroll
      for (int u = 0; u < 8; ++u) {
        acc[0] = __builtin_amdgcn_mfma_f32_32x32x16_bf16(__builtin_bit_cast(bf16x8, wf[0][u]), __builtin_bit_cast(bf16x8, of[u]), acc[0], 0, 0, 0);
        acc[1] = __builtin_amdgcn_mfma_f32_32x32x16_bf16(__builtin_bit_cast(bf16x8, wf[1][u]), __builtin_bit_cast(bf16x8, of[u]), acc[1], 0, 0, 0);
        mx = max(max(mx, of[u][0]), of[u][1]);
        mx = max(max(mx, of[u][2]), of[u][3]);
        asm volatile("" : "+v"(acc[0]), "+v"(acc[1]), "+v"(mx));
      }
      if (__all(mx != 0xffffffffu)) break;
    }
    STAMP(5);
#pragma unroll
    for (int p = 0; p < 4; ++p) {
      xw[p * 512] = (f32x4){acc[p >> 1][8 * (p & 1)], acc[p >> 1][8 * (p & 1) + 1], acc[p >> 1][8 * (p & 1) + 2], acc[p >> 1][8 * (p & 1) + 3]};
      xw[p * 512 + 64] = (f32x4){acc[p >> 1][8 * (p & 1) + 4], acc[p >> 1][8 * (p & 1) + 5], acc[p >> 1][8 * (p & 1) + 6], acc[p >> 1][8 * (p & 1) + 7]};
    }
    STAMP(15);
    BARRIER();
    STAMP(9);
    f32x4 pv[8], bv[8];
#pragma unroll
    for (int q = 0; q < 8; ++q) bv[q] = xr[4096 + q * 64];
#pragma unroll
    for (int q = 0; q < 8; ++q) pv[q] = xr[q * 64];
    const float om = 1.f - gv;
    const f32x4 a = ((bv[0] + bv[2]) + (bv[4] + bv[6])) * om + ((pv[0] + pv[2]) + (pv[4] + pv[6])) * gv;
    const f32x4 c = ((bv[1] + bv[3]) + (bv[5] + bv[7])) * om + ((pv[1] + pv[3]) + (pv[5] + pv[7])) * gv;
    const u32x4 o = {pk_bf16(a[0], a[1]), pk_bf16(a[2], a[3]), pk_bf16(c[0], c[1]), pk_bf16(c[2], c[3])};
    __builtin_amdgcn_raw_buffer_store_b128(o, mk_rsrc(out, 2048u * 1024u), oo, 0, OUT_AUX);
  }
  STAMP(6);
  STAMP_OUT();
}

extern "C" void kernel_launch(void* const* d_in, const int* in_sizes, int n_in, void* d_out, int out_size,
                              void* d_ws, size_t ws_size, hipStream_t stream) {
  fused_kernel<<<256, 768, 0, stream>>>((const u16*)d_in[0], (const u16*)d_in[1], (const u16*)d_in[2], (const u16*)d_in[4],
                                        (const u16*)d_in[5], (const u16*)d_in[6], (const u16*)d_in[7], (u16*)d_out,
                                        (unsigned char*)d_ws);
}
```

```cpp
#define WS_BASE ((ws_size - (18u << 20)) & ~(size_t)0x1fffff)
#define DRAIN_FIRST
#define P3_MODE 0
#include <hip/hip_runtime.h>
#include <hip/hip_bf16.h>

typedef unsigned short u16;
typedef unsigned long long u64;
typedef __bf16 bf16x8 __attribute__((ext_vector_type(8)));
typedef __bf16 bf16x16 __attribute__((ext_vector_type(16)));
typedef __bf16 bf16x2 __attribute__((ext_vector_type(2)));
typedef short s16x4 __attribute__((ext_vector_type(4)));
typedef float f32x2 __attribute__((ext_vector_type(2)));
typedef float f32x4 __attribute__((ext_vector_type(4)));
typedef float f32x8 __attribute__((ext_vector_type(8)));
typedef float f32x16 __attribute__((ext_vector_type(16)));
typedef unsigned u32x2 __attribute__((ext_vector_type(2)));
typedef unsigned u32x4 __attribute__((ext_vector_type(4)));
#define LAS __attribute__((address_space(3)))
#define GAS __attribute__((address_space(1)))
typedef __amdgpu_buffer_rsrc_t rsrc_t;

constexpr unsigned WS_EPOCH = 0;
constexpr unsigned WS_FKV = 4096;
constexpr unsigned WS_FACT = WS_FKV + 8192;
constexpr unsigned WS_FCL = WS_FACT + 8192;
constexpr unsigned WS_G = 32768;
constexpr unsigned WS_KV = 65536;
constexpr unsigned WS_ACT = WS_KV + 32u * 131072u;
constexpr unsigned WS_WT = WS_ACT + 64u * 114688u;
constexpr unsigned WS_OF = WS_WT + 16u * 144u * 1024u;
constexpr unsigned WS_CK = WS_OF + 64u * 32u * 1024u;
constexpr unsigned WS_END = WS_CK + 32u * 32u * 512u;
constexpr unsigned WS_DIAG = WS_END;

constexpr unsigned STG = 47104;
constexpr unsigned TILE = 2064;
constexpr unsigned L_RT = 3 * STG;
constexpr unsigned L_BP = 133120;
constexpr unsigned L_Q = L_BP + 16384;
constexpr unsigned L_B1 = L_Q + 8192;
constexpr unsigned L_G = 158720;
constexpr unsigned L_FL = L_G + 1024;
constexpr unsigned L_END = L_FL + 512;

constexpr unsigned SPIN_CAP = 1u << 15;
constexpr int AUX_SC1 = 16;
#define POLL_SLEEP 4

__device__ __forceinline__ unsigned pk_bf16(float a, float b) {
  f32x2 v = {a, b};
  return __builtin_bit_cast(unsigned, __builtin_convertvector(v, bf16x2));
}
__device__ __forceinline__ unsigned ck_mix(u32x4 a, u32x4 b, unsigned seed) {
  unsigned s = seed + a[0] + (a[1] ^ 0x9E3779B9u);
  s = ((s << 7) | (s >> 25)) ^ a[2] ^ b[0];
  s = s + a[3] + b[1];
  s = ((s << 11) | (s >> 21)) ^ b[2];
  return s * 0x85EBCA6Bu + b[3];
}
__device__ __forceinline__ float bf_lo(unsigned u) { return __uint_as_float(u << 16); }
__device__ __forceinline__ float bf_hi(unsigned u) { return __uint_as_float(u & 0xffff0000u); }
__device__ __forceinline__ rsrc_t mk_rsrc(const void* p, unsigned bytes) {
  return __builtin_amdgcn_make_buffer_rsrc((void*)p, 0, bytes, 0x00020000);
}
__device__ __forceinline__ u32x4 mk_desc(const void* p, unsigned bytes) {
  const u64 a = (u64)p;
  return (u32x4){(unsigned)a, (unsigned)(a >> 32) & 0xffffu, bytes, 0x00020000u};
}
__device__ __forceinline__ void dma16(u32x4 desc, unsigned lds_addr, unsigned voff, unsigned soff) {
  asm volatile("s_mov_b32 m0, %0\n\ts_nop 3\n\tbuffer_load_dwordx4 %1, %2, %3 offen lds"
               :: "s"(lds_addr), "v"(voff), "s"(desc), "s"(soff) : "memory");
}
__device__ __forceinline__ void dma16x2_sc1(u32x4 desc, unsigned lds_addr, unsigned voff, unsigned soff) {
  asm volatile("s_mov_b32 m0, %0\n\ts_nop 3\n\tbuffer_load_dwordx4 %1, %2, %3 offen sc1 lds\n\tbuffer_load_dwordx4 %1, %2, %3 offen offset:1024 sc1 lds"
               :: "s"(lds_addr), "v"(voff), "s"(desc), "s"(soff) : "memory");
}
template <int OFF>
__device__ __forceinline__ void dma16_next(u32x4 desc, unsigned voff, unsigned soff) {
  asm volatile("buffer_load_dwordx4 %0, %1, %2 offen offset:%3 lds" :: "v"(voff), "s"(desc), "s"(soff), "i"(OFF) : "memory");
}
#define BARRIER() asm volatile("s_waitcnt lgkmcnt(0)\n\ts_barrier" ::: "memory")
#define WAITV(n) asm volatile("s_waitcnt vmcnt(" #n ")" ::: "memory")
#define WAITL0() asm volatile("s_waitcnt lgkmcnt(0)" ::: "memory")
#define CFENCE() asm volatile("" ::: "memory")
#define LD128(off, soff) __builtin_amdgcn_raw_buffer_load_b128(rws, off, soff, AUX_SC1)
#define ST128(v, off, soff) __builtin_amdgcn_raw_buffer_store_b128(v, rws, off, soff, AUX_SC1)

#define STAMP(k) do {} while (0)
#define STAMP_OUT() do {} while (0)
#define ACC_BEG() do {} while (0)
#define ACC_END(a) do {} while (0)

#define STAMP2(k) do {} while (0)
__device__ __forceinline__ bf16x8 lds_frag(LAS char* sm, unsigned off) { return *(LAS bf16x8*)(sm + off); }
__device__ __forceinline__ bf16x8 lds_tr2(LAS char* sm, unsigned o0, unsigned o1) {
  s16x4 a = __builtin_amdgcn_ds_read_tr16_b64_v4i16((LAS s16x4*)(sm + o0));
  s16x4 b = __builtin_amdgcn_ds_read_tr16_b64_v4i16((LAS s16x4*)(sm + o1));
  return __builtin_bit_cast(bf16x8, __builtin_shufflevector(a, b, 0, 1, 2, 3, 4, 5, 6, 7));
}
__device__ __forceinline__ void lds_add(LAS char* sm, unsigned idx, unsigned v) {
  __hip_atomic_fetch_add((LAS unsigned*)(sm + L_FL) + idx, v, __ATOMIC_RELAXED, __HIP_MEMORY_SCOPE_WORKGROUP);
}
__device__ __forceinline__ void lds_wait(LAS char* sm, unsigned idx, unsigned want) {
#pragma unroll 1
  for (unsigned s = 0; s < SPIN_CAP * 4; ++s) {
    CFENCE();
    unsigned v = __hip_atomic_load((LAS unsigned*)(sm + L_FL) + idx, __ATOMIC_RELAXED, __HIP_MEMORY_SCOPE_WORKGROUP);
    if ((unsigned)__builtin_amdgcn_readfirstlane(v) >= want) break;
    __builtin_amdgcn_s_sleep(1);
  }
  CFENCE();
}
__device__ __forceinline__ void glb_wait(rsrc_t rs, unsigned off, u32x2 tag) {
#pragma unroll 1
  for (unsigned s = 0; s < SPIN_CAP; ++s) {
    CFENCE();
    u32x2 v = __builtin_amdgcn_raw_buffer_load_b64(rs, off, 0, AUX_SC1);
    if (__all(v.x == tag.x && v.y == tag.y)) break;
    __builtin_amdgcn_s_sleep(POLL_SLEEP);
  }
  CFENCE();
}

#define AE(b, i) "v_exp_f32 v[" #b "+" #i "], v[" #b "+" #i "]\n"
#define ATILE(cur, oth, t)                                                                                          \
  AE(cur, 0) AE(cur, 1)                                                                                              \
  "s_waitcnt lgkmcnt(2)\n v_smfmac_f32_32x32x32_bf16 %0, %3, v[" #oth "+8:" #oth "+15], %14\n"                       \
  "ds_read_b128 %3, %11 offset:2064*" #t "\n"                                                                        \
  AE(cur, 2) AE(cur, 3) AE(cur, 4) AE(cur, 5)                                                                        \
  "s_waitcnt lgkmcnt(2)\n v_mfma_f32_32x32x16_bf16 v[" #oth ":" #oth "+15], %1, %12, %13\n"                          \
  "ds_read_b128 %1, %4 offset:2064*(" #t "+2)\n"                                                                     \
  AE(cur, 6) AE(cur, 7) AE(cur, 8) AE(cur, 9) AE(cur, 10)                                                            \
  "s_waitcnt lgkmcnt(2)\n v_smfmac_f32_32x32x32_bf16 %0, %2, v[" #cur ":" #cur "+7], %14\n"                          \
  "ds_read_b128 %2, %5 offset:2064*(" #t "+1)\n"                                                                     \
  AE(cur, 11) AE(cur, 12) AE(cur, 13) AE(cur, 14) AE(cur, 15)
#define ATTN_LOOP                                                                                                   \
  "ds_read_b128 %1, %4\n ds_read_b128 %3, %5\n"                                                                      \
  "v_mov_b32 v120, 0\n v_mov_b32 v121, 0\n v_mov_b32 v122, 0\n v_mov_b32 v123, 0\n"                                  \
  "v_mov_b32 v124, 0\n v_mov_b32 v125, 0\n v_mov_b32 v126, 0\n v_mov_b32 v127, 0\n"                                  \
  "s_waitcnt lgkmcnt(0)\n v_mfma_f32_32x32x16_bf16 v[96:111], %1, %12, %13\n"                                        \
  "ds_read_b128 %1, %4 offset:2064\n ds_read_b128 %2, %5\n s_nop 11\n"                                               \
  "1:\n"                                                                                                             \
  ATILE(96, 112, 0) ATILE(112, 96, 1) ATILE(96, 112, 2) ATILE(112, 96, 3) ATILE(96, 112, 4)                          \
  "ds_read_b32 %8, %7\n"                                                                                             \
  ATILE(112, 96, 5)                                                                                                  \
  "v_readfirstlane_b32 %9, %8\n s_nop 3\n s_cmp_eq_u32 %9, 8\n s_cbranch_scc1 3f\n"         \
  "2:\n s_sleep 1\n ds_read_b32 %8, %7\n s_waitcnt lgkmcnt(0)\n v_readfirstlane_b32 %9, %8\n s_sub_u32 %10, %10, 1\n" \
  "s_cmp_le_i32 %10, 0\n s_cbranch_scc1 3f\n s_cmp_eq_u32 %9, 8\n s_cbranch_scc0 2b\n"                               \
  "3:\n"                                                                                                             \
  ATILE(96, 112, 6) ATILE(112, 96, 7)                                                                                \
  "v_add_u32 %4, 8*2064, %4\n v_add_u32 %5, 8*2064, %5\n v_add_u32 %11, 8*2064, %11\n v_add_u32 %7, 4, %7\n s_sub_u32 %6, %6, 1\n"                \
  "s_cmp_lg_u32 %6, 4\n s_cbranch_scc1 4f\n v_mov_b32 %8, 1\n s_mov_b64 exec, 1\n ds_add_u32 %7, %8 offset:80\n s_mov_b64 exec, -1\n 4:\n" \
  "s_cmp_lg_u32 %6, %15\n s_cbranch_scc1 5f\n s_setprio 0\n 5:\n"                                                    \
  "s_cmp_lg_u32 %6, 0\n s_cbranch_scc1 1b\n"                                                                         \
  "s_waitcnt lgkmcnt(0)\n v_smfmac_f32_32x32x32_bf16 %0, %3, v[120:127], %14\n s_nop 15\n"
#define ATTN_CLOBS "v96","v97","v98","v99","v100","v101","v102","v103","v104","v105","v106","v107","v108","v109","v110","v111", \
  "v112","v113","v114","v115","v116","v117","v118","v119","v120","v121","v122","v123","v124","v125","v126","v127","scc","memory"
#define CODE_BYTES 19968u
#define CPF_LANES 5
#define CPF_MUL 32
#define W2_N 7
#define OUT_AUX 0
#define ROTEXPR (((hd + bi) & 1) * 4)
#define WO_PRIO 3
#define KVOFF(h, g) (WS_KV + (unsigned)((h) * 32 + (g)) * 4096u)
#define FAST_N 2
#define ACT_AT 4
#define PACE_N 15
#define PACE2(n) WAITV(n)
#define PACE1(n) PACE2(n)
#define PACE() PACE1(PACE_N)
#define W2_LOW_AT 1
#define W2_LOW 1
#define W2_HOLD 99
#define W2_HOLD_N 9u
#define PA1 2
#define PB1 1
#define PA2 0
#define PB2 1
#define WO_AT 7
#define PRIO_LEFT 2
#define GATE_WAIT 16
#define RT_AT 5
#define P3_LEFT 0
#define P3_SLEEP 4
#define P3_HOFF (r8 * 1024 + 1020)
#define SPLIT_AT 6

__global__ void __launch_bounds__(768, 3)
fused_kernel(const u16* __restrict__ x, const u16* __restrict__ wqkv, const u16* __restrict__ wo,
             const u16* __restrict__ w1, const u16* __restrict__ b1, const u16* __restrict__ w2,
             const u16* __restrict__ gw, u16* __restrict__ out, unsigned char* __restrict__ ws) {
  __shared__ __attribute__((aligned(16))) char smem_[L_END];
  LAS char* sm = (LAS char*)smem_;
  const unsigned sb = (unsigned)(size_t)sm;
  asm volatile("" :: "s"(x), "s"(wqkv), "s"(wo), "s"(w1), "s"(b1), "s"(w2), "s"(gw), "s"(out), "s"(ws));
  const int lane = threadIdx.x & 63;
  const int wave = __builtin_amdgcn_readfirstlane(threadIdx.x >> 6);
  const int b = blockIdx.x, xcd = b & 7, idx = b >> 3;
  const int bi = 4 * (xcd & 1) + (idx & 3), hd = 8 * (xcd >> 1) + (idx >> 2);
  const bool has_w1 = (hd & 7) != 7;
  const int cw = hd - (hd >> 3);
  const int rot = ROTEXPR;
  const int hh = lane >> 5, l31 = lane & 31, l15 = lane & 15, g4 = lane >> 4;
  const int q4 = l15 >> 2, p4 = lane & 3, r8 = lane >> 3, sl8 = lane & 7;
  if (wave < 8) {
    const u32x4 dx = mk_desc(x, 2048u * 1024u);
    const unsigned xo0 = (unsigned)(256 * bi + 32 * wave + r8) * 1024u + (unsigned)(sl8 ^ (r8 >> 1)) * 16u, xo1 = (xo0 ^ 64u) + 7168u;
    dma16(dx, sb + wave * 4096u, xo0, rot * 128u);
    dma16_next<1024>(dx, xo1, rot * 128u);
    dma16_next<2048>(dx, xo0, rot * 128u + 14336u);
    dma16_next<3072>(dx, xo1, rot * 128u + 14336u);
  }
  __builtin_amdgcn_sched_barrier(0);
  const rsrc_t rws = mk_rsrc(ws, WS_END);
  const u32x4 ones = {0x3f803f80u, 0x3f803f80u, 0x3f803f80u, 0x3f803f80u};
  STAMP(0);

  auto mk_tag = [&](u32x2 e) {
    u64 z = ((u64)(unsigned)__builtin_amdgcn_readfirstlane(e.y) << 32 | (u64)(unsigned)__builtin_amdgcn_readfirstlane(e.x)) + 0x9E3779B97F4A7C15ull;
    z = (z ^ (z >> 30)) * 0xBF58476D1CE4E5B9ull;
    z = (z ^ (z >> 27)) * 0x94D049BB133111EBull;
    z ^= z >> 31;
    return (u32x2){(unsigned)z, (unsigned)(z >> 32)};
  };
  u32x2 tag;
  auto get_tag = [&]() {
    const u32x2 t = *(LAS u32x2*)(sm + L_FL + 256);
    tag = (u32x2){(unsigned)__builtin_amdgcn_readfirstlane(t.x), (unsigned)__builtin_amdgcn_readfirstlane(t.y)};
  };
  const unsigned xa0 = (unsigned)(64 * (wave & 3) + l15) * 128u + (unsigned)(g4 ^ (l15 >> 1)) * 16u;
  const unsigned xa1 = xa0 ^ 64u;
#define NEXT_BUF(v) v = (v == 2 * STG) ? 0 : v + STG
  const unsigned qo = (unsigned)(16 * (wave & 3) + (((lane >> 1) & 15) ^ ((g4 & 1) << 2))) * 3072u + (unsigned)hh * 1024u + (unsigned)hd * 32u + (lane & 1) * 16u;
  auto qkv_issue = [&](int n, unsigned buf) {
    const u32x4 d = mk_desc(wqkv, 512u * 3072u);
    const unsigned so = ((unsigned)(n + rot) & 7u) * (64u * 3072u);
    dma16(d, sb + buf + 32768u + (wave & 3) * 3584u, qo, so);
    if (lane < 32) dma16_next<1024>(d, qo, so + 1024u);
  };

  if (__builtin_expect(wave >= 4 && wave < 8, 1)) {
    const int lw = wave - 4;
    const u32x4 dx = mk_desc(x, 2048u * 1024u);
    const unsigned xo0 = (unsigned)(256 * bi + 64 * lw + r8) * 1024u + (unsigned)(sl8 ^ (r8 >> 1)) * 16u;
    const unsigned xo1 = (xo0 ^ 64u) + 8192u - 1024u;
    const u32x4 d1 = mk_desc(w1, 512u * 3584u);
    const unsigned wo0 = (unsigned)(16 * lw + r8) * 3584u + (unsigned)cw * 128u + (unsigned)(sl8 ^ (r8 & 2)) * 16u;
    const unsigned wo1 = (wo0 ^ 64u) + 8u * 3584u - 1024u;
    unsigned buf = 0;
    STAMP(16);
#pragma unroll 1
    for (int n = 0; n < 8; ++n) {
      const unsigned ks = (unsigned)(n + rot) & 7u, d = sb + buf + lw * 8192u;
      if (n < 2) qkv_issue(n, buf);
      if (n)
#pragma unroll
      for (int u = 0; u < 2; ++u) {
        dma16(dx, d + u * 4096u, xo0, ks * 128u + u * 32768u);
        dma16_next<1024>(dx, xo1, ks * 128u + u * 32768u);
        dma16_next<2048>(dx, xo0, ks * 128u + u * 32768u + 16384u - 2048u);
        dma16_next<3072>(dx, xo1, ks * 128u + u * 32768u + 16384u - 2048u);
      }
      if (has_w1) {
        dma16(d1, sb + buf + 32768u + 1536u + lw * 3584u, wo0, ks * (64u * 3584u));
        dma16_next<1024>(d1, wo1, ks * (64u * 3584u));
      }
      if (n == 0 && lw == 0) {
        if (has_w1) { if (lane < 8) dma16(mk_desc(b1, 3584u), sb + L_B1, lane * 16u + (unsigned)cw * 128u, 0u); }
        else dma16(mk_desc(gw, 1024u), sb + L_G, lane * 16u, 0u);
      }
      if (n == RT_AT) {
        if (has_w1) {
          const u32x4 d2 = mk_desc(w2, 1792u * 1024u);
          const unsigned o2 = (unsigned)(16 * (r8 >> 2) + (r8 & 3)) * 1024u + (unsigned)bi * 128u + (unsigned)sl8 * 16u;
          const unsigned so = (unsigned)(64 * cw + 32 * (lw >> 1) + 8 * (lw & 1)) * 1024u;
          dma16(d2, sb + L_RT + lw * 2048u, o2, so); dma16_next<1024>(d2, o2, so + 3072u);
        } else {
          const u32x4 dO = mk_desc(wo, 512u * 1024u);
          const unsigned oo = (unsigned)r8 * 1024u + (unsigned)bi * 128u + (unsigned)sl8 * 16u;
          const unsigned so = (unsigned)((hd >> 3) * 8 + 2 * lw) * 16384u;
          dma16(dO, sb + L_RT + lw * 2048u, oo, so); dma16_next<1024>(dO, oo, so + 7168u);
          dma16(dO, sb + 2 * STG + 32768u + 1536u + lw * 3584u, oo, so + 16384u); dma16_next<1024>(dO, oo, so + 16384u + 7168u);
        }
      }
      NEXT_BUF(buf);
      if (n >= 1) {
        if (n == RT_AT) WAITV(12); else if (n == 1) { if (has_w1) WAITV(12); else WAITV(10); } else if (has_w1) WAITV(10); else WAITV(8);
        if (n == 1) STAMP(20);
        BARRIER();
        if (n == 1) STAMP(24);
      }
    }
    if (RT_AT == 7) WAITV(2); else WAITV(0);
    BARRIER();
    if (lane < 2) *(LAS u32x4*)(sm + (2 * lw + lane) * TILE + 2048) = ones;
    WAITV(0); BARRIER();
  } else if (wave < 4) {
    const int j = wave;
    __builtin_amdgcn_s_setprio(3);
    if (wave == 0) ((LAS unsigned*)(sm + L_FL))[lane] = 0u;
    f32x4 acc[4][3];
#pragma unroll
    for (int t = 0; t < 4; ++t)
#pragma unroll
      for (int c = 0; c < 3; ++c) acc[t][c] = (f32x4){0.f, 0.f, 0.f, 0.f};
    const unsigned wa0 = 32768u + (unsigned)(g4 >> 1) * 3584u + (unsigned)(12 * (g4 & 1) + q4) * 32u + p4 * 8u;
    const unsigned wa1 = wa0 ^ 128u;
    bf16x8 xA[4], wA[3], xB[4], wB[3];
#pragma unroll
    for (int t = 0; t < 4; ++t) xB[t] = __builtin_bit_cast(bf16x8, (u32x4){0u, 0u, 0u, 0u});
#pragma unroll
    for (int c = 0; c < 3; ++c) wB[c] = __builtin_bit_cast(bf16x8, (u32x4){0u, 0u, 0u, 0u});
#define QKV_RD(X, W, kk)                                                                                           \
    _Pragma("unroll") for (int c = 0; c < 3; ++c) W[c] = lds_tr2(sm, buf + wa0 + kk * 7168 + c * 512, buf + wa1 + kk * 7168 + c * 512); \
    _Pragma("unroll") for (int t = 0; t < 4; ++t) X[t] = lds_frag(sm, buf + (kk ? xa1 : xa0) + t * 2048);
#define QKV_MM(X, W)                                                                                               \
    _Pragma("unroll") for (int t = 0; t < 4; ++t) {                                                                \
      acc[t][0] = __builtin_amdgcn_mfma_f32_16x16x32_bf16(W[0], X[t], acc[t][0], 0, 0, 0);                         \
      acc[t][1] = __builtin_amdgcn_mfma_f32_16x16x32_bf16(W[1], X[t], acc[t][1], 0, 0, 0);                         \
      acc[t][2] = __builtin_amdgcn_mfma_f32_16x16x32_bf16(X[t], W[2], acc[t][2], 0, 0, 0);                         \
    }                                                                                                              \
    _Pragma("unroll") for (int u = 0; u < 10; ++u) { __builtin_amdgcn_sched_group_barrier(0x008, 1, 0); __builtin_amdgcn_sched_group_barrier(0x100, 1, 0); } \
    __builtin_amdgcn_sched_group_barrier(0x008, 2, 0);                                                             \
    asm volatile("" : "+v"(acc[0][0]), "+v"(acc[0][1]), "+v"(acc[0][2]), "+v"(acc[1][0]), "+v"(acc[1][1]), "+v"(acc[1][2]), \
                      "+v"(acc[2][0]), "+v"(acc[2][1]), "+v"(acc[2][2]), "+v"(acc[3][0]), "+v"(acc[3][1]), "+v"(acc[3][2]) :: "memory"); \
    __builtin_amdgcn_sched_barrier(0);
    unsigned vo = 1024u + (unsigned)(g4 & 1) * 512u + (unsigned)((g4 >> 1) * 16 + (l15 & 3) + 8 * ((l15 >> 2) & 1) + 4 * (l15 >> 3)) * 16u;
    unsigned eq = L_Q + 2 * j * 1024 + lane * 16, ek = 2 * j * TILE + lane * 16, ev = 2 * j * TILE + vo;
    unsigned go = KVOFF(hd, bi * 4 + j);
    asm volatile("" : "+v"(vo), "+v"(eq), "+v"(ek), "+v"(ev), "+s"(go));
    unsigned buf = 0, nbuf = 2 * STG;
    WAITV(0);
#pragma unroll 1
    for (int n = 0; n < 8; ++n) {
      BARRIER();
      STAMP(16 + n);
      QKV_RD(xA, wA, 0)
      QKV_MM(xB, wB)
      if (n < 6) qkv_issue(n + 2, nbuf);
      QKV_RD(xB, wB, 1)
      QKV_MM(xA, wA)
      NEXT_BUF(buf); NEXT_BUF(nbuf);
      if (n < 6) WAITV(2); else WAITV(0);
    }
    QKV_MM(xB, wB)
    STAMP(1);
    u32x4 pub[3][2];
#pragma unroll
    for (int c = 2; c >= 0; --c)
#pragma unroll
      for (int tp = 0; tp < 2; ++tp) {
        const float sc = c ? 1.f : 0.25f * 1.4426950408889634f;
        const f32x4 a = acc[2 * tp][c] * sc, e = acc[2 * tp + 1][c] * sc;
        const auto r0 = __builtin_amdgcn_permlane16_swap(pk_bf16(a[0], a[1]), pk_bf16(e[0], e[1]), false, false);
        const auto r1 = __builtin_amdgcn_permlane16_swap(pk_bf16(a[2], a[3]), pk_bf16(e[2], e[3]), false, false);
        pub[c][tp] = (u32x4){r0[0], r1[0], r0[1], r1[1]};
        if (c) ST128(pub[c][tp], (c == 1 ? lane * 16u : vo) + tp * 2048, go);
        *(LAS u32x4*)(sm + (c == 0 ? eq + tp * 1024 : (c == 1 ? ek : ev) + tp * TILE)) = pub[c][tp];
      }
    asm volatile("" :: "v"(pub[1][0]), "v"(pub[1][1]), "v"(pub[2][0]), "v"(pub[2][1]));
    STAMP(10);
    BARRIER();
    get_tag();
    {
      const unsigned co = WS_CK + (unsigned)(hd * 32 + bi * 4 + j) * 512u;
      __builtin_amdgcn_raw_buffer_store_b32(ck_mix(pub[1][0], pub[1][1], tag.x), rws, lane * 8u, co, AUX_SC1);
      __builtin_amdgcn_raw_buffer_store_b32(ck_mix(pub[2][0], pub[2][1], tag.y), rws, ((vo - 1024u) >> 1) + 4u, co, AUX_SC1);
    }
    WAITV(0);
    if (lane == 0) __builtin_amdgcn_raw_buffer_store_b64(tag, rws, WS_FKV + (unsigned)((hd * 8 + bi) * 4 + j) * 8u, 0, AUX_SC1);
    STAMP(11);
  } else {
    const int j = wave - 8;
    u32x2 ep = {0u, 0u};
    if (j == 0) ep = __builtin_amdgcn_raw_buffer_load_b64(rws, WS_EPOCH, 0, AUX_SC1);
    auto put_tag = [&]() { if (j == 0) *(LAS u32x2*)(sm + L_FL + 256) = mk_tag(ep); else asm volatile("" :: "v"(ep)); };
    auto retile = [&](unsigned img, int ks) {
      const unsigned ta = img + (unsigned)(8 * hh + q4) * 128u + (unsigned)(4 * (g4 & 1) + 2 * (p4 & 1) + (p4 >> 1)) * 8u;
      ST128(__builtin_bit_cast(u32x4, lds_tr2(sm, ta, ta + 512)), WS_WT + lane * 16, (2 * bi * 144 + ks) * 1024);
      ST128(__builtin_bit_cast(u32x4, lds_tr2(sm, ta + 64, ta + 576)), WS_WT + lane * 16, ((2 * bi + 1) * 144 + ks) * 1024);
    };
    auto clear_o = [&]() {
      const u32x4 inv = {0xffffffffu, 0xffffffffu, 0xffffffffu, 0xffffffffu};
      const unsigned oo = WS_OF + (unsigned)((8 * bi + 2 * j) * 32 + hd) * 1024u + lane * 16u;
      ST128(inv, oo, 0); ST128(inv, oo + 32768, 0);
    };
    const unsigned fkv = WS_FKV + (unsigned)((hd * 8 + ((bi + lane) & 7)) * 4 + j) * 8u;
    u32x4 fd[FAST_N][4];
    u32x2 fc[FAST_N];
    auto fast_load = [&](int u) __attribute__((always_inline)) {
      const unsigned g = (unsigned)(((bi + 1 + u) & 7) * 4 + j);
      fc[u] = __builtin_amdgcn_raw_buffer_load_b64(rws, lane * 8u, WS_CK + (unsigned)(hd * 32 + g) * 512u, AUX_SC1);
#pragma unroll
      for (int e = 0; e < 4; ++e) fd[u][e] = LD128(lane * 16 + e * 1024, KVOFF(hd, g));
    };
    auto fast_take = [&]() __attribute__((always_inline)) {
#pragma unroll
      for (int u = 0; u < FAST_N; ++u) {
#pragma unroll 1
        for (unsigned s = 0; s < SPIN_CAP; ++s) {
          if (__all(ck_mix(fd[u][0], fd[u][2], tag.x) == fc[u].x && ck_mix(fd[u][1], fd[u][3], tag.y) == fc[u].y)) break;
          CFENCE();
#pragma unroll
          for (int v = u; v < FAST_N; ++v) fast_load(v);
        }
        LAS char* dst = sm + ((1 + u) * 8 + 2 * j) * TILE + lane * 16;
#pragma unroll
        for (int e = 0; e < 4; ++e) *(LAS u32x4*)(dst + (e >> 1) * TILE + (e & 1) * 1024) = fd[u][e];
        WAITL0();
        if (lane == 0) lds_add(sm, 1 + u, 2u);
        if (u == 0) STAMP(7);
      }
    };
    auto gather = [&](auto pre) __attribute__((always_inline)) {
      u32x2 fv = __builtin_amdgcn_raw_buffer_load_b64(rws, fkv, 0, AUX_SC1);
      fast_take();
      const u32x4 dws = mk_desc(ws, WS_END);
      unsigned spins = 0;
      bool done = false;
#pragma unroll 1
      for (int n0 = 1 + FAST_N; n0 < 8;) {
        const unsigned rdy = ((unsigned)__builtin_amdgcn_ballot_w64(fv.x == tag.x && fv.y == tag.y) & 0xffu) >> n0;
        int cnt = __builtin_ctz(~rdy);
        if (++spins > SPIN_CAP) cnt = 8 - n0;
        CFENCE();
#pragma unroll 1
        for (int u = n0; u < n0 + cnt; ++u) {
          const unsigned go = KVOFF(hd, ((bi + u) & 7) * 4 + j), d = sb + (unsigned)(u * 8 + 2 * j) * TILE;
          const unsigned gu = __builtin_amdgcn_readfirstlane(go), du = __builtin_amdgcn_readfirstlane(d);
          dma16x2_sc1(dws, du, lane * 16u, gu);
          dma16x2_sc1(dws, du + TILE, lane * 16u, gu + 2048u);
        }
        if (n0 + cnt < 8) {
          if (!cnt) __builtin_amdgcn_s_sleep(POLL_SLEEP);
          fv = __builtin_amdgcn_raw_buffer_load_b64(rws, fkv, 0, AUX_SC1);
        }
        WAITV(0);
        if (lane < cnt) lds_add(sm, n0 + lane, 2u);
        n0 += cnt;
        if (!done && n0 > ACT_AT) {
          pre();
          WAITV(0);
          if (lane == 0) __builtin_amdgcn_raw_buffer_store_b64(tag, rws, WS_FACT + (unsigned)((bi * 4 + j) * 32 + hd) * 8u, 0, AUX_SC1);
          STAMP(6);
          done = true;
        }
      }
    };
    auto put_ones = [&]() { if (lane < 14) *(LAS u32x4*)(sm + ((1 + (lane >> 1)) * 8 + 2 * j + (lane & 1)) * TILE + 2048) = ones; };
    if (has_w1) {
      f32x4 acc[4][4];
      const unsigned wa = 32768u + 1536u + (unsigned)(g4 >> 1) * 3584u + (unsigned)(8 * (g4 & 1) + q4) * 128u +
                          (unsigned)((q4 >> 1) | ((g4 & 1) << 1)) * 32u + p4 * 8u;
      bf16x8 xA[4], wA[4], xB[4], wB[4];
#pragma unroll
      for (int t = 0; t < 4; ++t) { xB[t] = __builtin_bit_cast(bf16x8, (u32x4){0u, 0u, 0u, 0u}); wB[t] = xB[t]; }
#define W1_RD(X, W, kk)                                                                                            \
      _Pragma("unroll") for (int c = 0; c < 4; ++c) W[c] = lds_tr2(sm, buf + (wa ^ (unsigned)(c * 32)) + kk * 7168, buf + (wa ^ (unsigned)(c * 32)) + kk * 7168 + 512); \
      _Pragma("unroll") for (int t = 0; t < 4; ++t) X[t] = lds_frag(sm, buf + (kk ? xa1 : xa0) + t * 2048);
#define W1_MM(X, W)                                                                                                \
      _Pragma("unroll") for (int t = 0; t < 4; ++t)                                                                \
        _Pragma("unroll") for (int c = 0; c < 4; ++c) acc[t][c] = __builtin_amdgcn_mfma_f32_16x16x32_bf16(W[c], X[t], acc[t][c], 0, 0, 0); \
      _Pragma("unroll") for (int u = 0; u < 12; ++u) { __builtin_amdgcn_sched_group_barrier(0x008, 1, 0); __builtin_amdgcn_sched_group_barrier(0x100, 1, 0); } \
      __builtin_amdgcn_sched_group_barrier(0x008, 4, 0);                                                           \
      asm volatile("" : "+v"(acc[0][0]), "+v"(acc[0][1]), "+v"(acc[0][2]), "+v"(acc[0][3]), "+v"(acc[1][0]), "+v"(acc[1][1]), "+v"(acc[1][2]), "+v"(acc[1][3]), \
                        "+v"(acc[2][0]), "+v"(acc[2][1]), "+v"(acc[2][2]), "+v"(acc[2][3]), "+v"(acc[3][0]), "+v"(acc[3][1]), "+v"(acc[3][2]), "+v"(acc[3][3]) :: "memory"); \
      __builtin_amdgcn_sched_barrier(0);
      unsigned buf = 0;
      BARRIER();
#pragma unroll
      for (int c = 0; c < 4; ++c) {
        const u32x2 bv = *(LAS u32x2*)(sm + L_B1 + (16 * c + 4 * g4) * 2);
#pragma unroll
        for (int t = 0; t < 4; ++t) acc[t][c] = (f32x4){bf_lo(bv.x), bf_hi(bv.x), bf_lo(bv.y), bf_hi(bv.y)};
      }
#pragma unroll 1
      for (int n = 0; n < 8; ++n) {
        W1_RD(xA, wA, 0)
        W1_MM(xB, wB)
        W1_RD(xB, wB, 1)
        W1_MM(xA, wA)
        NEXT_BUF(buf);
        if (n == 2) put_tag();
        if (n < 7) BARRIER();
      }
      BARRIER();
      __builtin_amdgcn_s_setprio(3);
      get_tag();
      put_ones();
      W1_MM(xB, wB)
      STAMP(1);
      u32x4 actv[4][2];
      auto mish1 = [&](int t) __attribute__((always_inline)) {
#pragma unroll
          for (int pc = 0; pc < 2; ++pc) {
            u32x4 o;
#pragma unroll
            for (int e = 0; e < 4; ++e) {
              f32x2 hv = {acc[t][2 * pc + (e >> 1)][2 * (e & 1)], acc[t][2 * pc + (e >> 1)][2 * (e & 1) + 1]};
              f32x2 a = hv * 1.4426950408889634f;
              f32x2 ex = {__builtin_amdgcn_exp2f(a.x), __builtin_amdgcn_exp2f(a.y)};
              f32x2 d = ex * (ex + 2.f) + 2.f;
              f32x2 r = {__builtin_amdgcn_rcpf(d.x), __builtin_amdgcn_rcpf(d.y)};
              f32x2 y = hv * (r * -2.f + 1.f);
              o[e] = pk_bf16(y.x, y.y);
            }
            asm volatile("" : "+v"(o));
            actv[t][pc] = o;
          }
      };
      mish1(0); mish1(1);
      mish1(2);
#pragma unroll
      for (int u = 0; u < FAST_N; ++u) fast_load(u);
      mish1(3);
      STAMP(2);
      gather([&]() __attribute__((always_inline)) {
        const unsigned ao = WS_ACT + (unsigned)(8 * bi + 2 * j) * 114688u + (unsigned)cw * 4096u + (unsigned)g4 * 512u + l15 * 16u;
#pragma unroll
        for (int t = 0; t < 4; ++t)
#pragma unroll
          for (int pc = 0; pc < 2; ++pc) { ST128(actv[t][pc], ao + (t >> 1) * 114688 + pc * 2048 + (t & 1) * 256, 0); PACE(); }
        retile(L_RT + j * 2048u, 4 * cw + j);
        return 10;
      });
    } else {
      f32x4 acc[4];
#pragma unroll
      for (int t = 0; t < 4; ++t) acc[t] = (f32x4){0.f, 0.f, 0.f, 0.f};
      unsigned buf = 0;
#pragma unroll 1
      for (int n = 0; n < 8; ++n) {
        BARRIER();
        if (hd == 7) {
#pragma unroll
          for (int kk = 0; kk < 2; ++kk) {
            const bf16x8 gf = lds_frag(sm, L_G + ((n + rot) & 7) * 128 + kk * 64 + g4 * 16);
#pragma unroll
            for (int t = 0; t < 4; ++t) acc[t] = __builtin_amdgcn_mfma_f32_16x16x32_bf16(gf, lds_frag(sm, buf + (kk ? xa1 : xa0) + t * 2048), acc[t], 0, 0, 0);
          }
        }
        if (n == 2) put_tag();
        if (n == 5 || n == 6) retile(n == 5 ? L_RT + j * 2048u : 2 * STG + 32768u + 1536u + j * 3584u, 112 + (hd >> 3) * 8 + 2 * j + (n - 5));
        NEXT_BUF(buf);
      }
      WAITV(0);
      get_tag();
      BARRIER();
      __builtin_amdgcn_s_setprio(3);
      put_ones();
      STAMP(1);
      if (hd == 7) {
        const float lg = g4 == 0 ? acc[0][0] : g4 == 1 ? acc[1][0] : g4 == 2 ? acc[2][0] : acc[3][0];
        const float gv = __builtin_amdgcn_rcpf(1.f + __builtin_amdgcn_exp2f(lg * -1.4426950408889634f));
        __builtin_amdgcn_raw_buffer_store_b32(__float_as_uint(gv), rws, WS_G + (unsigned)(256 * bi + 64 * j + lane) * 4u, 0, AUX_SC1);
      }
      STAMP(2);
      __builtin_amdgcn_s_sleep(GATE_WAIT);
#pragma unroll
      for (int u = 0; u < FAST_N; ++u) fast_load(u);
      gather([&]() { return 0; });
    }
    STAMP(3);
    clear_o();
    WAITV(0);
    if (lane == 0) { __builtin_amdgcn_raw_buffer_store_b64(tag, rws, WS_FCL + (unsigned)((bi * 4 + j) * 32 + hd) * 8u, 0, AUX_SC1); lds_add(sm, 8, 2u); }
    const int tm = hd >> 3, cp = hd & 7;
    if (j == 0) {
      const unsigned o1 = !hh ? WS_FACT + (unsigned)((4 * bi + tm) * 32 + l31) * 8u : WS_FACT + (unsigned)((4 * bi + p4) * 32 + 7) * 8u;
#pragma unroll 1
      for (unsigned s = 0; s < SPIN_CAP; ++s) {
        CFENCE();
        const u32x2 v = __builtin_amdgcn_raw_buffer_load_b64(rws, o1, 0, AUX_SC1);
        const u32x4 y = LD128(WS_FACT + (unsigned)cp * 1024u + lane * 16u, 0);
        if (__all(v.x == tag.x && v.y == tag.y && y[0] == tag.x && y[1] == tag.y && y[2] == tag.x && y[3] == tag.y)) break;
        __builtin_amdgcn_s_sleep(POLL_SLEEP);
      }
      CFENCE();
      if (lane == 0) lds_add(sm, 18, 1u);
    } else {
      if (b == 0 && j == 2) {
#pragma unroll 1
        for (int u = 0; u < 4; ++u) glb_wait(rws, WS_FKV + (unsigned)(64 * u + lane) * 32u, tag);
        if (lane == 0) __builtin_amdgcn_raw_buffer_store_b64(tag, rws, WS_EPOCH, 0, AUX_SC1);
      }
      lds_wait(sm, 18, 1u);
    }
    STAMP(4);
    {
      *(LAS unsigned*)(sm + L_G + (64 * j + lane) * 4) = __builtin_amdgcn_raw_buffer_load_b32(rws, WS_G + (unsigned)(256 * bi + 64 * j + lane) * 4u, 0, AUX_SC1);
      f32x16 acc[2][2];
#pragma unroll
      for (int t = 0; t < 2; ++t)
#pragma unroll
        for (int c = 0; c < 2; ++c)
#pragma unroll
          for (int r = 0; r < 16; ++r) acc[t][c][r] = 0.f;
      const unsigned ab = WS_ACT + (unsigned)(8 * bi + 2 * tm) * 114688u + (unsigned)(28 * j) * 1024u;
      const unsigned ab1 = WS_ACT + (unsigned)(8 * bi + 2 * tm + 1) * 114688u + (unsigned)(28 * j) * 1024u;
      const unsigned wb = WS_WT + (unsigned)((2 * cp) * 144 + 28 * j) * 1024u;
      const unsigned wb1 = WS_WT + (unsigned)((2 * cp + 1) * 144 + 28 * j) * 1024u;
      u32x4 af[4][2], wf[4][2];
#pragma unroll
      for (int u = 0; u < 4; ++u) {
        af[u][0] = LD128(lane * 16 + u * 1024, ab); af[u][1] = LD128(lane * 16 + u * 1024, ab1);
        wf[u][0] = LD128(lane * 16 + u * 1024, wb); wf[u][1] = LD128(lane * 16 + u * 1024, wb1);
      }
      WAITL0();
      if (lane == 0) lds_add(sm, 17, 1u);
#pragma unroll 1
      for (int it = 1; it <= W2_N; ++it) {
        const unsigned nx = (it < 7) ? it * 4096u : 0u;
        if (it == W2_LOW_AT) __builtin_amdgcn_s_setprio(W2_LOW);
        if (it == W2_HOLD) { __builtin_amdgcn_s_setprio(0); lds_wait(sm, 21, W2_HOLD_N); __builtin_amdgcn_s_setprio(2); }
        if (it == WO_AT) {
          lds_wait(sm, 25, 8u);
          const u32x4 dws = mk_desc(ws, WS_END);
#pragma unroll 1
          for (int u = 0; u < 8; ++u)
            dma16x2_sc1(dws, sb + (unsigned)(j * 16 + 2 * u) * 1024u, lane * 16u, WS_WT + (unsigned)((2 * cp + (u >> 2)) * 144 + 112 + 8 * j + 2 * (u & 3)) * 1024u);
        }
        if (it == 4 && j == 1) {
          glb_wait(rws, WS_FCL + (unsigned)((4 * bi + tm) * 32 + l31) * 8u, tag);
          if (lane == 0) lds_add(sm, 24, 1u);
        }
        const rsrc_t rws = mk_rsrc(ws, it < 7 ? WS_END : 0u);
#pragma unroll
        for (int u = 0; u < 4; ++u) {
#pragma unroll
          for (int t = 0; t < 2; ++t)
#pragma unroll
            for (int c = 0; c < 2; ++c)
              acc[t][c] = __builtin_amdgcn_mfma_f32_32x32x16_bf16(__builtin_bit_cast(bf16x8, wf[u][c]), __builtin_bit_cast(bf16x8, af[u][t]), acc[t][c], 0, 0, 0);
          af[u][0] = LD128(lane * 16 + u * 1024, ab + nx); af[u][1] = LD128(lane * 16 + u * 1024, ab1 + nx);
          wf[u][0] = LD128(lane * 16 + u * 1024, wb + nx); wf[u][1] = LD128(lane * 16 + u * 1024, wb1 + nx);
          __builtin_amdgcn_sched_barrier(0);
        }
      }
      STAMP(8);
      WAITV(0);
      if (lane == 0) lds_add(sm, 24, 1u);
      __builtin_amdgcn_s_setprio(0);
      lds_wait(sm, 21, 8u);
      __builtin_amdgcn_s_setprio(3);
      LAS f32x4* xw = (LAS f32x4*)(sm + 65536 + j * 2048 + lane * 16);
#pragma unroll
      for (int u = 0; u < 16; ++u) {
        const f32x16& av = acc[u >> 3][(u >> 2) & 1];
        xw[(u >> 1) * 512 + (u & 1) * 64] = (f32x4){av[4 * (u & 3)], av[4 * (u & 3) + 1], av[4 * (u & 3) + 2], av[4 * (u & 3) + 3]};
      }
    }
    STAMP(5);
    STAMP_OUT();
    BARRIER();
    return;
  }

  STAMP(2);
  const int w = wave;
  const unsigned ka = lane * 16;
  const bool vone = l31 >= 16;
  const unsigned va = vone ? 2048u : 1024u + (unsigned)(hh * 16 + (l31 < 16 ? l31 : 0)) * 16u, va1 = vone ? 2048u : va + 512u;
  const int IDX = 0xDDDD;
  const bf16x8 qf = lds_frag(sm, L_Q + w * 1024 + lane * 16);
  f32x16 negm, O;
  {
    f32x16 z;
#pragma unroll
    for (int r = 0; r < 16; ++r) { z[r] = 0.f; O[r] = 0.f; }
    f32x16 s0 = __builtin_amdgcn_mfma_f32_32x32x16_bf16(lds_frag(sm, ka), qf, z, 0, 0, 0);
    float m = s0[0];
#pragma unroll
    for (int r = 1; r < 16; ++r) m = fmaxf(m, s0[r]);
    const auto sw = __builtin_amdgcn_permlane32_swap(__float_as_uint(m), __float_as_uint(m), false, false);
    m = fmaxf(__uint_as_float(sw[0]), __uint_as_float(sw[1]));
#pragma unroll
    for (int r = 0; r < 16; ++r) negm[r] = -m;
  }
  STAMP(7);
  const int tm = hd >> 3, cp = hd & 7;
  const int tt = w & 1, kq = w >> 1;
  unsigned xwo = (unsigned)(tt * 16 + kq) * 2048u + lane * 16u, xro = (unsigned)(tt * 16 + kq * 4) * 2048u + lane * 16u;
  unsigned oo = (unsigned)(256 * bi + 64 * tm + 32 * tt + l31) * 1024u + (unsigned)(64 * cp + 16 * kq + 8 * hh) * 2u;
  unsigned wfo = (unsigned)kq * 16384u + lane * 16u, gao = L_G + (unsigned)(64 * tm + 32 * tt + l31) * 4u;
  unsigned osb = WS_OF + (unsigned)((8 * bi + w) * 32 + hd) * 1024u, ob = WS_OF + (unsigned)((8 * bi + 2 * tm + tt) * 32 + 8 * kq) * 1024u;
  asm volatile("" : "+v"(xwo), "+v"(xro), "+v"(oo), "+v"(wfo), "+v"(gao), "+s"(osb), "+s"(ob));
  {
    u32x4 kf, v0, v1;
    unsigned kb = sb + ka, vb = sb + va, vb1 = sb + va1, fb = sb + L_FL + 4, ft;
    int cnt = 8, st, sp = 0x40000, sw = (w < 4) ? PRIO_LEFT : -1;
    if (w < 4) __builtin_amdgcn_s_setprio(2); else __builtin_amdgcn_s_setprio(1);
    asm volatile(ATTN_LOOP
                 : "+v"(O), "=&v"(kf), "=&v"(v0), "=&v"(v1), "+v"(kb), "+v"(vb), "+s"(cnt), "+v"(fb), "=&v"(ft), "=&s"(st), "+s"(sp), "+v"(vb1)
                 : "v"(qf), "v"(negm), "v"(IDX), "s"(sw)
                 : ATTN_CLOBS);
  }
  __builtin_amdgcn_s_setprio(3);
  STAMP(3);
  u32x4 wf[2][8];
  const unsigned rdy = __hip_atomic_load((LAS unsigned*)(sm + L_FL) + 24, __ATOMIC_RELAXED, __HIP_MEMORY_SCOPE_WORKGROUP);
  CFENCE();
  float gv = *(LAS float*)(sm + gao);
#pragma unroll
  for (int u = 0; u < 16; ++u) wf[u >> 3][u & 7] = *(LAS u32x4*)(sm + wfo + u * 1024);
  CFENCE();
  float lsum = O[8];
  {
    float chk = lsum * 0.f;
#pragma unroll
    for (int r = 0; r < 8; ++r) chk = __builtin_fmaf(O[r], 0.f, chk);
    if (__any(chk != chk)) {
      get_tag();
      glb_wait(rws, WS_FKV + (unsigned)(hd * 32 + l31) * 8u, tag);
      float m = -3.0e38f;
      f32x16 z;
#pragma unroll
      for (int r = 0; r < 16; ++r) { z[r] = 0.f; O[r] = 0.f; }
#pragma unroll 1
      for (int t = 0; t < 64; ++t) {
        const unsigned go = KVOFF(hd, t >> 1) + (t & 1) * 2048u, vg = 1024u + (unsigned)(hh * 16 + l15) * 16u;
        const u32x4 kg = LD128(lane * 16, go);
        u32x4 va0 = LD128(vg, go), va1 = LD128(vg + 512, go);
        if (l31 >= 16) { va0 = ones; va1 = ones; }
        f32x16 S = __builtin_amdgcn_mfma_f32_32x32x16_bf16(__builtin_bit_cast(bf16x8, kg), qf, z, 0, 0, 0);
        float mx = m;
#pragma unroll
        for (int r = 0; r < 16; ++r) mx = fmaxf(mx, S[r]);
        mx = fmaxf(mx, __shfl_xor(mx, 32));
        const float al = __builtin_amdgcn_exp2f(m - mx);
        m = mx;
        f32x8 p0, p1;
#pragma unroll
        for (int r = 0; r < 16; ++r) O[r] *= al;
#pragma unroll
        for (int r = 0; r < 8; ++r) { p0[r] = __builtin_amdgcn_exp2f(S[r] - m); p1[r] = __builtin_amdgcn_exp2f(S[8 + r] - m); }
        O = __builtin_amdgcn_smfmac_f32_32x32x32_bf16(__builtin_bit_cast(bf16x8, va0), __builtin_bit_cast(bf16x16, p0), O, IDX, 0, 0);
        O = __builtin_amdgcn_smfmac_f32_32x32x32_bf16(__builtin_bit_cast(bf16x8, va1), __builtin_bit_cast(bf16x16, p1), O, IDX, 0, 0);
      }
      lsum = O[8];
    }
  }
  {
    const float sc = __builtin_amdgcn_rcpf(lsum);
    u32x4 o;
#pragma unroll
    for (int e = 0; e < 4; ++e) o[e] = min(pk_bf16(O[2 * e] * sc, O[2 * e + 1] * sc), 0xfffffffeu);
    ST128(o, lane * 16u, osb);
  }
  {
    STAMP(4);
    LAS f32x4* xw = (LAS f32x4*)(sm + xwo);
    LAS f32x4* xr = (LAS f32x4*)(sm + xro);
    if ((unsigned)__builtin_amdgcn_readfirstlane(rdy) < 5u) {
      __builtin_amdgcn_s_setprio(0);
      lds_wait(sm, 24, 5u);
      __builtin_amdgcn_s_setprio(3);
      gv = *(LAS float*)(sm + gao);
#pragma unroll
      for (int u = 0; u < 16; ++u) wf[u >> 3][u & 7] = *(LAS u32x4*)(sm + wfo + u * 1024);
    }
    WAITL0();
    if (lane == 0) lds_add(sm, 21, 1u);
    __builtin_amdgcn_s_setprio(0); lds_wait(sm, 21, 8u); __builtin_amdgcn_s_setprio(3);
    WAITV(0);
    STAMP(14);
    f32x16 acc[2];
#pragma unroll 1
    for (unsigned s = 0; s < SPIN_CAP; ++s) {
      CFENCE();
      u32x4 of[8];
#pragma unroll
      for (int u = 0; u < 8; ++u) { of[u] = LD128(lane * 16 + (u & 3) * 1024, ob + (u >> 2) * 4096); __builtin_amdgcn_sched_barrier(0); }
      __builtin_amdgcn_sched_barrier(0);
#pragma unroll
      for (int r = 0; r < 16; ++r) { acc[0][r] = 0.f; acc[1][r] = 0.f; }
      unsigned mx = 0;
#pragma unroll
      for (int u = 0; u < 8; ++u) {
        acc[0] = __builtin_amdgcn_mfma_f32_32x32x16_bf16(__builtin_bit_cast(bf16x8, wf[0][u]), __builtin_bit_cast(bf16x8, of[u]), acc[0], 0, 0, 0);
        acc[1] = __builtin_amdgcn_mfma_f32_32x32x16_bf16(__builtin_bit_cast(bf16x8, wf[1][u]), __builtin_bit_cast(bf16x8, of[u]), acc[1], 0, 0, 0);
        mx = max(max(mx, of[u][0]), of[u][1]);
        mx = max(max(mx, of[u][2]), of[u][3]);
        asm volatile("" : "+v"(acc[0]), "+v"(acc[1]), "+v"(mx));
      }
      if (__all(mx != 0xffffffffu)) break;
    }
    STAMP(5);
#pragma unroll
    for (int p = 0; p < 4; ++p) {
      xw[p * 512] = (f32x4){acc[p >> 1][8 * (p & 1)], acc[p >> 1][8 * (p & 1) + 1], acc[p >> 1][8 * (p & 1) + 2], acc[p >> 1][8 * (p & 1) + 3]};
      xw[p * 512 + 64] = (f32x4){acc[p >> 1][8 * (p & 1) + 4], acc[p >> 1][8 * (p & 1) + 5], acc[p >> 1][8 * (p & 1) + 6], acc[p >> 1][8 * (p & 1) + 7]};
    }
    STAMP(15);
    BARRIER();
    STAMP(9);
    f32x4 pv[8], bv[8];
#pragma unroll
    for (int q = 0; q < 8; ++q) bv[q] = xr[4096 + q * 64];
#pragma unroll
    for (int q = 0; q < 8; ++q) pv[q] = xr[q * 64];
    const float om = 1.f - gv;
    const f32x4 a = ((bv[0] + bv[2]) + (bv[4] + bv[6])) * om + ((pv[0] + pv[2]) + (pv[4] + pv[6])) * gv;
    const f32x4 c = ((bv[1] + bv[3]) + (bv[5] + bv[7])) * om + ((pv[1] + pv[3]) + (pv[5] + pv[7])) * gv;
    const u32x4 o = {pk_bf16(a[0], a[1]), pk_bf16(a[2], a[3]), pk_bf16(c[0], c[1]), pk_bf16(c[2], c[3])};
    __builtin_amdgcn_raw_buffer_store_b128(o, mk_rsrc(out, 2048u * 1024u), oo, 0, OUT_AUX);
  }
  STAMP(6);
  STAMP_OUT();
}

extern "C" void kernel_launch(void* const* d_in, const int* in_sizes, int n_in, void* d_out, int out_size,
                              void* d_ws, size_t ws_size, hipStream_t stream) {
  fused_kernel<<<256, 768, 0, stream>>>((const u16*)d_in[0], (const u16*)d_in[1], (const u16*)d_in[2], (const u16*)d_in[4],
                                        (const u16*)d_in[5], (const u16*)d_in[6], (const u16*)d_in[7], (u16*)d_out,
                                        (unsigned char*)d_ws + WS_BASE);
}
```

```cpp
#include <hip/hip_runtime.h>
#include <hip/hip_bf16.h>

typedef unsigned short u16;
typedef unsigned long long u64;
typedef __bf16 bf16x8 __attribute__((ext_vector_type(8)));
typedef __bf16 bf16x16 __attribute__((ext_vector_type(16)));
typedef __bf16 bf16x2 __attribute__((ext_vector_type(2)));
typedef short s16x4 __attribute__((ext_vector_type(4)));
typedef float f32x2 __attribute__((ext_vector_type(2)));
typedef float f32x4 __attribute__((ext_vector_type(4)));
typedef float f32x8 __attribute__((ext_vector_type(8)));
typedef float f32x16 __attribute__((ext_vector_type(16)));
typedef unsigned u32x2 __attribute__((ext_vector_type(2)));
typedef unsigned u32x4 __attribute__((ext_vector_type(4)));
#define LAS __attribute__((address_space(3)))
typedef __amdgpu_buffer_rsrc_t rsrc_t;

constexpr unsigned WS_EPOCH = 0;
constexpr unsigned WS_FKV = 4096;
constexpr unsigned WS_FACT = WS_FKV + 8192;
constexpr unsigned WS_FCL = WS_FACT + 8192;
constexpr unsigned WS_G = 32768;
constexpr unsigned WS_KV = 65536;
constexpr unsigned WS_ACT = WS_KV + 32u * 131072u;
constexpr unsigned WS_WT = WS_ACT + 64u * 114688u;
constexpr unsigned WS_OF = WS_WT + 16u * 144u * 1024u;
constexpr unsigned WS_CK = WS_OF + 64u * 32u * 1024u;
constexpr unsigned WS_END = WS_CK + 32u * 32u * 512u;

constexpr unsigned STG = 47104;
constexpr unsigned TILE = 2064;
constexpr unsigned L_RT = 3 * STG;
constexpr unsigned L_Q = L_RT + 8192;
constexpr unsigned L_B1 = L_Q + 8192;
constexpr unsigned L_G = L_B1 + 1024;
constexpr unsigned L_FL = L_G + 1024;
constexpr unsigned L_END = L_FL + 512;

constexpr unsigned SPIN_CAP = 1u << 18;
constexpr int AUX_SC1 = 16;
constexpr int FAST_N = 2;

__device__ __forceinline__ unsigned pk_bf16(float a, float b) {
  f32x2 v = {a, b};
  return __builtin_bit_cast(unsigned, __builtin_convertvector(v, bf16x2));
}
__device__ __forceinline__ unsigned ck_mix(u32x4 a, u32x4 b, unsigned seed) {
  unsigned s = seed;
#pragma unroll
  for (int e = 0; e < 4; ++e) {
    s = __builtin_rotateleft32(s, 5) + a[e];
    s = __builtin_rotateleft32(s, 11) ^ b[e];
  }
  return s;
}
__device__ __forceinline__ float bf_lo(unsigned u) { return __uint_as_float(u << 16); }
__device__ __forceinline__ float bf_hi(unsigned u) { return __uint_as_float(u & 0xffff0000u); }
__device__ __forceinline__ rsrc_t mk_rsrc(const void* p, unsigned bytes) {
  return __builtin_amdgcn_make_buffer_rsrc((void*)p, 0, bytes, 0x00020000);
}
__device__ __forceinline__ u32x4 mk_desc(const void* p, unsigned bytes) {
  const u64 a = (u64)p;
  return (u32x4){(unsigned)a, (unsigned)(a >> 32) & 0xffffu, bytes, 0x00020000u};
}
__device__ __forceinline__ void dma16(u32x4 desc, unsigned lds_addr, unsigned voff, unsigned soff) {
  asm volatile("s_mov_b32 m0, %0\n\ts_nop 3\n\tbuffer_load_dwordx4 %1, %2, %3 offen lds"
               :: "s"(lds_addr), "v"(voff), "s"(desc), "s"(soff) : "memory");
}
__device__ __forceinline__ void dma16x2_sc1(u32x4 desc, unsigned lds_addr, unsigned voff, unsigned soff) {
  asm volatile("s_mov_b32 m0, %0\n\ts_nop 3\n\tbuffer_load_dwordx4 %1, %2, %3 offen sc1 lds\n\tbuffer_load_dwordx4 %1, %2, %3 offen offset:1024 sc1 lds"
               :: "s"(lds_addr), "v"(voff), "s"(desc), "s"(soff) : "memory");
}
template <int OFF>
__device__ __forceinline__ void dma16_next(u32x4 desc, unsigned voff, unsigned soff) {
  asm volatile("buffer_load_dwordx4 %0, %1, %2 offen offset:%3 lds" :: "v"(voff), "s"(desc), "s"(soff), "i"(OFF) : "memory");
}
#define BARRIER() asm volatile("s_waitcnt lgkmcnt(0)\n\ts_barrier" ::: "memory")
#define WAITV(n) asm volatile("s_waitcnt vmcnt(" #n ")" ::: "memory")
#define WAITL0() asm volatile("s_waitcnt lgkmcnt(0)" ::: "memory")
#define CFENCE() asm volatile("" ::: "memory")
#define LD128(off, soff) __builtin_amdgcn_raw_buffer_load_b128(rws, off, soff, AUX_SC1)
#define ST128(v, off, soff) __builtin_amdgcn_raw_buffer_store_b128(v, rws, off, soff, AUX_SC1)


__device__ __forceinline__ bf16x8 lds_frag(LAS char* sm, unsigned off) { return *(LAS bf16x8*)(sm + off); }
__device__ __forceinline__ bf16x8 lds_tr2(LAS char* sm, unsigned o0, unsigned o1) {
  s16x4 a = __builtin_amdgcn_ds_read_tr16_b64_v4i16((LAS s16x4*)(sm + o0));
  s16x4 b = __builtin_amdgcn_ds_read_tr16_b64_v4i16((LAS s16x4*)(sm + o1));
  return __builtin_bit_cast(bf16x8, __builtin_shufflevector(a, b, 0, 1, 2, 3, 4, 5, 6, 7));
}
__device__ __forceinline__ void lds_add(LAS char* sm, unsigned idx, unsigned v) {
  __hip_atomic_fetch_add((LAS unsigned*)(sm + L_FL) + idx, v, __ATOMIC_RELAXED, __HIP_MEMORY_SCOPE_WORKGROUP);
}
__device__ __forceinline__ void lds_wait(LAS char* sm, unsigned idx, unsigned want) {
#pragma unroll 1
  for (unsigned s = 0; s < SPIN_CAP * 16; ++s) {
    CFENCE();
    unsigned v = __hip_atomic_load((LAS unsigned*)(sm + L_FL) + idx, __ATOMIC_RELAXED, __HIP_MEMORY_SCOPE_WORKGROUP);
    if ((unsigned)__builtin_amdgcn_readfirstlane(v) >= want) break;
    __builtin_amdgcn_s_sleep(1);
  }
  CFENCE();
}
__device__ __forceinline__ void glb_wait(rsrc_t rs, unsigned off, u32x2 tag) {
#pragma unroll 1
  for (unsigned s = 0; s < SPIN_CAP; ++s) {
    CFENCE();
    u32x2 v = __builtin_amdgcn_raw_buffer_load_b64(rs, off, 0, AUX_SC1);
    if (__all(v.x == tag.x && v.y == tag.y)) break;
    __builtin_amdgcn_s_sleep(4);
  }
  CFENCE();
}

#define AE(b, i) "v_exp_f32 v[" #b "+" #i "], v[" #b "+" #i "]\n"
#define ATILE(cur, oth, t)                                                                                          \
  AE(cur, 0) AE(cur, 1)                                                                                              \
  "s_waitcnt lgkmcnt(2)\n v_smfmac_f32_32x32x32_bf16 %0, %3, v[" #oth "+8:" #oth "+15], %14\n"                       \
  "ds_read_b128 %3, %11 offset:2064*" #t "\n"                                                                        \
  AE(cur, 2) AE(cur, 3) AE(cur, 4) AE(cur, 5)                                                                        \
  "s_waitcnt lgkmcnt(2)\n v_mfma_f32_32x32x16_bf16 v[" #oth ":" #oth "+15], %1, %12, %13\n"                          \
  "ds_read_b128 %1, %4 offset:2064*(" #t "+2)\n"                                                                     \
  AE(cur, 6) AE(cur, 7) AE(cur, 8) AE(cur, 9) AE(cur, 10)                                                            \
  "s_waitcnt lgkmcnt(2)\n v_smfmac_f32_32x32x32_bf16 %0, %2, v[" #cur ":" #cur "+7], %14\n"                          \
  "ds_read_b128 %2, %5 offset:2064*(" #t "+1)\n"                                                                     \
  AE(cur, 11) AE(cur, 12) AE(cur, 13) AE(cur, 14) AE(cur, 15)
#define ATTN_LOOP                                                                                                   \
  "ds_read_b128 %1, %4\n ds_read_b128 %3, %5\n"                                                                      \
  "v_mov_b32 v120, 0\n v_mov_b32 v121, 0\n v_mov_b32 v122, 0\n v_mov_b32 v123, 0\n"                                  \
  "v_mov_b32 v124, 0\n v_mov_b32 v125, 0\n v_mov_b32 v126, 0\n v_mov_b32 v127, 0\n"                                  \
  "s_waitcnt lgkmcnt(0)\n v_mfma_f32_32x32x16_bf16 v[96:111], %1, %12, %13\n"                                        \
  "ds_read_b128 %1, %4 offset:2064\n ds_read_b128 %2, %5\n s_nop 11\n"                                               \
  "1:\n"                                                                                                             \
  ATILE(96, 112, 0) ATILE(112, 96, 1) ATILE(96, 112, 2) ATILE(112, 96, 3) ATILE(96, 112, 4)                          \
  "ds_read_b32 %8, %7\n"                                                                                             \
  ATILE(112, 96, 5)                                                                                                  \
  "v_readfirstlane_b32 %9, %8\n s_nop 3\n s_cmp_eq_u32 %9, 8\n s_cbranch_scc1 3f\n"         \
  "2:\n s_sleep 1\n ds_read_b32 %8, %7\n s_waitcnt lgkmcnt(0)\n v_readfirstlane_b32 %9, %8\n s_sub_u32 %10, %10, 1\n" \
  "s_cmp_le_i32 %10, 0\n s_cbranch_scc1 3f\n s_cmp_eq_u32 %9, 8\n s_cbranch_scc0 2b\n"                               \
  "3:\n"                                                                                                             \
  ATILE(96, 112, 6) ATILE(112, 96, 7)                                                                                \
  "v_add_u32 %4, 8*2064, %4\n v_add_u32 %5, 8*2064, %5\n v_add_u32 %11, 8*2064, %11\n v_add_u32 %7, 4, %7\n s_sub_u32 %6, %6, 1\n"                \
  "s_cmp_lg_u32 %6, 4\n s_cbranch_scc1 4f\n v_mov_b32 %8, 1\n s_mov_b64 exec, 1\n ds_add_u32 %7, %8 offset:80\n s_mov_b64 exec, -1\n 4:\n" \
  "s_cmp_lg_u32 %6, %15\n s_cbranch_scc1 5f\n s_setprio 0\n 5:\n"                                                    \
  "s_cmp_lg_u32 %6, 0\n s_cbranch_scc1 1b\n"                                                                         \
  "s_waitcnt lgkmcnt(0)\n v_smfmac_f32_32x32x32_bf16 %0, %3, v[120:127], %14\n s_nop 15\n"
#define ATTN_CLOBS "v96","v97","v98","v99","v100","v101","v102","v103","v104","v105","v106","v107","v108","v109","v110","v111", \
  "v112","v113","v114","v115","v116","v117","v118","v119","v120","v121","v122","v123","v124","v125","v126","v127","scc","memory"
#define KVOFF(h, g) (WS_KV + (unsigned)((h) * 32 + (g)) * 4096u)

__global__ void __launch_bounds__(768, 3)
fused_kernel(const u16* __restrict__ x, const u16* __restrict__ wqkv, const u16* __restrict__ wo,
             const u16* __restrict__ w1, const u16* __restrict__ b1, const u16* __restrict__ w2,
             const u16* __restrict__ gw, u16* __restrict__ out, unsigned char* __restrict__ ws) {
  __shared__ __attribute__((aligned(16))) char smem_[L_END];
  LAS char* sm = (LAS char*)smem_;
  const unsigned sb = (unsigned)(size_t)sm;
  asm volatile("" :: "s"(x), "s"(wqkv), "s"(wo), "s"(w1), "s"(b1), "s"(w2), "s"(gw), "s"(out), "s"(ws));
  const int lane = threadIdx.x & 63;
  const int wave = __builtin_amdgcn_readfirstlane(threadIdx.x >> 6);
  const int b = blockIdx.x, xcd = b & 7, idx = b >> 3;
  const int bi = 4 * (xcd & 1) + (idx & 3), hd = 8 * (xcd >> 1) + (idx >> 2);
  const bool has_w1 = (hd & 7) != 7;
  const int cw = hd - (hd >> 3);
  const int rot = ((hd + bi) & 1) * 4;
  const int hh = lane >> 5, l31 = lane & 31, l15 = lane & 15, g4 = lane >> 4;
  const int q4 = l15 >> 2, p4 = lane & 3, r8 = lane >> 3, sl8 = lane & 7;
  if (wave < 8) {
    const u32x4 dx = mk_desc(x, 2048u * 1024u);
    const unsigned xo0 = (unsigned)(256 * bi + 32 * wave + r8) * 1024u + (unsigned)(sl8 ^ (r8 >> 1)) * 16u, xo1 = (xo0 ^ 64u) + 7168u;
    dma16(dx, sb + wave * 4096u, xo0, rot * 128u);
    dma16_next<1024>(dx, xo1, rot * 128u);
    dma16_next<2048>(dx, xo0, rot * 128u + 14336u);
    dma16_next<3072>(dx, xo1, rot * 128u + 14336u);
  }
  __builtin_amdgcn_sched_barrier(0);
  const rsrc_t rws = mk_rsrc(ws, WS_END);
  const u32x4 ones = {0x3f803f80u, 0x3f803f80u, 0x3f803f80u, 0x3f803f80u};

  auto mk_tag = [&](u32x2 e) {
    u64 z = ((u64)(unsigned)__builtin_amdgcn_readfirstlane(e.y) << 32 | (u64)(unsigned)__builtin_amdgcn_readfirstlane(e.x)) + 0x9E3779B97F4A7C15ull;
    z = (z ^ (z >> 30)) * 0xBF58476D1CE4E5B9ull;
    z = (z ^ (z >> 27)) * 0x94D049BB133111EBull;
    z ^= z >> 31;
    return (u32x2){(unsigned)z, (unsigned)(z >> 32)};
  };
  u32x2 tag;
  auto get_tag = [&]() {
    const u32x2 t = *(LAS u32x2*)(sm + L_FL + 256);
    tag = (u32x2){(unsigned)__builtin_amdgcn_readfirstlane(t.x), (unsigned)__builtin_amdgcn_readfirstlane(t.y)};
  };
  const unsigned xa0 = (unsigned)(64 * (wave & 3) + l15) * 128u + (unsigned)(g4 ^ (l15 >> 1)) * 16u;
  const unsigned xa1 = xa0 ^ 64u;
#define NEXT_BUF(v) v = (v == 2 * STG) ? 0 : v + STG
  const unsigned qo = (unsigned)(16 * (wave & 3) + (((lane >> 1) & 15) ^ ((g4 & 1) << 2))) * 3072u + (unsigned)hh * 1024u + (unsigned)hd * 32u + (lane & 1) * 16u;
  auto qkv_issue = [&](int n, unsigned buf) {
    const u32x4 d = mk_desc(wqkv, 512u * 3072u);
    const unsigned so = ((unsigned)(n + rot) & 7u) * (64u * 3072u);
    dma16(d, sb + buf + 32768u + (wave & 3) * 3584u, qo, so);
    if (lane < 32) dma16_next<1024>(d, qo, so + 1024u);
  };

  if (__builtin_expect(wave >= 4 && wave < 8, 1)) {
    const int lw = wave - 4;
    const u32x4 dx = mk_desc(x, 2048u * 1024u);
    const unsigned xo0 = (unsigned)(256 * bi + 64 * lw + r8) * 1024u + (unsigned)(sl8 ^ (r8 >> 1)) * 16u;
    const unsigned xo1 = (xo0 ^ 64u) + 8192u - 1024u;
    const u32x4 d1 = mk_desc(w1, 512u * 3584u);
    const unsigned wo0 = (unsigned)(16 * lw + r8) * 3584u + (unsigned)cw * 128u + (unsigned)(sl8 ^ (r8 & 2)) * 16u;
    const unsigned wo1 = (wo0 ^ 64u) + 8u * 3584u - 1024u;
    unsigned buf = 0;
#pragma unroll 1
    for (int n = 0; n < 8; ++n) {
      const unsigned ks = (unsigned)(n + rot) & 7u, d = sb + buf + lw * 8192u;
      if (n < 2) qkv_issue(n, buf);
      if (n)
#pragma unroll
      for (int u = 0; u < 2; ++u) {
        dma16(dx, d + u * 4096u, xo0, ks * 128u + u * 32768u);
        dma16_next<1024>(dx, xo1, ks * 128u + u * 32768u);
        dma16_next<2048>(dx, xo0, ks * 128u + u * 32768u + 16384u - 2048u);
        dma16_next<3072>(dx, xo1, ks * 128u + u * 32768u + 16384u - 2048u);
      }
      if (has_w1) {
        dma16(d1, sb + buf + 32768u + 1536u + lw * 3584u, wo0, ks * (64u * 3584u));
        dma16_next<1024>(d1, wo1, ks * (64u * 3584u));
      }
      if (n == 0 && lw == 0) {
        if (has_w1) { if (lane < 8) dma16(mk_desc(b1, 3584u), sb + L_B1, lane * 16u + (unsigned)cw * 128u, 0u); }
        else dma16(mk_desc(gw, 1024u), sb + L_G, lane * 16u, 0u);
      }
      if (n == 5) {
        if (has_w1) {
          const u32x4 d2 = mk_desc(w2, 1792u * 1024u);
          const unsigned o2 = (unsigned)(16 * (r8 >> 2) + (r8 & 3)) * 1024u + (unsigned)bi * 128u + (unsigned)sl8 * 16u;
          const unsigned so = (unsigned)(64 * cw + 32 * (lw >> 1) + 8 * (lw & 1)) * 1024u;
          dma16(d2, sb + L_RT + lw * 2048u, o2, so); dma16_next<1024>(d2, o2, so + 3072u);
        } else {
          const u32x4 dO = mk_desc(wo, 512u * 1024u);
          const unsigned oo = (unsigned)r8 * 1024u + (unsigned)bi * 128u + (unsigned)sl8 * 16u;
          const unsigned so = (unsigned)((hd >> 3) * 8 + 2 * lw) * 16384u;
          dma16(dO, sb + L_RT + lw * 2048u, oo, so); dma16_next<1024>(dO, oo, so + 7168u);
          dma16(dO, sb + 2 * STG + 32768u + 1536u + lw * 3584u, oo, so + 16384u); dma16_next<1024>(dO, oo, so + 16384u + 7168u);
        }
      }
      NEXT_BUF(buf);
      if (n >= 1) {
        if (n == 5) WAITV(12); else if (n == 1) { if (has_w1) WAITV(12); else WAITV(10); } else if (has_w1) WAITV(10); else WAITV(8);
        BARRIER();
      }
    }
    WAITV(0);
    BARRIER();
    if (lane < 2) *(LAS u32x4*)(sm + (2 * lw + lane) * TILE + 2048) = ones;
    BARRIER();
  } else if (wave < 4) {
    const int j = wave;
    __builtin_amdgcn_s_setprio(3);
    if (wave == 0) ((LAS unsigned*)(sm + L_FL))[lane] = 0u;
    f32x4 acc[4][3];
#pragma unroll
    for (int t = 0; t < 4; ++t)
#pragma unroll
      for (int c = 0; c < 3; ++c) acc[t][c] = (f32x4){0.f, 0.f, 0.f, 0.f};
    const unsigned wa0 = 32768u + (unsigned)(g4 >> 1) * 3584u + (unsigned)(12 * (g4 & 1) + q4) * 32u + p4 * 8u;
    const unsigned wa1 = wa0 ^ 128u;
    bf16x8 xA[4], wA[3], xB[4], wB[3];
#pragma unroll
    for (int t = 0; t < 4; ++t) xB[t] = __builtin_bit_cast(bf16x8, (u32x4){0u, 0u, 0u, 0u});
#pragma unroll
    for (int c = 0; c < 3; ++c) wB[c] = __builtin_bit_cast(bf16x8, (u32x4){0u, 0u, 0u, 0u});
#define QKV_RD(X, W, kk)                                                                                           \
    _Pragma("unroll") for (int c = 0; c < 3; ++c) W[c] = lds_tr2(sm, buf + wa0 + kk * 7168 + c * 512, buf + wa1 + kk * 7168 + c * 512); \
    _Pragma("unroll") for (int t = 0; t < 4; ++t) X[t] = lds_frag(sm, buf + (kk ? xa1 : xa0) + t * 2048);
#define QKV_MM(X, W)                                                                                               \
    _Pragma("unroll") for (int t = 0; t < 4; ++t) {                                                                \
      acc[t][0] = __builtin_amdgcn_mfma_f32_16x16x32_bf16(W[0], X[t], acc[t][0], 0, 0, 0);                         \
      acc[t][1] = __builtin_amdgcn_mfma_f32_16x16x32_bf16(W[1], X[t], acc[t][1], 0, 0, 0);                         \
      acc[t][2] = __builtin_amdgcn_mfma_f32_16x16x32_bf16(X[t], W[2], acc[t][2], 0, 0, 0);                         \
    }                                                                                                              \
    _Pragma("unroll") for (int u = 0; u < 10; ++u) { __builtin_amdgcn_sched_group_barrier(0x008, 1, 0); __builtin_amdgcn_sched_group_barrier(0x100, 1, 0); } \
    __builtin_amdgcn_sched_group_barrier(0x008, 2, 0);                                                             \
    asm volatile("" : "+v"(acc[0][0]), "+v"(acc[0][1]), "+v"(acc[0][2]), "+v"(acc[1][0]), "+v"(acc[1][1]), "+v"(acc[1][2]), \
                      "+v"(acc[2][0]), "+v"(acc[2][1]), "+v"(acc[2][2]), "+v"(acc[3][0]), "+v"(acc[3][1]), "+v"(acc[3][2]) :: "memory"); \
    __builtin_amdgcn_sched_barrier(0);
    unsigned vo = 1024u + (unsigned)(g4 & 1) * 512u + (unsigned)((g4 >> 1) * 16 + (l15 & 3) + 8 * ((l15 >> 2) & 1) + 4 * (l15 >> 3)) * 16u;
    unsigned eq = L_Q + 2 * j * 1024 + lane * 16, ek = 2 * j * TILE + lane * 16, ev = 2 * j * TILE + vo;
    unsigned go = KVOFF(hd, bi * 4 + j);
    asm volatile("" : "+v"(vo), "+v"(eq), "+v"(ek), "+v"(ev), "+s"(go));
    unsigned buf = 0, nbuf = 2 * STG;
    WAITV(0);
#pragma unroll 1
    for (int n = 0; n < 8; ++n) {
      BARRIER();
      QKV_RD(xA, wA, 0)
      QKV_MM(xB, wB)
      if (n < 6) qkv_issue(n + 2, nbuf);
      QKV_RD(xB, wB, 1)
      QKV_MM(xA, wA)
      NEXT_BUF(buf); NEXT_BUF(nbuf);
      if (n < 6) WAITV(2); else WAITV(0);
    }
    QKV_MM(xB, wB)
    u32x4 pub[3][2];
#pragma unroll
    for (int c = 2; c >= 0; --c)
#pragma unroll
      for (int tp = 0; tp < 2; ++tp) {
        const float sc = c ? 1.f : 0.25f * 1.4426950408889634f;
        const f32x4 a = acc[2 * tp][c] * sc, e = acc[2 * tp + 1][c] * sc;
        const auto r0 = __builtin_amdgcn_permlane16_swap(pk_bf16(a[0], a[1]), pk_bf16(e[0], e[1]), false, false);
        const auto r1 = __builtin_amdgcn_permlane16_swap(pk_bf16(a[2], a[3]), pk_bf16(e[2], e[3]), false, false);
        pub[c][tp] = (u32x4){r0[0], r1[0], r0[1], r1[1]};
        if (c) ST128(pub[c][tp], (c == 1 ? lane * 16u : vo) + tp * 2048, go);
        *(LAS u32x4*)(sm + (c == 0 ? eq + tp * 1024 : (c == 1 ? ek : ev) + tp * TILE)) = pub[c][tp];
      }
    asm volatile("" :: "v"(pub[1][0]), "v"(pub[1][1]), "v"(pub[2][0]), "v"(pub[2][1]));
    BARRIER();
    get_tag();
    {
      const unsigned co = WS_CK + (unsigned)(hd * 32 + bi * 4 + j) * 512u;
      __builtin_amdgcn_raw_buffer_store_b32(ck_mix(pub[1][0], pub[1][1], tag.x), rws, lane * 8u, co, AUX_SC1);
      __builtin_amdgcn_raw_buffer_store_b32(ck_mix(pub[2][0], pub[2][1], tag.y), rws, ((vo - 1024u) >> 1) + 4u, co, AUX_SC1);
    }
    WAITV(0);
    if (lane == 0) __builtin_amdgcn_raw_buffer_store_b64(tag, rws, WS_FKV + (unsigned)((hd * 8 + bi) * 4 + j) * 8u, 0, AUX_SC1);
  } else {
    const int j = wave - 8;
    u32x2 ep = {0u, 0u};
    if (j == 0) ep = __builtin_amdgcn_raw_buffer_load_b64(rws, WS_EPOCH, 0, AUX_SC1);
    auto put_tag = [&]() { if (j == 0) *(LAS u32x2*)(sm + L_FL + 256) = mk_tag(ep); };
    auto retile = [&](unsigned img, int ks) {
      const unsigned ta = img + (unsigned)(8 * hh + q4) * 128u + (unsigned)(4 * (g4 & 1) + 2 * (p4 & 1) + (p4 >> 1)) * 8u;
      ST128(__builtin_bit_cast(u32x4, lds_tr2(sm, ta, ta + 512)), WS_WT + lane * 16, (2 * bi * 144 + ks) * 1024);
      ST128(__builtin_bit_cast(u32x4, lds_tr2(sm, ta + 64, ta + 576)), WS_WT + lane * 16, ((2 * bi + 1) * 144 + ks) * 1024);
    };
    auto clear_o = [&]() {
      const u32x4 inv = {0xffffffffu, 0xffffffffu, 0xffffffffu, 0xffffffffu};
      const unsigned oo = WS_OF + (unsigned)((8 * bi + 2 * j) * 32 + hd) * 1024u + lane * 16u;
      ST128(inv, oo, 0); ST128(inv, oo + 32768, 0);
    };
    const unsigned fkv = WS_FKV + (unsigned)((hd * 8 + ((bi + lane) & 7)) * 4 + j) * 8u;
    u32x4 fd[FAST_N][4];
    u32x2 fc[FAST_N];
    auto fast_load = [&](int u) __attribute__((always_inline)) {
      const unsigned g = (unsigned)(((bi + 1 + u) & 7) * 4 + j);
      fc[u] = __builtin_amdgcn_raw_buffer_load_b64(rws, lane * 8u, WS_CK + (unsigned)(hd * 32 + g) * 512u, AUX_SC1);
#pragma unroll
      for (int e = 0; e < 4; ++e) fd[u][e] = LD128(lane * 16 + e * 1024, KVOFF(hd, g));
    };
    auto fast_take = [&]() __attribute__((always_inline)) {
#pragma unroll
      for (int u = 0; u < FAST_N; ++u) {
#pragma unroll 1
        for (unsigned s = 0; s < SPIN_CAP; ++s) {
          if (__all(ck_mix(fd[u][0], fd[u][2], tag.x) == fc[u].x && ck_mix(fd[u][1], fd[u][3], tag.y) == fc[u].y)) break;
          CFENCE();
#pragma unroll
          for (int v = u; v < FAST_N; ++v) fast_load(v);
        }
        LAS char* dst = sm + ((1 + u) * 8 + 2 * j) * TILE + lane * 16;
#pragma unroll
        for (int e = 0; e < 4; ++e) *(LAS u32x4*)(dst + (e >> 1) * TILE + (e & 1) * 1024) = fd[u][e];
        WAITL0();
        if (lane == 0) lds_add(sm, 1 + u, 2u);
      }
    };
    auto gather = [&](auto pre) __attribute__((always_inline)) {
      u32x2 fv = __builtin_amdgcn_raw_buffer_load_b64(rws, fkv, 0, AUX_SC1);
      fast_take();
      const u32x4 dws = mk_desc(ws, WS_END);
      unsigned spins = 0;
      bool done = false;
#pragma unroll 1
      for (int n0 = 1 + FAST_N; n0 < 8;) {
        const unsigned rdy = ((unsigned)__builtin_amdgcn_ballot_w64(fv.x == tag.x && fv.y == tag.y) & 0xffu) >> n0;
        int cnt = __builtin_ctz(~rdy);
        if (++spins > SPIN_CAP) cnt = 8 - n0;
        CFENCE();
#pragma unroll 1
        for (int u = n0; u < n0 + cnt; ++u) {
          const unsigned go = KVOFF(hd, ((bi + u) & 7) * 4 + j), d = sb + (unsigned)(u * 8 + 2 * j) * TILE;
          const unsigned gu = __builtin_amdgcn_readfirstlane(go), du = __builtin_amdgcn_readfirstlane(d);
          dma16x2_sc1(dws, du, lane * 16u, gu);
          dma16x2_sc1(dws, du + TILE, lane * 16u, gu + 2048u);
        }
        if (n0 + cnt < 8) {
          if (!cnt) __builtin_amdgcn_s_sleep(4);
          fv = __builtin_amdgcn_raw_buffer_load_b64(rws, fkv, 0, AUX_SC1);
        }
        WAITV(0);
        if (lane < cnt) lds_add(sm, n0 + lane, 2u);
        n0 += cnt;
        if (!done && n0 > 4) {
          pre();
          WAITV(0);
          if (lane == 0) __builtin_amdgcn_raw_buffer_store_b64(tag, rws, WS_FACT + (unsigned)((bi * 4 + j) * 32 + hd) * 8u, 0, AUX_SC1);
          done = true;
        }
      }
    };
    auto put_ones = [&]() { if (lane < 14) *(LAS u32x4*)(sm + ((1 + (lane >> 1)) * 8 + 2 * j + (lane & 1)) * TILE + 2048) = ones; };
    if (has_w1) {
      f32x4 acc[4][4];
      const unsigned wa = 32768u + 1536u + (unsigned)(g4 >> 1) * 3584u + (unsigned)(8 * (g4 & 1) + q4) * 128u +
                          (unsigned)((q4 >> 1) | ((g4 & 1) << 1)) * 32u + p4 * 8u;
      bf16x8 xA[4], wA[4], xB[4], wB[4];
#pragma unroll
      for (int t = 0; t < 4; ++t) { xB[t] = __builtin_bit_cast(bf16x8, (u32x4){0u, 0u, 0u, 0u}); wB[t] = xB[t]; }
#define W1_RD(X, W, kk)                                                                                            \
      _Pragma("unroll") for (int c = 0; c < 4; ++c) W[c] = lds_tr2(sm, buf + (wa ^ (unsigned)(c * 32)) + kk * 7168, buf + (wa ^ (unsigned)(c * 32)) + kk * 7168 + 512); \
      _Pragma("unroll") for (int t = 0; t < 4; ++t) X[t] = lds_frag(sm, buf + (kk ? xa1 : xa0) + t * 2048);
#define W1_MM(X, W)                                                                                                \
      _Pragma("unroll") for (int t = 0; t < 4; ++t)                                                                \
        _Pragma("unroll") for (int c = 0; c < 4; ++c) acc[t][c] = __builtin_amdgcn_mfma_f32_16x16x32_bf16(W[c], X[t], acc[t][c], 0, 0, 0); \
      _Pragma("unroll") for (int u = 0; u < 12; ++u) { __builtin_amdgcn_sched_group_barrier(0x008, 1, 0); __builtin_amdgcn_sched_group_barrier(0x100, 1, 0); } \
      __builtin_amdgcn_sched_group_barrier(0x008, 4, 0);                                                           \
      asm volatile("" : "+v"(acc[0][0]), "+v"(acc[0][1]), "+v"(acc[0][2]), "+v"(acc[0][3]), "+v"(acc[1][0]), "+v"(acc[1][1]), "+v"(acc[1][2]), "+v"(acc[1][3]), \
                        "+v"(acc[2][0]), "+v"(acc[2][1]), "+v"(acc[2][2]), "+v"(acc[2][3]), "+v"(acc[3][0]), "+v"(acc[3][1]), "+v"(acc[3][2]), "+v"(acc[3][3]) :: "memory"); \
      __builtin_amdgcn_sched_barrier(0);
      unsigned buf = 0;
      BARRIER();
#pragma unroll
      for (int c = 0; c < 4; ++c) {
        const u32x2 bv = *(LAS u32x2*)(sm + L_B1 + (16 * c + 4 * g4) * 2);
#pragma unroll
        for (int t = 0; t < 4; ++t) acc[t][c] = (f32x4){bf_lo(bv.x), bf_hi(bv.x), bf_lo(bv.y), bf_hi(bv.y)};
      }
#pragma unroll 1
      for (int n = 0; n < 8; ++n) {
        W1_RD(xA, wA, 0)
        W1_MM(xB, wB)
        W1_RD(xB, wB, 1)
        W1_MM(xA, wA)
        NEXT_BUF(buf);
        if (n == 2) put_tag();
        if (n < 7) BARRIER();
      }
      BARRIER();
      __builtin_amdgcn_s_setprio(3);
      get_tag();
      put_ones();
      W1_MM(xB, wB)
      u32x4 actv[4][2];
      auto mish1 = [&](int t) __attribute__((always_inline)) {
#pragma unroll
          for (int pc = 0; pc < 2; ++pc) {
            u32x4 o;
#pragma unroll
            for (int e = 0; e < 4; ++e) {
              f32x2 hv = {acc[t][2 * pc + (e >> 1)][2 * (e & 1)], acc[t][2 * pc + (e >> 1)][2 * (e & 1) + 1]};
              f32x2 a = hv * 1.4426950408889634f;
              f32x2 ex = {__builtin_amdgcn_exp2f(a.x), __builtin_amdgcn_exp2f(a.y)};
              f32x2 d = ex * (ex + 2.f) + 2.f;
              f32x2 r = {__builtin_amdgcn_rcpf(d.x), __builtin_amdgcn_rcpf(d.y)};
              f32x2 y = hv * (r * -2.f + 1.f);
              o[e] = pk_bf16(y.x, y.y);
            }
            asm volatile("" : "+v"(o));
            actv[t][pc] = o;
          }
      };
      mish1(0); mish1(1);
      mish1(2);
#pragma unroll
      for (int u = 0; u < FAST_N; ++u) fast_load(u);
      mish1(3);
      gather([&]() __attribute__((always_inline)) {
        const unsigned ao = WS_ACT + (unsigned)(8 * bi + 2 * j) * 114688u + (unsigned)cw * 4096u + (unsigned)g4 * 512u + l15 * 16u;
#pragma unroll
        for (int t = 0; t < 4; ++t)
#pragma unroll
          for (int pc = 0; pc < 2; ++pc) { ST128(actv[t][pc], ao + (t >> 1) * 114688 + pc * 2048 + (t & 1) * 256, 0); }
        retile(L_RT + j * 2048u, 4 * cw + j);
      });
    } else {
      f32x4 acc[4];
#pragma unroll
      for (int t = 0; t < 4; ++t) acc[t] = (f32x4){0.f, 0.f, 0.f, 0.f};
      unsigned buf = 0;
#pragma unroll 1
      for (int n = 0; n < 8; ++n) {
        BARRIER();
        if (hd == 7) {
#pragma unroll
          for (int kk = 0; kk < 2; ++kk) {
            const bf16x8 gf = lds_frag(sm, L_G + ((n + rot) & 7) * 128 + kk * 64 + g4 * 16);
#pragma unroll
            for (int t = 0; t < 4; ++t) acc[t] = __builtin_amdgcn_mfma_f32_16x16x32_bf16(gf, lds_frag(sm, buf + (kk ? xa1 : xa0) + t * 2048), acc[t], 0, 0, 0);
          }
        }
        if (n == 2) put_tag();
        if (n == 5 || n == 6) retile(n == 5 ? L_RT + j * 2048u : 2 * STG + 32768u + 1536u + j * 3584u, 112 + (hd >> 3) * 8 + 2 * j + (n - 5));
        NEXT_BUF(buf);
      }
      WAITV(0);
      get_tag();
      BARRIER();
      __builtin_amdgcn_s_setprio(3);
      put_ones();
      if (hd == 7) {
        const float lg = g4 == 0 ? acc[0][0] : g4 == 1 ? acc[1][0] : g4 == 2 ? acc[2][0] : acc[3][0];
        const float gv = __builtin_amdgcn_rcpf(1.f + __builtin_amdgcn_exp2f(lg * -1.4426950408889634f));
        __builtin_amdgcn_raw_buffer_store_b32(__float_as_uint(gv), rws, WS_G + (unsigned)(256 * bi + 64 * j + lane) * 4u, 0, AUX_SC1);
      }
      __builtin_amdgcn_s_sleep(16);
#pragma unroll
      for (int u = 0; u < FAST_N; ++u) fast_load(u);
      gather([&]() {});
    }
    clear_o();
    WAITV(0);
    if (lane == 0) { __builtin_amdgcn_raw_buffer_store_b64(tag, rws, WS_FCL + (unsigned)((bi * 4 + j) * 32 + hd) * 8u, 0, AUX_SC1); lds_add(sm, 8, 2u); }
    const int tm = hd >> 3, cp = hd & 7;
    if (j == 0) {
      const unsigned o1 = WS_FACT + (unsigned)((4 * bi + tm) * 32 + l31) * 8u;
#pragma unroll 1
      for (unsigned s = 0; s < SPIN_CAP; ++s) {
        CFENCE();
        const u32x2 v = __builtin_amdgcn_raw_buffer_load_b64(rws, o1, 0, AUX_SC1);
        const u32x4 y = LD128(WS_FACT + (unsigned)cp * 1024u + lane * 16u, 0);
        if (__all(v.x == tag.x && v.y == tag.y && y[0] == tag.x && y[1] == tag.y && y[2] == tag.x && y[3] == tag.y)) break;
        __builtin_amdgcn_s_sleep(4);
      }
      CFENCE();
      if (lane == 0) lds_add(sm, 18, 1u);
    } else {
      if (b == 0 && j == 2) {
#pragma unroll 1
        for (int u = 0; u < 4; ++u) glb_wait(rws, WS_FKV + (unsigned)(64 * u + lane) * 32u, tag);
        if (lane == 0) __builtin_amdgcn_raw_buffer_store_b64(tag, rws, WS_EPOCH, 0, AUX_SC1);
      }
      lds_wait(sm, 18, 1u);
    }
    {
      f32x16 acc[2][2];
#pragma unroll
      for (int t = 0; t < 2; ++t)
#pragma unroll
        for (int c = 0; c < 2; ++c)
#pragma unroll
          for (int r = 0; r < 16; ++r) acc[t][c][r] = 0.f;
      const unsigned ab = WS_ACT + (unsigned)(8 * bi + 2 * tm) * 114688u + (unsigned)(28 * j) * 1024u;
      const unsigned ab1 = WS_ACT + (unsigned)(8 * bi + 2 * tm + 1) * 114688u + (unsigned)(28 * j) * 1024u;
      const unsigned wb = WS_WT + (unsigned)((2 * cp) * 144 + 28 * j) * 1024u;
      const unsigned wb1 = WS_WT + (unsigned)((2 * cp + 1) * 144 + 28 * j) * 1024u;
      u32x4 af[4][2], wf[4][2];
#pragma unroll
      for (int u = 0; u < 4; ++u) {
        af[u][0] = LD128(lane * 16 + u * 1024, ab); af[u][1] = LD128(lane * 16 + u * 1024, ab1);
        wf[u][0] = LD128(lane * 16 + u * 1024, wb); wf[u][1] = LD128(lane * 16 + u * 1024, wb1);
      }
      __builtin_amdgcn_s_setprio(1);
#pragma unroll 1
      for (int it = 1; it <= 7; ++it) {
        const unsigned nx = (it < 7) ? it * 4096u : 0u;
        if (it == 7) {
          lds_wait(sm, 25, 8u);
          const u32x4 dws = mk_desc(ws, WS_END);
#pragma unroll 1
          for (int u = 0; u < 8; ++u)
            dma16x2_sc1(dws, sb + (unsigned)(j * 16 + 2 * u) * 1024u, lane * 16u, WS_WT + (unsigned)((2 * cp + (u >> 2)) * 144 + 112 + 8 * j + 2 * (u & 3)) * 1024u);
        }
        if (it == 4 && j == 1) {
          glb_wait(rws, WS_FCL + (unsigned)((4 * bi + tm) * 32 + l31) * 8u, tag);
          *(LAS unsigned*)(sm + L_G + lane * 4) = __builtin_amdgcn_raw_buffer_load_b32(rws, WS_G + (unsigned)(256 * bi + 64 * tm + lane) * 4u, 0, AUX_SC1);
          WAITL0();
          if (lane == 0) lds_add(sm, 24, 1u);
        }
        const rsrc_t rws = mk_rsrc(ws, it < 7 ? WS_END : 0u);
#pragma unroll
        for (int u = 0; u < 4; ++u) {
#pragma unroll
          for (int t = 0; t < 2; ++t)
#pragma unroll
            for (int c = 0; c < 2; ++c)
              acc[t][c] = __builtin_amdgcn_mfma_f32_32x32x16_bf16(__builtin_bit_cast(bf16x8, wf[u][c]), __builtin_bit_cast(bf16x8, af[u][t]), acc[t][c], 0, 0, 0);
          af[u][0] = LD128(lane * 16 + u * 1024, ab + nx); af[u][1] = LD128(lane * 16 + u * 1024, ab1 + nx);
          wf[u][0] = LD128(lane * 16 + u * 1024, wb + nx); wf[u][1] = LD128(lane * 16 + u * 1024, wb1 + nx);
          __builtin_amdgcn_sched_barrier(0);
        }
      }
      WAITV(0);
      if (lane == 0) lds_add(sm, 24, 1u);
      __builtin_amdgcn_s_setprio(0);
      lds_wait(sm, 21, 8u);
      __builtin_amdgcn_s_setprio(3);
      LAS f32x4* xw = (LAS f32x4*)(sm + 65536 + j * 2048 + lane * 16);
#pragma unroll
      for (int u = 0; u < 16; ++u) {
        const f32x16& av = acc[u >> 3][(u >> 2) & 1];
        xw[(u >> 1) * 512 + (u & 1) * 64] = (f32x4){av[4 * (u & 3)], av[4 * (u & 3) + 1], av[4 * (u & 3) + 2], av[4 * (u & 3) + 3]};
      }
    }
    BARRIER();
    return;
  }

  const int w = wave;
  const unsigned ka = lane * 16;
  const unsigned va = l31 >= 16 ? 2048u : 1024u + (unsigned)(hh * 16 + l31) * 16u, va1 = l31 >= 16 ? 2048u : va + 512u;
  const int IDX = 0xDDDD;
  const bf16x8 qf = lds_frag(sm, L_Q + w * 1024 + lane * 16);
  f32x16 negm, O;
  {
    f32x16 z;
#pragma unroll
    for (int r = 0; r < 16; ++r) { z[r] = 0.f; O[r] = 0.f; }
    f32x16 s0 = __builtin_amdgcn_mfma_f32_32x32x16_bf16(lds_frag(sm, ka), qf, z, 0, 0, 0);
    float m = s0[0];
#pragma unroll
    for (int r = 1; r < 16; ++r) m = fmaxf(m, s0[r]);
    const auto sw = __builtin_amdgcn_permlane32_swap(__float_as_uint(m), __float_as_uint(m), false, false);
    m = fmaxf(__uint_as_float(sw[0]), __uint_as_float(sw[1]));
#pragma unroll
    for (int r = 0; r < 16; ++r) negm[r] = -m;
  }
  const int tm = hd >> 3, cp = hd & 7;
  const int tt = w & 1, kq = w >> 1;
  unsigned xwo = (unsigned)(tt * 16 + kq) * 2048u + lane * 16u, xro = (unsigned)(tt * 16 + kq * 4) * 2048u + lane * 16u;
  unsigned oo = (unsigned)(256 * bi + 64 * tm + 32 * tt + l31) * 1024u + (unsigned)(64 * cp + 16 * kq + 8 * hh) * 2u;
  unsigned wfo = (unsigned)kq * 16384u + lane * 16u, gao = L_G + (unsigned)(32 * tt + l31) * 4u;
  unsigned osb = WS_OF + (unsigned)((8 * bi + w) * 32 + hd) * 1024u, ob = WS_OF + (unsigned)((8 * bi + 2 * tm + tt) * 32 + 8 * kq) * 1024u;
  asm volatile("" : "+v"(xwo), "+v"(xro), "+v"(oo), "+v"(wfo), "+v"(gao), "+s"(osb), "+s"(ob));
  {
    u32x4 kf, v0, v1;
    unsigned kb = sb + ka, vb = sb + va, vb1 = sb + va1, fb = sb + L_FL + 4, ft;
    int cnt = 8, st, sp = 0x400000, sw = (w < 4) ? 2 : -1;
    if (w < 4) __builtin_amdgcn_s_setprio(2); else __builtin_amdgcn_s_setprio(1);
    asm volatile(ATTN_LOOP
                 : "+v"(O), "=&v"(kf), "=&v"(v0), "=&v"(v1), "+v"(kb), "+v"(vb), "+s"(cnt), "+v"(fb), "=&v"(ft), "=&s"(st), "+s"(sp), "+v"(vb1)
                 : "v"(qf), "v"(negm), "v"(IDX), "s"(sw)
                 : ATTN_CLOBS);
  }
  __builtin_amdgcn_s_setprio(3);
  u32x4 wf[2][8];
  const unsigned rdy = __hip_atomic_load((LAS unsigned*)(sm + L_FL) + 24, __ATOMIC_RELAXED, __HIP_MEMORY_SCOPE_WORKGROUP);
  CFENCE();
  float gv = *(LAS float*)(sm + gao);
#pragma unroll
  for (int u = 0; u < 16; ++u) wf[u >> 3][u & 7] = *(LAS u32x4*)(sm + wfo + u * 1024);
  CFENCE();
  float lsum = O[8];
  {
    float chk = lsum * 0.f;
#pragma unroll
    for (int r = 0; r < 8; ++r) chk = __builtin_fmaf(O[r], 0.f, chk);
    if (__any(chk != chk)) {
      get_tag();
      glb_wait(rws, WS_FKV + (unsigned)(hd * 32 + l31) * 8u, tag);
      float m = -3.0e38f;
      f32x16 z;
#pragma unroll
      for (int r = 0; r < 16; ++r) { z[r] = 0.f; O[r] = 0.f; }
#pragma unroll 1
      for (int t = 0; t < 64; ++t) {
        const unsigned go = KVOFF(hd, t >> 1) + (t & 1) * 2048u, vg = 1024u + (unsigned)(hh * 16 + l15) * 16u;
        const u32x4 kg = LD128(lane * 16, go);
        u32x4 va0 = LD128(vg, go), va1 = LD128(vg + 512, go);
        if (l31 >= 16) { va0 = ones; va1 = ones; }
        f32x16 S = __builtin_amdgcn_mfma_f32_32x32x16_bf16(__builtin_bit_cast(bf16x8, kg), qf, z, 0, 0, 0);
        float mx = m;
#pragma unroll
        for (int r = 0; r < 16; ++r) mx = fmaxf(mx, S[r]);
        mx = fmaxf(mx, __shfl_xor(mx, 32));
        const float al = __builtin_amdgcn_exp2f(m - mx);
        m = mx;
        f32x8 p0, p1;
#pragma unroll
        for (int r = 0; r < 16; ++r) O[r] *= al;
#pragma unroll
        for (int r = 0; r < 8; ++r) { p0[r] = __builtin_amdgcn_exp2f(S[r] - m); p1[r] = __builtin_amdgcn_exp2f(S[8 + r] - m); }
        O = __builtin_amdgcn_smfmac_f32_32x32x32_bf16(__builtin_bit_cast(bf16x8, va0), __builtin_bit_cast(bf16x16, p0), O, IDX, 0, 0);
        O = __builtin_amdgcn_smfmac_f32_32x32x32_bf16(__builtin_bit_cast(bf16x8, va1), __builtin_bit_cast(bf16x16, p1), O, IDX, 0, 0);
      }
      lsum = O[8];
    }
  }
  {
    const float sc = __builtin_amdgcn_rcpf(lsum);
    u32x4 o;
#pragma unroll
    for (int e = 0; e < 4; ++e) o[e] = min(pk_bf16(O[2 * e] * sc, O[2 * e + 1] * sc), 0xfffffffeu);
    ST128(o, lane * 16u, osb);
  }
  {
    LAS f32x4* xw = (LAS f32x4*)(sm + xwo);
    LAS f32x4* xr = (LAS f32x4*)(sm + xro);
    if ((unsigned)__builtin_amdgcn_readfirstlane(rdy) < 5u) {
      __builtin_amdgcn_s_setprio(0);
      lds_wait(sm, 24, 5u);
      __builtin_amdgcn_s_setprio(3);
      gv = *(LAS float*)(sm + gao);
#pragma unroll
      for (int u = 0; u < 16; ++u) wf[u >> 3][u & 7] = *(LAS u32x4*)(sm + wfo + u * 1024);
    }
    WAITL0();
    if (lane == 0) lds_add(sm, 21, 1u);
    __builtin_amdgcn_s_setprio(0); lds_wait(sm, 21, 8u); __builtin_amdgcn_s_setprio(3);
    WAITV(0);
    f32x16 acc[2];
#pragma unroll 1
    for (unsigned s = 0; s < SPIN_CAP; ++s) {
      CFENCE();
      u32x4 of[8];
#pragma unroll
      for (int u = 0; u < 8; ++u) { of[u] = LD128(lane * 16 + (u & 3) * 1024, ob + (u >> 2) * 4096); __builtin_amdgcn_sched_barrier(0); }
      __builtin_amdgcn_sched_barrier(0);
#pragma unroll
      for (int r = 0; r < 16; ++r) { acc[0][r] = 0.f; acc[1][r] = 0.f; }
      unsigned mx = 0;
#pragma unroll
      for (int u = 0; u < 8; ++u) {
        acc[0] = __builtin_amdgcn_mfma_f32_32x32x16_bf16(__builtin_bit_cast(bf16x8, wf[0][u]), __builtin_bit_cast(bf16x8, of[u]), acc[0], 0, 0, 0);
        acc[1] = __builtin_amdgcn_mfma_f32_32x32x16_bf16(__builtin_bit_cast(bf16x8, wf[1][u]), __builtin_bit_cast(bf16x8, of[u]), acc[1], 0, 0, 0);
        mx = max(max(mx, of[u][0]), of[u][1]);
        mx = max(max(mx, of[u][2]), of[u][3]);
        asm volatile("" : "+v"(acc[0]), "+v"(acc[1]), "+v"(mx));
      }
      if (__all(mx != 0xffffffffu)) break;
    }
#pragma unroll
    for (int p = 0; p < 4; ++p) {
      xw[p * 512] = (f32x4){acc[p >> 1][8 * (p & 1)], acc[p >> 1][8 * (p & 1) + 1], acc[p >> 1][8 * (p & 1) + 2], acc[p >> 1][8 * (p & 1) + 3]};
      xw[p * 512 + 64] = (f32x4){acc[p >> 1][8 * (p & 1) + 4], acc[p >> 1][8 * (p & 1) + 5], acc[p >> 1][8 * (p & 1) + 6], acc[p >> 1][8 * (p & 1) + 7]};
    }
    BARRIER();
    f32x4 pv[8], bv[8];
#pragma unroll
    for (int q = 0; q < 8; ++q) bv[q] = xr[4096 + q * 64];
#pragma unroll
    for (int q = 0; q < 8; ++q) pv[q] = xr[q * 64];
    const float om = 1.f - gv;
    const f32x4 a = ((bv[0] + bv[2]) + (bv[4] + bv[6])) * om + ((pv[0] + pv[2]) + (pv[4] + pv[6])) * gv;
    const f32x4 c = ((bv[1] + bv[3]) + (bv[5] + bv[7])) * om + ((pv[1] + pv[3]) + (pv[5] + pv[7])) * gv;
    const u32x4 o = {pk_bf16(a[0], a[1]), pk_bf16(a[2], a[3]), pk_bf16(c[0], c[1]), pk_bf16(c[2], c[3])};
    __builtin_amdgcn_raw_buffer_store_b128(o, mk_rsrc(out, 2048u * 1024u), oo, 0, 0);
  }
}

extern "C" void kernel_launch(void* const* d_in, const int* in_sizes, int n_in, void* d_out, int out_size,
                              void* d_ws, size_t ws_size, hipStream_t stream) {
  fused_kernel<<<256, 768, 0, stream>>>((const u16*)d_in[0], (const u16*)d_in[1], (const u16*)d_in[2], (const u16*)d_in[4],
                                        (const u16*)d_in[5], (const u16*)d_in[6], (const u16*)d_in[7], (u16*)d_out,
                                        (unsigned char*)d_ws);
}
```

```cpp
#define PADTEST 0
#define PADH 0
#include <hip/hip_runtime.h>
#include <hip/hip_bf16.h>

typedef unsigned short u16;
typedef unsigned long long u64;
typedef __bf16 bf16x8 __attribute__((ext_vector_type(8)));
typedef __bf16 bf16x16 __attribute__((ext_vector_type(16)));
typedef __bf16 bf16x2 __attribute__((ext_vector_type(2)));
typedef short s16x4 __attribute__((ext_vector_type(4)));
typedef float f32x2 __attribute__((ext_vector_type(2)));
typedef float f32x4 __attribute__((ext_vector_type(4)));
typedef float f32x8 __attribute__((ext_vector_type(8)));
typedef float f32x16 __attribute__((ext_vector_type(16)));
typedef unsigned u32x2 __attribute__((ext_vector_type(2)));
typedef unsigned u32x4 __attribute__((ext_vector_type(4)));
#define LAS __attribute__((address_space(3)))
#define GAS __attribute__((address_space(1)))
typedef __amdgpu_buffer_rsrc_t rsrc_t;

constexpr unsigned WS_EPOCH = 0;
constexpr unsigned WS_FKV = 4096;
constexpr unsigned WS_FACT = WS_FKV + 8192;
constexpr unsigned WS_FCL = WS_FACT + 8192;
constexpr unsigned WS_G = 32768;
constexpr unsigned WS_KV = 65536;
constexpr unsigned WS_ACT = WS_KV + 32u * 131072u;
constexpr unsigned WS_WT = WS_ACT + 64u * 114688u;
constexpr unsigned WS_OF = WS_WT + 16u * 144u * 1024u;
constexpr unsigned WS_CK = WS_OF + 64u * 32u * 1024u;
constexpr unsigned WS_END = WS_CK + 32u * 32u * 512u;
constexpr unsigned WS_DIAG = WS_END;

constexpr unsigned STG = 47104;
constexpr unsigned TILE = 2064;
constexpr unsigned L_RT = 3 * STG;
constexpr unsigned L_Q = L_RT + 8192;
constexpr unsigned L_B1 = L_Q + 8192;
constexpr unsigned L_G = L_B1 + 1024;
constexpr unsigned L_FL = L_G + 1024;
constexpr unsigned L_END = L_FL + 512;

constexpr unsigned SPIN_CAP = 1u << 18;
constexpr int AUX_SC1 = 16;
constexpr int FAST_N = 2;

__device__ __forceinline__ unsigned pk_bf16(float a, float b) {
  f32x2 v = {a, b};
  return __builtin_bit_cast(unsigned, __builtin_convertvector(v, bf16x2));
}
__device__ __forceinline__ unsigned ck_mix(u32x4 a, u32x4 b, unsigned seed) {
  unsigned s = seed;
#pragma unroll
  for (int e = 0; e < 4; ++e) {
    s = __builtin_rotateleft32(s, 5) + a[e];
    s = __builtin_rotateleft32(s, 11) ^ b[e];
  }
  return s;
}
__device__ __forceinline__ float bf_lo(unsigned u) { return __uint_as_float(u << 16); }
__device__ __forceinline__ float bf_hi(unsigned u) { return __uint_as_float(u & 0xffff0000u); }
__device__ __forceinline__ rsrc_t mk_rsrc(const void* p, unsigned bytes) {
  return __builtin_amdgcn_make_buffer_rsrc((void*)p, 0, bytes, 0x00020000);
}
__device__ __forceinline__ u32x4 mk_desc(const void* p, unsigned bytes) {
  const u64 a = (u64)p;
  return (u32x4){(unsigned)a, (unsigned)(a >> 32) & 0xffffu, bytes, 0x00020000u};
}
__device__ __forceinline__ void dma16(u32x4 desc, unsigned lds_addr, unsigned voff, unsigned soff) {
  asm volatile("s_mov_b32 m0, %0\n\ts_nop 3\n\tbuffer_load_dwordx4 %1, %2, %3 offen lds"
               :: "s"(lds_addr), "v"(voff), "s"(desc), "s"(soff) : "memory");
}
__device__ __forceinline__ void dma16x2_sc1(u32x4 desc, unsigned lds_addr, unsigned voff, unsigned soff) {
  asm volatile("s_mov_b32 m0, %0\n\ts_nop 3\n\tbuffer_load_dwordx4 %1, %2, %3 offen sc1 lds\n\tbuffer_load_dwordx4 %1, %2, %3 offen offset:1024 sc1 lds"
               :: "s"(lds_addr), "v"(voff), "s"(desc), "s"(soff) : "memory");
}
template <int OFF>
__device__ __forceinline__ void dma16_next(u32x4 desc, unsigned voff, unsigned soff) {
  asm volatile("buffer_load_dwordx4 %0, %1, %2 offen offset:%3 lds" :: "v"(voff), "s"(desc), "s"(soff), "i"(OFF) : "memory");
}
#define BARRIER() asm volatile("s_waitcnt lgkmcnt(0)\n\ts_barrier" ::: "memory")
#define WAITV(n) asm volatile("s_waitcnt vmcnt(" #n ")" ::: "memory")
#define WAITL0() asm volatile("s_waitcnt lgkmcnt(0)" ::: "memory")
#define CFENCE() asm volatile("" ::: "memory")
#define LD128(off, soff) __builtin_amdgcn_raw_buffer_load_b128(rws, off, soff, AUX_SC1)
#define ST128(v, off, soff) __builtin_amdgcn_raw_buffer_store_b128(v, rws, off, soff, AUX_SC1)

#define STAMP(k) do { if ((int)(k) == PADTEST && (wave >= 8) == PADH) { pad_t = __builtin_amdgcn_s_memrealtime(); asm volatile("s_waitcnt lgkmcnt(0)" ::: "memory"); } } while (0)
#define STAMP_OUT() do { for (int z_ = 0; z_ < 4000 && __builtin_amdgcn_s_memrealtime() < pad_t + 6000ull; ++z_) __builtin_amdgcn_s_sleep(2); } while (0)

__device__ __forceinline__ bf16x8 lds_frag(LAS char* sm, unsigned off) { return *(LAS bf16x8*)(sm + off); }
__device__ __forceinline__ bf16x8 lds_tr2(LAS char* sm, unsigned o0, unsigned o1) {
  s16x4 a = __builtin_amdgcn_ds_read_tr16_b64_v4i16((LAS s16x4*)(sm + o0));
  s16x4 b = __builtin_amdgcn_ds_read_tr16_b64_v4i16((LAS s16x4*)(sm + o1));
  return __builtin_bit_cast(bf16x8, __builtin_shufflevector(a, b, 0, 1, 2, 3, 4, 5, 6, 7));
}
__device__ __forceinline__ void lds_add(LAS char* sm, unsigned idx, unsigned v) {
  __hip_atomic_fetch_add((LAS unsigned*)(sm + L_FL) + idx, v, __ATOMIC_RELAXED, __HIP_MEMORY_SCOPE_WORKGROUP);
}
__device__ __forceinline__ void lds_wait(LAS char* sm, unsigned idx, unsigned want) {
#pragma unroll 1
  for (unsigned s = 0; s < SPIN_CAP * 16; ++s) {
    CFENCE();
    unsigned v = __hip_atomic_load((LAS unsigned*)(sm + L_FL) + idx, __ATOMIC_RELAXED, __HIP_MEMORY_SCOPE_WORKGROUP);
    if ((unsigned)__builtin_amdgcn_readfirstlane(v) >= want) break;
    __builtin_amdgcn_s_sleep(1);
  }
  CFENCE();
}
__device__ __forceinline__ void glb_wait(rsrc_t rs, unsigned off, u32x2 tag) {
#pragma unroll 1
  for (unsigned s = 0; s < SPIN_CAP; ++s) {
    CFENCE();
    u32x2 v = __builtin_amdgcn_raw_buffer_load_b64(rs, off, 0, AUX_SC1);
    if (__all(v.x == tag.x && v.y == tag.y)) break;
    __builtin_amdgcn_s_sleep(4);
  }
  CFENCE();
}

#define AE(b, i) "v_exp_f32 v[" #b "+" #i "], v[" #b "+" #i "]\n"
#define ATILE(cur, oth, t)                                                                                          \
  AE(cur, 0) AE(cur, 1)                                                                                              \
  "s_waitcnt lgkmcnt(2)\n v_smfmac_f32_32x32x32_bf16 %0, %3, v[" #oth "+8:" #oth "+15], %14\n"                       \
  "ds_read_b128 %3, %11 offset:2064*" #t "\n"                                                                        \
  AE(cur, 2) AE(cur, 3) AE(cur, 4) AE(cur, 5)                                                                        \
  "s_waitcnt lgkmcnt(2)\n v_mfma_f32_32x32x16_bf16 v[" #oth ":" #oth "+15], %1, %12, %13\n"                          \
  "ds_read_b128 %1, %4 offset:2064*(" #t "+2)\n"                                                                     \
  AE(cur, 6) AE(cur, 7) AE(cur, 8) AE(cur, 9) AE(cur, 10)                                                            \
  "s_waitcnt lgkmcnt(2)\n v_smfmac_f32_32x32x32_bf16 %0, %2, v[" #cur ":" #cur "+7], %14\n"                          \
  "ds_read_b128 %2, %5 offset:2064*(" #t "+1)\n"                                                                     \
  AE(cur, 11) AE(cur, 12) AE(cur, 13) AE(cur, 14) AE(cur, 15)
#define ATTN_LOOP                                                                                                   \
  "ds_read_b128 %1, %4\n ds_read_b128 %3, %5\n"                                                                      \
  "v_mov_b32 v120, 0\n v_mov_b32 v121, 0\n v_mov_b32 v122, 0\n v_mov_b32 v123, 0\n"                                  \
  "v_mov_b32 v124, 0\n v_mov_b32 v125, 0\n v_mov_b32 v126, 0\n v_mov_b32 v127, 0\n"                                  \
  "s_waitcnt lgkmcnt(0)\n v_mfma_f32_32x32x16_bf16 v[96:111], %1, %12, %13\n"                                        \
  "ds_read_b128 %1, %4 offset:2064\n ds_read_b128 %2, %5\n s_nop 11\n"                                               \
  "1:\n"                                                                                                             \
  ATILE(96, 112, 0) ATILE(112, 96, 1) ATILE(96, 112, 2) ATILE(112, 96, 3) ATILE(96, 112, 4)                          \
  "ds_read_b32 %8, %7\n"                                                                                             \
  ATILE(112, 96, 5)                                                                                                  \
  "v_readfirstlane_b32 %9, %8\n s_nop 3\n s_cmp_eq_u32 %9, 8\n s_cbranch_scc1 3f\n"         \
  "2:\n s_sleep 1\n ds_read_b32 %8, %7\n s_waitcnt lgkmcnt(0)\n v_readfirstlane_b32 %9, %8\n s_sub_u32 %10, %10, 1\n" \
  "s_cmp_le_i32 %10, 0\n s_cbranch_scc1 3f\n s_cmp_eq_u32 %9, 8\n s_cbranch_scc0 2b\n"                               \
  "3:\n"                                                                                                             \
  ATILE(96, 112, 6) ATILE(112, 96, 7)                                                                                \
  "v_add_u32 %4, 8*2064, %4\n v_add_u32 %5, 8*2064, %5\n v_add_u32 %11, 8*2064, %11\n v_add_u32 %7, 4, %7\n s_sub_u32 %6, %6, 1\n"                \
  "s_cmp_lg_u32 %6, 4\n s_cbranch_scc1 4f\n v_mov_b32 %8, 1\n s_mov_b64 exec, 1\n ds_add_u32 %7, %8 offset:80\n s_mov_b64 exec, -1\n 4:\n" \
  "s_cmp_lg_u32 %6, %15\n s_cbranch_scc1 5f\n s_setprio 0\n 5:\n"                                                    \
  "s_cmp_lg_u32 %6, 0\n s_cbranch_scc1 1b\n"                                                                         \
  "s_waitcnt lgkmcnt(0)\n v_smfmac_f32_32x32x32_bf16 %0, %3, v[120:127], %14\n s_nop 15\n"
#define ATTN_CLOBS "v96","v97","v98","v99","v100","v101","v102","v103","v104","v105","v106","v107","v108","v109","v110","v111", \
  "v112","v113","v114","v115","v116","v117","v118","v119","v120","v121","v122","v123","v124","v125","v126","v127","scc","memory"
#define KVOFF(h, g) (WS_KV + (unsigned)((h) * 32 + (g)) * 4096u)

__global__ void __launch_bounds__(768, 3)
fused_kernel(const u16* __restrict__ x, const u16* __restrict__ wqkv, const u16* __restrict__ wo,
             const u16* __restrict__ w1, const u16* __restrict__ b1, const u16* __restrict__ w2,
             const u16* __restrict__ gw, u16* __restrict__ out, unsigned char* __restrict__ ws) {
  __shared__ __attribute__((aligned(16))) char smem_[L_END];
  LAS char* sm = (LAS char*)smem_;
  const unsigned sb = (unsigned)(size_t)sm;
  asm volatile("" :: "s"(x), "s"(wqkv), "s"(wo), "s"(w1), "s"(b1), "s"(w2), "s"(gw), "s"(out), "s"(ws));
  const int lane = threadIdx.x & 63;
  const int wave = __builtin_amdgcn_readfirstlane(threadIdx.x >> 6);
  const int b = blockIdx.x, xcd = b & 7, idx = b >> 3;
  const int bi = 4 * (xcd & 1) + (idx & 3), hd = 8 * (xcd >> 1) + (idx >> 2);
  const bool has_w1 = (hd & 7) != 7;
  const int cw = hd - (hd >> 3);
  const int rot = ((hd + bi) & 1) * 4;
  const int hh = lane >> 5, l31 = lane & 31, l15 = lane & 15, g4 = lane >> 4;
  const int q4 = l15 >> 2, p4 = lane & 3, r8 = lane >> 3, sl8 = lane & 7;
  if (wave < 8) {
    const u32x4 dx = mk_desc(x, 2048u * 1024u);
    const unsigned xo0 = (unsigned)(256 * bi + 32 * wave + r8) * 1024u + (unsigned)(sl8 ^ (r8 >> 1)) * 16u, xo1 = (xo0 ^ 64u) + 7168u;
    dma16(dx, sb + wave * 4096u, xo0, rot * 128u);
    dma16_next<1024>(dx, xo1, rot * 128u);
    dma16_next<2048>(dx, xo0, rot * 128u + 14336u);
    dma16_next<3072>(dx, xo1, rot * 128u + 14336u);
  }
  __builtin_amdgcn_sched_barrier(0);
  const rsrc_t rws = mk_rsrc(ws, WS_END);
  const u32x4 ones = {0x3f803f80u, 0x3f803f80u, 0x3f803f80u, 0x3f803f80u};
  u64 pad_t = 0;
  STAMP(0);

  auto mk_tag = [&](u32x2 e) {
    u64 z = ((u64)(unsigned)__builtin_amdgcn_readfirstlane(e.y) << 32 | (u64)(unsigned)__builtin_amdgcn_readfirstlane(e.x)) + 0x9E3779B97F4A7C15ull;
    z = (z ^ (z >> 30)) * 0xBF58476D1CE4E5B9ull;
    z = (z ^ (z >> 27)) * 0x94D049BB133111EBull;
    z ^= z >> 31;
    return (u32x2){(unsigned)z, (unsigned)(z >> 32)};
  };
  u32x2 tag;
  auto get_tag = [&]() {
    const u32x2 t = *(LAS u32x2*)(sm + L_FL + 256);
    tag = (u32x2){(unsigned)__builtin_amdgcn_readfirstlane(t.x), (unsigned)__builtin_amdgcn_readfirstlane(t.y)};
  };
  const unsigned xa0 = (unsigned)(64 * (wave & 3) + l15) * 128u + (unsigned)(g4 ^ (l15 >> 1)) * 16u;
  const unsigned xa1 = xa0 ^ 64u;
#define NEXT_BUF(v) v = (v == 2 * STG) ? 0 : v + STG
  const unsigned qo = (unsigned)(16 * (wave & 3) + (((lane >> 1) & 15) ^ ((g4 & 1) << 2))) * 3072u + (unsigned)hh * 1024u + (unsigned)hd * 32u + (lane & 1) * 16u;
  auto qkv_issue = [&](int n, unsigned buf) {
    const u32x4 d = mk_desc(wqkv, 512u * 3072u);
    const unsigned so = ((unsigned)(n + rot) & 7u) * (64u * 3072u);
    dma16(d, sb + buf + 32768u + (wave & 3) * 3584u, qo, so);
    if (lane < 32) dma16_next<1024>(d, qo, so + 1024u);
  };

  if (__builtin_expect(wave >= 4 && wave < 8, 1)) {
    const int lw = wave - 4;
    const u32x4 dx = mk_desc(x, 2048u * 1024u);
    const unsigned xo0 = (unsigned)(256 * bi + 64 * lw + r8) * 1024u + (unsigned)(sl8 ^ (r8 >> 1)) * 16u;
    const unsigned xo1 = (xo0 ^ 64u) + 8192u - 1024u;
    const u32x4 d1 = mk_desc(w1, 512u * 3584u);
    const unsigned wo0 = (unsigned)(16 * lw + r8) * 3584u + (unsigned)cw * 128u + (unsigned)(sl8 ^ (r8 & 2)) * 16u;
    const unsigned wo1 = (wo0 ^ 64u) + 8u * 3584u - 1024u;
    unsigned buf = 0;
    STAMP(16);
#pragma unroll 1
    for (int n = 0; n < 8; ++n) {
      const unsigned ks = (unsigned)(n + rot) & 7u, d = sb + buf + lw * 8192u;
      if (n < 2) qkv_issue(n, buf);
      if (n)
#pragma unroll
      for (int u = 0; u < 2; ++u) {
        dma16(dx, d + u * 4096u, xo0, ks * 128u + u * 32768u);
        dma16_next<1024>(dx, xo1, ks * 128u + u * 32768u);
        dma16_next<2048>(dx, xo0, ks * 128u + u * 32768u + 16384u - 2048u);
        dma16_next<3072>(dx, xo1, ks * 128u + u * 32768u + 16384u - 2048u);
      }
      if (has_w1) {
        dma16(d1, sb + buf + 32768u + 1536u + lw * 3584u, wo0, ks * (64u * 3584u));
        dma16_next<1024>(d1, wo1, ks * (64u * 3584u));
      }
      if (n == 0 && lw == 0) {
        if (has_w1) { if (lane < 8) dma16(mk_desc(b1, 3584u), sb + L_B1, lane * 16u + (unsigned)cw * 128u, 0u); }
        else dma16(mk_desc(gw, 1024u), sb + L_G, lane * 16u, 0u);
      }
      if (n == 5) {
        if (has_w1) {
          const u32x4 d2 = mk_desc(w2, 1792u * 1024u);
          const unsigned o2 = (unsigned)(16 * (r8 >> 2) + (r8 & 3)) * 1024u + (unsigned)bi * 128u + (unsigned)sl8 * 16u;
          const unsigned so = (unsigned)(64 * cw + 32 * (lw >> 1) + 8 * (lw & 1)) * 1024u;
          dma16(d2, sb + L_RT + lw * 2048u, o2, so); dma16_next<1024>(d2, o2, so + 3072u);
        } else {
          const u32x4 dO = mk_desc(wo, 512u * 1024u);
          const unsigned oo = (unsigned)r8 * 1024u + (unsigned)bi * 128u + (unsigned)sl8 * 16u;
          const unsigned so = (unsigned)((hd >> 3) * 8 + 2 * lw) * 16384u;
          dma16(dO, sb + L_RT + lw * 2048u, oo, so); dma16_next<1024>(dO, oo, so + 7168u);
          dma16(dO, sb + 2 * STG + 32768u + 1536u + lw * 3584u, oo, so + 16384u); dma16_next<1024>(dO, oo, so + 16384u + 7168u);
        }
      }
      NEXT_BUF(buf);
      if (n >= 1) {
        if (n == 5) WAITV(12); else if (n == 1) { if (has_w1) WAITV(12); else WAITV(10); } else if (has_w1) WAITV(10); else WAITV(8);
        if (n == 1) STAMP(20);
        BARRIER();
        if (n == 1) STAMP(24);
      }
    }
    WAITV(0);
    BARRIER();
    if (lane < 2) *(LAS u32x4*)(sm + (2 * lw + lane) * TILE + 2048) = ones;
    BARRIER();
  } else if (wave < 4) {
    const int j = wave;
    __builtin_amdgcn_s_setprio(3);
    if (wave == 0) ((LAS unsigned*)(sm + L_FL))[lane] = 0u;
    f32x4 acc[4][3];
#pragma unroll
    for (int t = 0; t < 4; ++t)
#pragma unroll
      for (int c = 0; c < 3; ++c) acc[t][c] = (f32x4){0.f, 0.f, 0.f, 0.f};
    const unsigned wa0 = 32768u + (unsigned)(g4 >> 1) * 3584u + (unsigned)(12 * (g4 & 1) + q4) * 32u + p4 * 8u;
    const unsigned wa1 = wa0 ^ 128u;
    bf16x8 xA[4], wA[3], xB[4], wB[3];
#pragma unroll
    for (int t = 0; t < 4; ++t) xB[t] = __builtin_bit_cast(bf16x8, (u32x4){0u, 0u, 0u, 0u});
#pragma unroll
    for (int c = 0; c < 3; ++c) wB[c] = __builtin_bit_cast(bf16x8, (u32x4){0u, 0u, 0u, 0u});
#define QKV_RD(X, W, kk)                                                                                           \
    _Pragma("unroll") for (int c = 0; c < 3; ++c) W[c] = lds_tr2(sm, buf + wa0 + kk * 7168 + c * 512, buf + wa1 + kk * 7168 + c * 512); \
    _Pragma("unroll") for (int t = 0; t < 4; ++t) X[t] = lds_frag(sm, buf + (kk ? xa1 : xa0) + t * 2048);
#define QKV_MM(X, W)                                                                                               \
    _Pragma("unroll") for (int t = 0; t < 4; ++t) {                                                                \
      acc[t][0] = __builtin_amdgcn_mfma_f32_16x16x32_bf16(W[0], X[t], acc[t][0], 0, 0, 0);                         \
      acc[t][1] = __builtin_amdgcn_mfma_f32_16x16x32_bf16(W[1], X[t], acc[t][1], 0, 0, 0);                         \
      acc[t][2] = __builtin_amdgcn_mfma_f32_16x16x32_bf16(X[t], W[2], acc[t][2], 0, 0, 0);                         \
    }                                                                                                              \
    _Pragma("unroll") for (int u = 0; u < 10; ++u) { __builtin_amdgcn_sched_group_barrier(0x008, 1, 0); __builtin_amdgcn_sched_group_barrier(0x100, 1, 0); } \
    __builtin_amdgcn_sched_group_barrier(0x008, 2, 0);                                                             \
    asm volatile("" : "+v"(acc[0][0]), "+v"(acc[0][1]), "+v"(acc[0][2]), "+v"(acc[1][0]), "+v"(acc[1][1]), "+v"(acc[1][2]), \
                      "+v"(acc[2][0]), "+v"(acc[2][1]), "+v"(acc[2][2]), "+v"(acc[3][0]), "+v"(acc[3][1]), "+v"(acc[3][2]) :: "memory"); \
    __builtin_amdgcn_sched_barrier(0);
    unsigned vo = 1024u + (unsigned)(g4 & 1) * 512u + (unsigned)((g4 >> 1) * 16 + (l15 & 3) + 8 * ((l15 >> 2) & 1) + 4 * (l15 >> 3)) * 16u;
    unsigned eq = L_Q + 2 * j * 1024 + lane * 16, ek = 2 * j * TILE + lane * 16, ev = 2 * j * TILE + vo;
    unsigned go = KVOFF(hd, bi * 4 + j);
    asm volatile("" : "+v"(vo), "+v"(eq), "+v"(ek), "+v"(ev), "+s"(go));
    unsigned buf = 0, nbuf = 2 * STG;
    WAITV(0);
#pragma unroll 1
    for (int n = 0; n < 8; ++n) {
      BARRIER();
      STAMP(16 + n);
      QKV_RD(xA, wA, 0)
      QKV_MM(xB, wB)
      if (n < 6) qkv_issue(n + 2, nbuf);
      QKV_RD(xB, wB, 1)
      QKV_MM(xA, wA)
      NEXT_BUF(buf); NEXT_BUF(nbuf);
      if (n < 6) WAITV(2); else WAITV(0);
    }
    QKV_MM(xB, wB)
    STAMP(1);
    u32x4 pub[3][2];
#pragma unroll
    for (int c = 2; c >= 0; --c)
#pragma unroll
      for (int tp = 0; tp < 2; ++tp) {
        const float sc = c ? 1.f : 0.25f * 1.4426950408889634f;
        const f32x4 a = acc[2 * tp][c] * sc, e = acc[2 * tp + 1][c] * sc;
        const auto r0 = __builtin_amdgcn_permlane16_swap(pk_bf16(a[0], a[1]), pk_bf16(e[0], e[1]), false, false);
        const auto r1 = __builtin_amdgcn_permlane16_swap(pk_bf16(a[2], a[3]), pk_bf16(e[2], e[3]), false, false);
        pub[c][tp] = (u32x4){r0[0], r1[0], r0[1], r1[1]};
        if (c) ST128(pub[c][tp], (c == 1 ? lane * 16u : vo) + tp * 2048, go);
        *(LAS u32x4*)(sm + (c == 0 ? eq + tp * 1024 : (c == 1 ? ek : ev) + tp * TILE)) = pub[c][tp];
      }
    asm volatile("" :: "v"(pub[1][0]), "v"(pub[1][1]), "v"(pub[2][0]), "v"(pub[2][1]));
    STAMP(10);
    BARRIER();
    get_tag();
    {
      const unsigned co = WS_CK + (unsigned)(hd * 32 + bi * 4 + j) * 512u;
      __builtin_amdgcn_raw_buffer_store_b32(ck_mix(pub[1][0], pub[1][1], tag.x), rws, lane * 8u, co, AUX_SC1);
      __builtin_amdgcn_raw_buffer_store_b32(ck_mix(pub[2][0], pub[2][1], tag.y), rws, ((vo - 1024u) >> 1) + 4u, co, AUX_SC1);
    }
    WAITV(0);
    if (lane == 0) __builtin_amdgcn_raw_buffer_store_b64(tag, rws, WS_FKV + (unsigned)((hd * 8 + bi) * 4 + j) * 8u, 0, AUX_SC1);
    STAMP(11);
  } else {
    const int j = wave - 8;
    u32x2 ep = {0u, 0u};
    if (j == 0) ep = __builtin_amdgcn_raw_buffer_load_b64(rws, WS_EPOCH, 0, AUX_SC1);
    auto put_tag = [&]() { if (j == 0) *(LAS u32x2*)(sm + L_FL + 256) = mk_tag(ep); };
    auto retile = [&](unsigned img, int ks) {
      const unsigned ta = img + (unsigned)(8 * hh + q4) * 128u + (unsigned)(4 * (g4 & 1) + 2 * (p4 & 1) + (p4 >> 1)) * 8u;
      ST128(__builtin_bit_cast(u32x4, lds_tr2(sm, ta, ta + 512)), WS_WT + lane * 16, (2 * bi * 144 + ks) * 1024);
      ST128(__builtin_bit_cast(u32x4, lds_tr2(sm, ta + 64, ta + 576)), WS_WT + lane * 16, ((2 * bi + 1) * 144 + ks) * 1024);
    };
    auto clear_o = [&]() {
      const u32x4 inv = {0xffffffffu, 0xffffffffu, 0xffffffffu, 0xffffffffu};
      const unsigned oo = WS_OF + (unsigned)((8 * bi + 2 * j) * 32 + hd) * 1024u + lane * 16u;
      ST128(inv, oo, 0); ST128(inv, oo + 32768, 0);
    };
    const unsigned fkv = WS_FKV + (unsigned)((hd * 8 + ((bi + lane) & 7)) * 4 + j) * 8u;
    u32x4 fd[FAST_N][4];
    u32x2 fc[FAST_N];
    auto fast_load = [&](int u) __attribute__((always_inline)) {
      const unsigned g = (unsigned)(((bi + 1 + u) & 7) * 4 + j);
      fc[u] = __builtin_amdgcn_raw_buffer_load_b64(rws, lane * 8u, WS_CK + (unsigned)(hd * 32 + g) * 512u, AUX_SC1);
#pragma unroll
      for (int e = 0; e < 4; ++e) fd[u][e] = LD128(lane * 16 + e * 1024, KVOFF(hd, g));
    };
    auto fast_take = [&]() __attribute__((always_inline)) {
#pragma unroll
      for (int u = 0; u < FAST_N; ++u) {
#pragma unroll 1
        for (unsigned s = 0; s < SPIN_CAP; ++s) {
          if (__all(ck_mix(fd[u][0], fd[u][2], tag.x) == fc[u].x && ck_mix(fd[u][1], fd[u][3], tag.y) == fc[u].y)) break;
          CFENCE();
#pragma unroll
          for (int v = u; v < FAST_N; ++v) fast_load(v);
        }
        LAS char* dst = sm + ((1 + u) * 8 + 2 * j) * TILE + lane * 16;
#pragma unroll
        for (int e = 0; e < 4; ++e) *(LAS u32x4*)(dst + (e >> 1) * TILE + (e & 1) * 1024) = fd[u][e];
        WAITL0();
        if (lane == 0) lds_add(sm, 1 + u, 2u);
        if (u == 0) STAMP(7);
      }
    };
    auto gather = [&](auto pre) __attribute__((always_inline)) {
      u32x2 fv = __builtin_amdgcn_raw_buffer_load_b64(rws, fkv, 0, AUX_SC1);
      fast_take();
      const u32x4 dws = mk_desc(ws, WS_END);
      unsigned spins = 0;
      bool done = false;
#pragma unroll 1
      for (int n0 = 1 + FAST_N; n0 < 8;) {
        const unsigned rdy = ((unsigned)__builtin_amdgcn_ballot_w64(fv.x == tag.x && fv.y == tag.y) & 0xffu) >> n0;
        int cnt = __builtin_ctz(~rdy);
        if (++spins > SPIN_CAP) cnt = 8 - n0;
        CFENCE();
#pragma unroll 1
        for (int u = n0; u < n0 + cnt; ++u) {
          const unsigned go = KVOFF(hd, ((bi + u) & 7) * 4 + j), d = sb + (unsigned)(u * 8 + 2 * j) * TILE;
          const unsigned gu = __builtin_amdgcn_readfirstlane(go), du = __builtin_amdgcn_readfirstlane(d);
          dma16x2_sc1(dws, du, lane * 16u, gu);
          dma16x2_sc1(dws, du + TILE, lane * 16u, gu + 2048u);
        }
        if (n0 + cnt < 8) {
          if (!cnt) __builtin_amdgcn_s_sleep(4);
          fv = __builtin_amdgcn_raw_buffer_load_b64(rws, fkv, 0, AUX_SC1);
        }
        WAITV(0);
        if (lane < cnt) lds_add(sm, n0 + lane, 2u);
        n0 += cnt;
        if (!done && n0 > 4) {
          pre();
          WAITV(0);
          if (lane == 0) __builtin_amdgcn_raw_buffer_store_b64(tag, rws, WS_FACT + (unsigned)((bi * 4 + j) * 32 + hd) * 8u, 0, AUX_SC1);
          STAMP(6);
          done = true;
        }
      }
    };
    auto put_ones = [&]() { if (lane < 14) *(LAS u32x4*)(sm + ((1 + (lane >> 1)) * 8 + 2 * j + (lane & 1)) * TILE + 2048) = ones; };
    if (has_w1) {
      f32x4 acc[4][4];
      const unsigned wa = 32768u + 1536u + (unsigned)(g4 >> 1) * 3584u + (unsigned)(8 * (g4 & 1) + q4) * 128u +
                          (unsigned)((q4 >> 1) | ((g4 & 1) << 1)) * 32u + p4 * 8u;
      bf16x8 xA[4], wA[4], xB[4], wB[4];
#pragma unroll
      for (int t = 0; t < 4; ++t) { xB[t] = __builtin_bit_cast(bf16x8, (u32x4){0u, 0u, 0u, 0u}); wB[t] = xB[t]; }
#define W1_RD(X, W, kk)                                                                                            \
      _Pragma("unroll") for (int c = 0; c < 4; ++c) W[c] = lds_tr2(sm, buf + (wa ^ (unsigned)(c * 32)) + kk * 7168, buf + (wa ^ (unsigned)(c * 32)) + kk * 7168 + 512); \
      _Pragma("unroll") for (int t = 0; t < 4; ++t) X[t] = lds_frag(sm, buf + (kk ? xa1 : xa0) + t * 2048);
#define W1_MM(X, W)                                                                                                \
      _Pragma("unroll") for (int t = 0; t < 4; ++t)                                                                \
        _Pragma("unroll") for (int c = 0; c < 4; ++c) acc[t][c] = __builtin_amdgcn_mfma_f32_16x16x32_bf16(W[c], X[t], acc[t][c], 0, 0, 0); \
      _Pragma("unroll") for (int u = 0; u < 12; ++u) { __builtin_amdgcn_sched_group_barrier(0x008, 1, 0); __builtin_amdgcn_sched_group_barrier(0x100, 1, 0); } \
      __builtin_amdgcn_sched_group_barrier(0x008, 4, 0);                                                           \
      asm volatile("" : "+v"(acc[0][0]), "+v"(acc[0][1]), "+v"(acc[0][2]), "+v"(acc[0][3]), "+v"(acc[1][0]), "+v"(acc[1][1]), "+v"(acc[1][2]), "+v"(acc[1][3]), \
                        "+v"(acc[2][0]), "+v"(acc[2][1]), "+v"(acc[2][2]), "+v"(acc[2][3]), "+v"(acc[3][0]), "+v"(acc[3][1]), "+v"(acc[3][2]), "+v"(acc[3][3]) :: "memory"); \
      __builtin_amdgcn_sched_barrier(0);
      unsigned buf = 0;
      BARRIER();
#pragma unroll
      for (int c = 0; c < 4; ++c) {
        const u32x2 bv = *(LAS u32x2*)(sm + L_B1 + (16 * c + 4 * g4) * 2);
#pragma unroll
        for (int t = 0; t < 4; ++t) acc[t][c] = (f32x4){bf_lo(bv.x), bf_hi(bv.x), bf_lo(bv.y), bf_hi(bv.y)};
      }
#pragma unroll 1
      for (int n = 0; n < 8; ++n) {
        W1_RD(xA, wA, 0)
        W1_MM(xB, wB)
        W1_RD(xB, wB, 1)
        W1_MM(xA, wA)
        NEXT_BUF(buf);
        if (n == 2) put_tag();
        if (n < 7) BARRIER();
      }
      BARRIER();
      __builtin_amdgcn_s_setprio(3);
      get_tag();
      put_ones();
      W1_MM(xB, wB)
      STAMP(1);
      u32x4 actv[4][2];
      auto mish1 = [&](int t) __attribute__((always_inline)) {
#pragma unroll
          for (int pc = 0; pc < 2; ++pc) {
            u32x4 o;
#pragma unroll
            for (int e = 0; e < 4; ++e) {
              f32x2 hv = {acc[t][2 * pc + (e >> 1)][2 * (e & 1)], acc[t][2 * pc + (e >> 1)][2 * (e & 1) + 1]};
              f32x2 a = hv * 1.4426950408889634f;
              f32x2 ex = {__builtin_amdgcn_exp2f(a.x), __builtin_amdgcn_exp2f(a.y)};
              f32x2 d = ex * (ex + 2.f) + 2.f;
              f32x2 r = {__builtin_amdgcn_rcpf(d.x), __builtin_amdgcn_rcpf(d.y)};
              f32x2 y = hv * (r * -2.f + 1.f);
              o[e] = pk_bf16(y.x, y.y);
            }
            asm volatile("" : "+v"(o));
            actv[t][pc] = o;
          }
      };
      mish1(0); mish1(1);
      mish1(2);
#pragma unroll
      for (int u = 0; u < FAST_N; ++u) fast_load(u);
      mish1(3);
      STAMP(2);
      gather([&]() __attribute__((always_inline)) {
        const unsigned ao = WS_ACT + (unsigned)(8 * bi + 2 * j) * 114688u + (unsigned)cw * 4096u + (unsigned)g4 * 512u + l15 * 16u;
#pragma unroll
        for (int t = 0; t < 4; ++t)
#pragma unroll
          for (int pc = 0; pc < 2; ++pc) { ST128(actv[t][pc], ao + (t >> 1) * 114688 + pc * 2048 + (t & 1) * 256, 0); }
        retile(L_RT + j * 2048u, 4 * cw + j);
      });
    } else {
      f32x4 acc[4];
#pragma unroll
      for (int t = 0; t < 4; ++t) acc[t] = (f32x4){0.f, 0.f, 0.f, 0.f};
      unsigned buf = 0;
#pragma unroll 1
      for (int n = 0; n < 8; ++n) {
        BARRIER();
        if (hd == 7) {
#pragma unroll
          for (int kk = 0; kk < 2; ++kk) {
            const bf16x8 gf = lds_frag(sm, L_G + ((n + rot) & 7) * 128 + kk * 64 + g4 * 16);
#pragma unroll
            for (int t = 0; t < 4; ++t) acc[t] = __builtin_amdgcn_mfma_f32_16x16x32_bf16(gf, lds_frag(sm, buf + (kk ? xa1 : xa0) + t * 2048), acc[t], 0, 0, 0);
          }
        }
        if (n == 2) put_tag();
        if (n == 5 || n == 6) retile(n == 5 ? L_RT + j * 2048u : 2 * STG + 32768u + 1536u + j * 3584u, 112 + (hd >> 3) * 8 + 2 * j + (n - 5));
        NEXT_BUF(buf);
      }
      WAITV(0);
      get_tag();
      BARRIER();
      __builtin_amdgcn_s_setprio(3);
      put_ones();
      STAMP(1);
      if (hd == 7) {
        const float lg = g4 == 0 ? acc[0][0] : g4 == 1 ? acc[1][0] : g4 == 2 ? acc[2][0] : acc[3][0];
        const float gv = __builtin_amdgcn_rcpf(1.f + __builtin_amdgcn_exp2f(lg * -1.4426950408889634f));
        __builtin_amdgcn_raw_buffer_store_b32(__float_as_uint(gv), rws, WS_G + (unsigned)(256 * bi + 64 * j + lane) * 4u, 0, AUX_SC1);
      }
      STAMP(2);
      __builtin_amdgcn_s_sleep(16);
#pragma unroll
      for (int u = 0; u < FAST_N; ++u) fast_load(u);
      gather([&]() {});
    }
    STAMP(3);
    clear_o();
    WAITV(0);
    if (lane == 0) { __builtin_amdgcn_raw_buffer_store_b64(tag, rws, WS_FCL + (unsigned)((bi * 4 + j) * 32 + hd) * 8u, 0, AUX_SC1); lds_add(sm, 8, 2u); }
    const int tm = hd >> 3, cp = hd & 7;
    if (j == 0) {
      const unsigned o1 = WS_FACT + (unsigned)((4 * bi + tm) * 32 + l31) * 8u;
#pragma unroll 1
      for (unsigned s = 0; s < SPIN_CAP; ++s) {
        CFENCE();
        const u32x2 v = __builtin_amdgcn_raw_buffer_load_b64(rws, o1, 0, AUX_SC1);
        const u32x4 y = LD128(WS_FACT + (unsigned)cp * 1024u + lane * 16u, 0);
        if (__all(v.x == tag.x && v.y == tag.y && y[0] == tag.x && y[1] == tag.y && y[2] == tag.x && y[3] == tag.y)) break;
        __builtin_amdgcn_s_sleep(4);
      }
      CFENCE();
      if (lane == 0) lds_add(sm, 18, 1u);
    } else {
      if (b == 0 && j == 2) {
#pragma unroll 1
        for (int u = 0; u < 4; ++u) glb_wait(rws, WS_FKV + (unsigned)(64 * u + lane) * 32u, tag);
        if (lane == 0) __builtin_amdgcn_raw_buffer_store_b64(tag, rws, WS_EPOCH, 0, AUX_SC1);
      }
      lds_wait(sm, 18, 1u);
    }
    STAMP(4);
    {
      f32x16 acc[2][2];
#pragma unroll
      for (int t = 0; t < 2; ++t)
#pragma unroll
        for (int c = 0; c < 2; ++c)
#pragma unroll
          for (int r = 0; r < 16; ++r) acc[t][c][r] = 0.f;
      const unsigned ab = WS_ACT + (unsigned)(8 * bi + 2 * tm) * 114688u + (unsigned)(28 * j) * 1024u;
      const unsigned ab1 = WS_ACT + (unsigned)(8 * bi + 2 * tm + 1) * 114688u + (unsigned)(28 * j) * 1024u;
      const unsigned wb = WS_WT + (unsigned)((2 * cp) * 144 + 28 * j) * 1024u;
      const unsigned wb1 = WS_WT + (unsigned)((2 * cp + 1) * 144 + 28 * j) * 1024u;
      u32x4 af[4][2], wf[4][2];
#pragma unroll
      for (int u = 0; u < 4; ++u) {
        af[u][0] = LD128(lane * 16 + u * 1024, ab); af[u][1] = LD128(lane * 16 + u * 1024, ab1);
        wf[u][0] = LD128(lane * 16 + u * 1024, wb); wf[u][1] = LD128(lane * 16 + u * 1024, wb1);
      }
      __builtin_amdgcn_s_setprio(1);
#pragma unroll 1
      for (int it = 1; it <= 7; ++it) {
        const unsigned nx = (it < 7) ? it * 4096u : 0u;
        if (it == 7) {
          lds_wait(sm, 25, 8u);
          const u32x4 dws = mk_desc(ws, WS_END);
#pragma unroll 1
          for (int u = 0; u < 8; ++u)
            dma16x2_sc1(dws, sb + (unsigned)(j * 16 + 2 * u) * 1024u, lane * 16u, WS_WT + (unsigned)((2 * cp + (u >> 2)) * 144 + 112 + 8 * j + 2 * (u & 3)) * 1024u);
        }
        if (it == 4 && j == 1) {
          glb_wait(rws, WS_FCL + (unsigned)((4 * bi + tm) * 32 + l31) * 8u, tag);
          *(LAS unsigned*)(sm + L_G + lane * 4) = __builtin_amdgcn_raw_buffer_load_b32(rws, WS_G + (unsigned)(256 * bi + 64 * tm + lane) * 4u, 0, AUX_SC1);
          WAITL0();
          if (lane == 0) lds_add(sm, 24, 1u);
        }
        const rsrc_t rws = mk_rsrc(ws, it < 7 ? WS_END : 0u);
#pragma unroll
        for (int u = 0; u < 4; ++u) {
#pragma unroll
          for (int t = 0; t < 2; ++t)
#pragma unroll
            for (int c = 0; c < 2; ++c)
              acc[t][c] = __builtin_amdgcn_mfma_f32_32x32x16_bf16(__builtin_bit_cast(bf16x8, wf[u][c]), __builtin_bit_cast(bf16x8, af[u][t]), acc[t][c], 0, 0, 0);
          af[u][0] = LD128(lane * 16 + u * 1024, ab + nx); af[u][1] = LD128(lane * 16 + u * 1024, ab1 + nx);
          wf[u][0] = LD128(lane * 16 + u * 1024, wb + nx); wf[u][1] = LD128(lane * 16 + u * 1024, wb1 + nx);
          __builtin_amdgcn_sched_barrier(0);
        }
      }
      STAMP(8);
      WAITV(0);
      if (lane == 0) lds_add(sm, 24, 1u);
      __builtin_amdgcn_s_setprio(0);
      lds_wait(sm, 21, 8u);
      __builtin_amdgcn_s_setprio(3);
      LAS f32x4* xw = (LAS f32x4*)(sm + 65536 + j * 2048 + lane * 16);
#pragma unroll
      for (int u = 0; u < 16; ++u) {
        const f32x16& av = acc[u >> 3][(u >> 2) & 1];
        xw[(u >> 1) * 512 + (u & 1) * 64] = (f32x4){av[4 * (u & 3)], av[4 * (u & 3) + 1], av[4 * (u & 3) + 2], av[4 * (u & 3) + 3]};
      }
    }
    STAMP(5);
    BARRIER();
    STAMP_OUT();
    return;
  }

  STAMP(2);
  const int w = wave;
  const unsigned ka = lane * 16;
  const unsigned va = l31 >= 16 ? 2048u : 1024u + (unsigned)(hh * 16 + l31) * 16u, va1 = l31 >= 16 ? 2048u : va + 512u;
  const int IDX = 0xDDDD;
  const bf16x8 qf = lds_frag(sm, L_Q + w * 1024 + lane * 16);
  f32x16 negm, O;
  {
    f32x16 z;
#pragma unroll
    for (int r = 0; r < 16; ++r) { z[r] = 0.f; O[r] = 0.f; }
    f32x16 s0 = __builtin_amdgcn_mfma_f32_32x32x16_bf16(lds_frag(sm, ka), qf, z, 0, 0, 0);
    float m = s0[0];
#pragma unroll
    for (int r = 1; r < 16; ++r) m = fmaxf(m, s0[r]);
    const auto sw = __builtin_amdgcn_permlane32_swap(__float_as_uint(m), __float_as_uint(m), false, false);
    m = fmaxf(__uint_as_float(sw[0]), __uint_as_float(sw[1]));
#pragma unroll
    for (int r = 0; r < 16; ++r) negm[r] = -m;
  }
  STAMP(7);
  const int tm = hd >> 3, cp = hd & 7;
  const int tt = w & 1, kq = w >> 1;
  unsigned xwo = (unsigned)(tt * 16 + kq) * 2048u + lane * 16u, xro = (unsigned)(tt * 16 + kq * 4) * 2048u + lane * 16u;
  unsigned oo = (unsigned)(256 * bi + 64 * tm + 32 * tt + l31) * 1024u + (unsigned)(64 * cp + 16 * kq + 8 * hh) * 2u;
  unsigned wfo = (unsigned)kq * 16384u + lane * 16u, gao = L_G + (unsigned)(32 * tt + l31) * 4u;
  unsigned osb = WS_OF + (unsigned)((8 * bi + w) * 32 + hd) * 1024u, ob = WS_OF + (unsigned)((8 * bi + 2 * tm + tt) * 32 + 8 * kq) * 1024u;
  asm volatile("" : "+v"(xwo), "+v"(xro), "+v"(oo), "+v"(wfo), "+v"(gao), "+s"(osb), "+s"(ob));
  {
    u32x4 kf, v0, v1;
    unsigned kb = sb + ka, vb = sb + va, vb1 = sb + va1, fb = sb + L_FL + 4, ft;
    int cnt = 8, st, sp = 0x400000, sw = (w < 4) ? 2 : -1;
    if (w < 4) __builtin_amdgcn_s_setprio(2); else __builtin_amdgcn_s_setprio(1);
    asm volatile(ATTN_LOOP
                 : "+v"(O), "=&v"(kf), "=&v"(v0), "=&v"(v1), "+v"(kb), "+v"(vb), "+s"(cnt), "+v"(fb), "=&v"(ft), "=&s"(st), "+s"(sp), "+v"(vb1)
                 : "v"(qf), "v"(negm), "v"(IDX), "s"(sw)
                 : ATTN_CLOBS);
  }
  __builtin_amdgcn_s_setprio(3);
  STAMP(3);
  u32x4 wf[2][8];
  const unsigned rdy = __hip_atomic_load((LAS unsigned*)(sm + L_FL) + 24, __ATOMIC_RELAXED, __HIP_MEMORY_SCOPE_WORKGROUP);
  CFENCE();
  float gv = *(LAS float*)(sm + gao);
#pragma unroll
  for (int u = 0; u < 16; ++u) wf[u >> 3][u & 7] = *(LAS u32x4*)(sm + wfo + u * 1024);
  CFENCE();
  float lsum = O[8];
  {
    float chk = lsum * 0.f;
#pragma unroll
    for (int r = 0; r < 8; ++r) chk = __builtin_fmaf(O[r], 0.f, chk);
    if (__any(chk != chk)) {
      get_tag();
      glb_wait(rws, WS_FKV + (unsigned)(hd * 32 + l31) * 8u, tag);
      float m = -3.0e38f;
      f32x16 z;
#pragma unroll
      for (int r = 0; r < 16; ++r) { z[r] = 0.f; O[r] = 0.f; }
#pragma unroll 1
      for (int t = 0; t < 64; ++t) {
        const unsigned go = KVOFF(hd, t >> 1) + (t & 1) * 2048u, vg = 1024u + (unsigned)(hh * 16 + l15) * 16u;
        const u32x4 kg = LD128(lane * 16, go);
        u32x4 va0 = LD128(vg, go), va1 = LD128(vg + 512, go);
        if (l31 >= 16) { va0 = ones; va1 = ones; }
        f32x16 S = __builtin_amdgcn_mfma_f32_32x32x16_bf16(__builtin_bit_cast(bf16x8, kg), qf, z, 0, 0, 0);
        float mx = m;
#pragma unroll
        for (int r = 0; r < 16; ++r) mx = fmaxf(mx, S[r]);
        mx = fmaxf(mx, __shfl_xor(mx, 32));
        const float al = __builtin_amdgcn_exp2f(m - mx);
        m = mx;
        f32x8 p0, p1;
#pragma unroll
        for (int r = 0; r < 16; ++r) O[r] *= al;
#pragma unroll
        for (int r = 0; r < 8; ++r) { p0[r] = __builtin_amdgcn_exp2f(S[r] - m); p1[r] = __builtin_amdgcn_exp2f(S[8 + r] - m); }
        O = __builtin_amdgcn_smfmac_f32_32x32x32_bf16(__builtin_bit_cast(bf16x8, va0), __builtin_bit_cast(bf16x16, p0), O, IDX, 0, 0);
        O = __builtin_amdgcn_smfmac_f32_32x32x32_bf16(__builtin_bit_cast(bf16x8, va1), __builtin_bit_cast(bf16x16, p1), O, IDX, 0, 0);
      }
      lsum = O[8];
    }
  }
  {
    const float sc = __builtin_amdgcn_rcpf(lsum);
    u32x4 o;
#pragma unroll
    for (int e = 0; e < 4; ++e) o[e] = min(pk_bf16(O[2 * e] * sc, O[2 * e + 1] * sc), 0xfffffffeu);
    ST128(o, lane * 16u, osb);
  }
  {
    STAMP(4);
    LAS f32x4* xw = (LAS f32x4*)(sm + xwo);
    LAS f32x4* xr = (LAS f32x4*)(sm + xro);
    if ((unsigned)__builtin_amdgcn_readfirstlane(rdy) < 5u) {
      __builtin_amdgcn_s_setprio(0);
      lds_wait(sm, 24, 5u);
      __builtin_amdgcn_s_setprio(3);
      gv = *(LAS float*)(sm + gao);
#pragma unroll
      for (int u = 0; u < 16; ++u) wf[u >> 3][u & 7] = *(LAS u32x4*)(sm + wfo + u * 1024);
    }
    WAITL0();
    if (lane == 0) lds_add(sm, 21, 1u);
    __builtin_amdgcn_s_setprio(0); lds_wait(sm, 21, 8u); __builtin_amdgcn_s_setprio(3);
    WAITV(0);
    STAMP(14);
    f32x16 acc[2];
#pragma unroll 1
    for (unsigned s = 0; s < SPIN_CAP; ++s) {
      CFENCE();
      u32x4 of[8];
#pragma unroll
      for (int u = 0; u < 8; ++u) { of[u] = LD128(lane * 16 + (u & 3) * 1024, ob + (u >> 2) * 4096); __builtin_amdgcn_sched_barrier(0); }
      __builtin_amdgcn_sched_barrier(0);
#pragma unroll
      for (int r = 0; r < 16; ++r) { acc[0][r] = 0.f; acc[1][r] = 0.f; }
      unsigned mx = 0;
#pragma unroll
      for (int u = 0; u < 8; ++u) {
        acc[0] = __builtin_amdgcn_mfma_f32_32x32x16_bf16(__builtin_bit_cast(bf16x8, wf[0][u]), __builtin_bit_cast(bf16x8, of[u]), acc[0], 0, 0, 0);
        acc[1] = __builtin_amdgcn_mfma_f32_32x32x16_bf16(__builtin_bit_cast(bf16x8, wf[1][u]), __builtin_bit_cast(bf16x8, of[u]), acc[1], 0, 0, 0);
        mx = max(max(mx, of[u][0]), of[u][1]);
        mx = max(max(mx, of[u][2]), of[u][3]);
        asm volatile("" : "+v"(acc[0]), "+v"(acc[1]), "+v"(mx));
      }
      if (__all(mx != 0xffffffffu)) break;
    }
    STAMP(5);
#pragma unroll
    for (int p = 0; p < 4; ++p) {
      xw[p * 512] = (f32x4){acc[p >> 1][8 * (p & 1)], acc[p >> 1][8 * (p & 1) + 1], acc[p >> 1][8 * (p & 1) + 2], acc[p >> 1][8 * (p & 1) + 3]};
      xw[p * 512 + 64] = (f32x4){acc[p >> 1][8 * (p & 1) + 4], acc[p >> 1][8 * (p & 1) + 5], acc[p >> 1][8 * (p & 1) + 6], acc[p >> 1][8 * (p & 1) + 7]};
    }
    STAMP(15);
    BARRIER();
    STAMP(9);
    f32x4 pv[8], bv[8];
#pragma unroll
    for (int q = 0; q < 8; ++q) bv[q] = xr[4096 + q * 64];
#pragma unroll
    for (int q = 0; q < 8; ++q) pv[q] = xr[q * 64];
    const float om = 1.f - gv;
    const f32x4 a = ((bv[0] + bv[2]) + (bv[4] + bv[6])) * om + ((pv[0] + pv[2]) + (pv[4] + pv[6])) * gv;
    const f32x4 c = ((bv[1] + bv[3]) + (bv[5] + bv[7])) * om + ((pv[1] + pv[3]) + (pv[5] + pv[7])) * gv;
    const u32x4 o = {pk_bf16(a[0], a[1]), pk_bf16(a[2], a[3]), pk_bf16(c[0], c[1]), pk_bf16(c[2], c[3])};
    __builtin_amdgcn_raw_buffer_store_b128(o, mk_rsrc(out, 2048u * 1024u), oo, 0, 0);
  }
  STAMP(6);
  STAMP_OUT();
}

extern "C" void kernel_launch(void* const* d_in, const int* in_sizes, int n_in, void* d_out, int out_size,
                              void* d_ws, size_t ws_size, hipStream_t stream) {
  fused_kernel<<<256, 768, 0, stream>>>((const u16*)d_in[0], (const u16*)d_in[1], (const u16*)d_in[2], (const u16*)d_in[4],
                                        (const u16*)d_in[5], (const u16*)d_in[6], (const u16*)d_in[7], (u16*)d_out,
                                        (unsigned char*)d_ws);
}
```

```cpp
#include <hip/hip_runtime.h>
#include <hip/hip_bf16.h>

typedef unsigned short u16;
typedef unsigned long long u64;
typedef __bf16 bf16x8 __attribute__((ext_vector_type(8)));
typedef __bf16 bf16x16 __attribute__((ext_vector_type(16)));
typedef __bf16 bf16x2 __attribute__((ext_vector_type(2)));
typedef short s16x4 __attribute__((ext_vector_type(4)));
typedef float f32x2 __attribute__((ext_vector_type(2)));
typedef float f32x4 __attribute__((ext_vector_type(4)));
typedef float f32x8 __attribute__((ext_vector_type(8)));
typedef float f32x16 __attribute__((ext_vector_type(16)));
typedef unsigned u32x2 __attribute__((ext_vector_type(2)));
typedef unsigned u32x4 __attribute__((ext_vector_type(4)));
#define LAS __attribute__((address_space(3)))
typedef __amdgpu_buffer_rsrc_t rsrc_t;

constexpr unsigned WS_EPOCH = 0;
constexpr unsigned WS_FKV = 4096;
constexpr unsigned WS_FACT = WS_FKV + 8192;
constexpr unsigned WS_FCL = WS_FACT + 8192;
constexpr unsigned WS_G = 32768;
constexpr unsigned WS_KV = 65536;
constexpr unsigned WS_ACT = WS_KV + 32u * 131072u;
constexpr unsigned WS_WT = WS_ACT + 64u * 114688u;
constexpr unsigned WS_OF = WS_WT + 16u * 144u * 1024u;
constexpr unsigned WS_CK = WS_OF + 64u * 32u * 1024u;
constexpr unsigned WS_END = WS_CK + 32u * 32u * 512u;

constexpr unsigned STG = 47104;
constexpr unsigned TILE = 2048;
constexpr unsigned L_RT = 3 * STG;
constexpr unsigned L_Q = L_RT + 8192;
constexpr unsigned L_B1 = L_Q + 8192;
constexpr unsigned L_G = L_B1 + 1024;
constexpr unsigned L_FL = L_G + 1024;
constexpr unsigned L_END = L_FL + 512;

constexpr unsigned SPIN_CAP = 1u << 18;
constexpr int AUX_SC1 = 16;
constexpr int FAST_N = 2;

__device__ __forceinline__ unsigned pk_bf16(float a, float b) {
  f32x2 v = {a, b};
  return __builtin_bit_cast(unsigned, __builtin_convertvector(v, bf16x2));
}
__device__ __forceinline__ unsigned ck_mix(u32x4 a, u32x4 b, unsigned seed) {
  unsigned s = seed;
#pragma unroll
  for (int e = 0; e < 4; ++e) {
    s = __builtin_rotateleft32(s, 5) + a[e];
    s = __builtin_rotateleft32(s, 11) ^ b[e];
  }
  return s;
}
__device__ __forceinline__ float bf_lo(unsigned u) { return __uint_as_float(u << 16); }
__device__ __forceinline__ float bf_hi(unsigned u) { return __uint_as_float(u & 0xffff0000u); }
__device__ __forceinline__ rsrc_t mk_rsrc(const void* p, unsigned bytes) {
  return __builtin_amdgcn_make_buffer_rsrc((void*)p, 0, bytes, 0x00020000);
}
__device__ __forceinline__ u32x4 mk_desc(const void* p, unsigned bytes) {
  const u64 a = (u64)p;
  return (u32x4){(unsigned)a, (unsigned)(a >> 32) & 0xffffu, bytes, 0x00020000u};
}
__device__ __forceinline__ void dma16(u32x4 desc, unsigned lds_addr, unsigned voff, unsigned soff) {
  asm volatile("s_mov_b32 m0, %0\n\ts_nop 3\n\tbuffer_load_dwordx4 %1, %2, %3 offen lds"
               :: "s"(lds_addr), "v"(voff), "s"(desc), "s"(soff) : "memory");
}
__device__ __forceinline__ void dma16x2_sc1(u32x4 desc, unsigned lds_addr, unsigned voff, unsigned soff) {
  asm volatile("s_mov_b32 m0, %0\n\ts_nop 3\n\tbuffer_load_dwordx4 %1, %2, %3 offen sc1 lds\n\tbuffer_load_dwordx4 %1, %2, %3 offen offset:1024 sc1 lds"
               :: "s"(lds_addr), "v"(voff), "s"(desc), "s"(soff) : "memory");
}
template <int OFF>
__device__ __forceinline__ void dma16_next(u32x4 desc, unsigned voff, unsigned soff) {
  asm volatile("buffer_load_dwordx4 %0, %1, %2 offen offset:%3 lds" :: "v"(voff), "s"(desc), "s"(soff), "i"(OFF) : "memory");
}
#define BARRIER() asm volatile("s_waitcnt lgkmcnt(0)\n\ts_barrier" ::: "memory")
#define WAITV(n) asm volatile("s_waitcnt vmcnt(" #n ")" ::: "memory")
#define WAITL0() asm volatile("s_waitcnt lgkmcnt(0)" ::: "memory")
#define CFENCE() asm volatile("" ::: "memory")
#define LD128(off, soff) __builtin_amdgcn_raw_buffer_load_b128(rws, off, soff, AUX_SC1)
#define ST128(v, off, soff) __builtin_amdgcn_raw_buffer_store_b128(v, rws, off, soff, AUX_SC1)


__device__ __forceinline__ bf16x8 lds_frag(LAS char* sm, unsigned off) { return *(LAS bf16x8*)(sm + off); }
__device__ __forceinline__ bf16x8 lds_tr2(LAS char* sm, unsigned o0, unsigned o1) {
  s16x4 a = __builtin_amdgcn_ds_read_tr16_b64_v4i16((LAS s16x4*)(sm + o0));
  s16x4 b = __builtin_amdgcn_ds_read_tr16_b64_v4i16((LAS s16x4*)(sm + o1));
  return __builtin_bit_cast(bf16x8, __builtin_shufflevector(a, b, 0, 1, 2, 3, 4, 5, 6, 7));
}
__device__ __forceinline__ void lds_add(LAS char* sm, unsigned idx, unsigned v) {
  __hip_atomic_fetch_add((LAS unsigned*)(sm + L_FL) + idx, v, __ATOMIC_RELAXED, __HIP_MEMORY_SCOPE_WORKGROUP);
}
__device__ __forceinline__ void lds_wait(LAS char* sm, unsigned idx, unsigned want) {
#pragma unroll 1
  for (unsigned s = 0; s < SPIN_CAP * 16; ++s) {
    CFENCE();
    unsigned v = __hip_atomic_load((LAS unsigned*)(sm + L_FL) + idx, __ATOMIC_RELAXED, __HIP_MEMORY_SCOPE_WORKGROUP);
    if ((unsigned)__builtin_amdgcn_readfirstlane(v) >= want) break;
    __builtin_amdgcn_s_sleep(1);
  }
  CFENCE();
}
__device__ __forceinline__ void glb_wait(rsrc_t rs, unsigned off, u32x2 tag) {
#pragma unroll 1
  for (unsigned s = 0; s < SPIN_CAP; ++s) {
    CFENCE();
    u32x2 v = __builtin_amdgcn_raw_buffer_load_b64(rs, off, 0, AUX_SC1);
    if (__all(v.x == tag.x && v.y == tag.y)) break;
    __builtin_amdgcn_s_sleep(4);
  }
  CFENCE();
}

#define AE(b, i) "v_exp_f32 v[" #b "+" #i "], v[" #b "+" #i "]\n"
#define AV(r, off) "s_mov_b64 exec, %15\n ds_read_b128 " r ", %5 offset:" off "\n s_mov_b64 exec, -1\n"
#define ATILE(cur, oth, t)                                                                                          \
  AE(cur, 0) AE(cur, 1)                                                                                              \
  "s_waitcnt lgkmcnt(2)\n v_smfmac_f32_32x32x32_bf16 %0, %3, v[" #oth "+8:" #oth "+15], %13\n"                       \
  AV("%3", "2048*" #t "+512")                                                                                        \
  AE(cur, 2) AE(cur, 3) AE(cur, 4) AE(cur, 5)                                                                        \
  "s_waitcnt lgkmcnt(2)\n v_mfma_f32_32x32x16_bf16 v[" #oth ":" #oth "+15], %1, %11, %12\n"                          \
  "ds_read_b128 %1, %4 offset:2048*(" #t "+2)\n"                                                                     \
  AE(cur, 6) AE(cur, 7) AE(cur, 8) AE(cur, 9) AE(cur, 10)                                                            \
  "s_waitcnt lgkmcnt(2)\n v_smfmac_f32_32x32x32_bf16 %0, %2, v[" #cur ":" #cur "+7], %13\n"                          \
  AV("%2", "2048*(" #t "+1)")                                                                                        \
  AE(cur, 11) AE(cur, 12) AE(cur, 13) AE(cur, 14) AE(cur, 15)
#define ATTN_LOOP                                                                                                   \
  "v_mov_b32 v120, 0\n v_mov_b32 v121, 0\n v_mov_b32 v122, 0\n v_mov_b32 v123, 0\n"                                  \
  "v_mov_b32 v124, 0\n v_mov_b32 v125, 0\n v_mov_b32 v126, 0\n v_mov_b32 v127, 0\n"                                  \
  "v_mfma_f32_32x32x16_bf16 v[96:111], %1, %11, %12\n"                                                               \
  "ds_read_b128 %1, %4 offset:2048\n" AV("%2", "0") "s_nop 11\n"                                                     \
  "1:\n"                                                                                                             \
  ATILE(96, 112, 0) ATILE(112, 96, 1) ATILE(96, 112, 2) ATILE(112, 96, 3) ATILE(96, 112, 4)                          \
  "ds_read_b32 %8, %7\n"                                                                                             \
  ATILE(112, 96, 5)                                                                                                  \
  "v_readfirstlane_b32 %9, %8\n s_nop 3\n s_cmp_eq_u32 %9, 8\n s_cbranch_scc1 3f\n"                                  \
  "2:\n s_sleep 1\n ds_read_b32 %8, %7\n s_waitcnt lgkmcnt(0)\n v_readfirstlane_b32 %9, %8\n s_sub_u32 %10, %10, 1\n" \
  "s_cmp_le_i32 %10, 0\n s_cbranch_scc1 3f\n s_cmp_eq_u32 %9, 8\n s_cbranch_scc0 2b\n"                               \
  "3:\n"                                                                                                             \
  ATILE(96, 112, 6) ATILE(112, 96, 7)                                                                                \
  "v_add_u32 %4, 8*2048, %4\n v_add_u32 %5, 8*2048, %5\n v_add_u32 %7, 4, %7\n s_sub_u32 %6, %6, 1\n"                \
  "s_cmp_lg_u32 %6, 4\n s_cbranch_scc1 4f\n v_mov_b32 %8, 1\n s_mov_b64 exec, 1\n ds_add_u32 %7, %8 offset:80\n s_mov_b64 exec, -1\n 4:\n" \
  "s_cmp_lg_u32 %6, %14\n s_cbranch_scc1 5f\n s_setprio 0\n 5:\n"                                                    \
  "s_cmp_lg_u32 %6, 0\n s_cbranch_scc1 1b\n"                                                                         \
  "s_waitcnt lgkmcnt(0)\n v_smfmac_f32_32x32x32_bf16 %0, %3, v[120:127], %13\n s_nop 15\n"
#define ATTN_CLOBS "v96","v97","v98","v99","v100","v101","v102","v103","v104","v105","v106","v107","v108","v109","v110","v111", \
  "v112","v113","v114","v115","v116","v117","v118","v119","v120","v121","v122","v123","v124","v125","v126","v127","scc","memory"
#define KVOFF(h, g) (WS_KV + (unsigned)((h) * 32 + (g)) * 4096u)

__global__ void __launch_bounds__(768, 3)
fused_kernel(const u16* __restrict__ x, const u16* __restrict__ wqkv, const u16* __restrict__ wo,
             const u16* __restrict__ w1, const u16* __restrict__ b1, const u16* __restrict__ w2,
             const u16* __restrict__ gw, u16* __restrict__ out, unsigned char* __restrict__ ws) {
  __shared__ __attribute__((aligned(16))) char smem_[L_END];
  LAS char* sm = (LAS char*)smem_;
  const unsigned sb = (unsigned)(size_t)sm;
  asm volatile("" :: "s"(x), "s"(wqkv), "s"(wo), "s"(w1), "s"(b1), "s"(w2), "s"(gw), "s"(out), "s"(ws));
  const int lane = threadIdx.x & 63;
  const int wave = __builtin_amdgcn_readfirstlane(threadIdx.x >> 6);
  const int b = blockIdx.x, xcd = b & 7, idx = b >> 3;
  const int bi = 4 * (xcd & 1) + (idx & 3), hd = 8 * (xcd >> 1) + (idx >> 2);
  const bool has_w1 = (hd & 7) != 7;
  const int cw = hd - (hd >> 3);
  const int rot = ((hd + bi) & 1) * 4;
  const int hh = lane >> 5, l31 = lane & 31, l15 = lane & 15, g4 = lane >> 4;
  const int q4 = l15 >> 2, p4 = lane & 3, r8 = lane >> 3, sl8 = lane & 7;
  if (wave < 8) {
    const u32x4 dx = mk_desc(x, 2048u * 1024u);
    const unsigned xo0 = (unsigned)(256 * bi + 32 * wave + r8) * 1024u + (unsigned)(sl8 ^ (r8 >> 1)) * 16u, xo1 = (xo0 ^ 64u) + 7168u;
    dma16(dx, sb + wave * 4096u, xo0, rot * 128u);
    dma16_next<1024>(dx, xo1, rot * 128u);
    dma16_next<2048>(dx, xo0, rot * 128u + 14336u);
    dma16_next<3072>(dx, xo1, rot * 128u + 14336u);
  }
  __builtin_amdgcn_sched_barrier(0);
  const rsrc_t rws = mk_rsrc(ws, WS_END);

  auto mk_tag = [&](u32x2 e) {
    u64 z = ((u64)(unsigned)__builtin_amdgcn_readfirstlane(e.y) << 32 | (u64)(unsigned)__builtin_amdgcn_readfirstlane(e.x)) + 0x9E3779B97F4A7C15ull;
    z = (z ^ (z >> 30)) * 0xBF58476D1CE4E5B9ull;
    z = (z ^ (z >> 27)) * 0x94D049BB133111EBull;
    z ^= z >> 31;
    return (u32x2){(unsigned)z, (unsigned)(z >> 32)};
  };
  u32x2 tag;
  auto get_tag = [&]() {
    const u32x2 t = *(LAS u32x2*)(sm + L_FL + 256);
    tag = (u32x2){(unsigned)__builtin_amdgcn_readfirstlane(t.x), (unsigned)__builtin_amdgcn_readfirstlane(t.y)};
  };
  const unsigned xa0 = (unsigned)(64 * (wave & 3) + l15) * 128u + (unsigned)(g4 ^ (l15 >> 1)) * 16u;
  const unsigned xa1 = xa0 ^ 64u;
#define NEXT_BUF(v) v = (v == 2 * STG) ? 0 : v + STG
  const unsigned qo = (unsigned)(16 * (wave & 3) + (((lane >> 1) & 15) ^ ((g4 & 1) << 2))) * 3072u + (unsigned)hh * 1024u + (unsigned)hd * 32u + (lane & 1) * 16u;
  auto qkv_issue = [&](int n, unsigned buf) {
    const u32x4 d = mk_desc(wqkv, 512u * 3072u);
    const unsigned so = ((unsigned)(n + rot) & 7u) * (64u * 3072u);
    dma16(d, sb + buf + 32768u + (wave & 3) * 3584u, qo, so);
    if (lane < 32) dma16_next<1024>(d, qo, so + 1024u);
  };

  if (__builtin_expect(wave >= 4 && wave < 8, 1)) {
    const int lw = wave - 4;
    const u32x4 dx = mk_desc(x, 2048u * 1024u);
    const unsigned xo0 = (unsigned)(256 * bi + 64 * lw + r8) * 1024u + (unsigned)(sl8 ^ (r8 >> 1)) * 16u;
    const unsigned xo1 = (xo0 ^ 64u) + 8192u - 1024u;
    const u32x4 d1 = mk_desc(w1, 512u * 3584u);
    const unsigned wo0 = (unsigned)(16 * lw + r8) * 3584u + (unsigned)cw * 128u + (unsigned)(sl8 ^ (r8 & 2)) * 16u;
    const unsigned wo1 = (wo0 ^ 64u) + 8u * 3584u - 1024u;
    unsigned buf = 0;
#pragma unroll 1
    for (int n = 0; n < 8; ++n) {
      const unsigned ks = (unsigned)(n + rot) & 7u, d = sb + buf + lw * 8192u;
      if (n < 2) qkv_issue(n, buf);
      if (n)
#pragma unroll
      for (int u = 0; u < 2; ++u) {
        dma16(dx, d + u * 4096u, xo0, ks * 128u + u * 32768u);
        dma16_next<1024>(dx, xo1, ks * 128u + u * 32768u);
        dma16_next<2048>(dx, xo0, ks * 128u + u * 32768u + 16384u - 2048u);
        dma16_next<3072>(dx, xo1, ks * 128u + u * 32768u + 16384u - 2048u);
      }
      if (has_w1) {
        dma16(d1, sb + buf + 32768u + 1536u + lw * 3584u, wo0, ks * (64u * 3584u));
        dma16_next<1024>(d1, wo1, ks * (64u * 3584u));
      }
      if (n == 0 && lw == 0) {
        if (has_w1) { if (lane < 8) dma16(mk_desc(b1, 3584u), sb + L_B1, lane * 16u + (unsigned)cw * 128u, 0u); }
        else dma16(mk_desc(gw, 1024u), sb + L_G, lane * 16u, 0u);
      }
      if (n == 5) {
        if (has_w1) {
          const u32x4 d2 = mk_desc(w2, 1792u * 1024u);
          const unsigned o2 = (unsigned)(16 * (r8 >> 2) + (r8 & 3)) * 1024u + (unsigned)bi * 128u + (unsigned)sl8 * 16u;
          const unsigned so = (unsigned)(64 * cw + 32 * (lw >> 1) + 8 * (lw & 1)) * 1024u;
          dma16(d2, sb + L_RT + lw * 2048u, o2, so); dma16_next<1024>(d2, o2, so + 3072u);
        } else {
          const u32x4 dO = mk_desc(wo, 512u * 1024u);
          const unsigned oo = (unsigned)r8 * 1024u + (unsigned)bi * 128u + (unsigned)sl8 * 16u;
          const unsigned so = (unsigned)((hd >> 3) * 8 + 2 * lw) * 16384u;
          dma16(dO, sb + L_RT + lw * 2048u, oo, so); dma16_next<1024>(dO, oo, so + 7168u);
          dma16(dO, sb + 2 * STG + 32768u + 1536u + lw * 3584u, oo, so + 16384u); dma16_next<1024>(dO, oo, so + 16384u + 7168u);
        }
      }
      NEXT_BUF(buf);
      if (n >= 1) {
        if (n == 5) WAITV(12); else if (n == 1) { if (has_w1) WAITV(12); else WAITV(10); } else if (has_w1) WAITV(10); else WAITV(8);
        BARRIER();
      }
    }
    WAITV(0);
    BARRIER();
    BARRIER();
  } else if (wave < 4) {
    const int j = wave;
    __builtin_amdgcn_s_setprio(3);
    if (wave == 0) ((LAS unsigned*)(sm + L_FL))[lane] = 0u;
    f32x4 acc[4][3];
#pragma unroll
    for (int t = 0; t < 4; ++t)
#pragma unroll
      for (int c = 0; c < 3; ++c) acc[t][c] = (f32x4){0.f, 0.f, 0.f, 0.f};
    const unsigned wa0 = 32768u + (unsigned)(g4 >> 1) * 3584u + (unsigned)(12 * (g4 & 1) + q4) * 32u + p4 * 8u;
    const unsigned wa1 = wa0 ^ 128u;
    bf16x8 xA[4], wA[3], xB[4], wB[3];
#pragma unroll
    for (int t = 0; t < 4; ++t) xB[t] = __builtin_bit_cast(bf16x8, (u32x4){0u, 0u, 0u, 0u});
#pragma unroll
    for (int c = 0; c < 3; ++c) wB[c] = __builtin_bit_cast(bf16x8, (u32x4){0u, 0u, 0u, 0u});
#define QKV_RD(X, W, kk)                                                                                           \
    _Pragma("unroll") for (int c = 0; c < 3; ++c) W[c] = lds_tr2(sm, buf + wa0 + kk * 7168 + c * 512, buf + wa1 + kk * 7168 + c * 512); \
    _Pragma("unroll") for (int t = 0; t < 4; ++t) X[t] = lds_frag(sm, buf + (kk ? xa1 : xa0) + t * 2048);
#define QKV_MM(X, W)                                                                                               \
    _Pragma("unroll") for (int t = 0; t < 4; ++t) {                                                                \
      acc[t][0] = __builtin_amdgcn_mfma_f32_16x16x32_bf16(W[0], X[t], acc[t][0], 0, 0, 0);                         \
      acc[t][1] = __builtin_amdgcn_mfma_f32_16x16x32_bf16(W[1], X[t], acc[t][1], 0, 0, 0);                         \
      acc[t][2] = __builtin_amdgcn_mfma_f32_16x16x32_bf16(X[t], W[2], acc[t][2], 0, 0, 0);                         \
    }                                                                                                              \
    _Pragma("unroll") for (int u = 0; u < 10; ++u) { __builtin_amdgcn_sched_group_barrier(0x008, 1, 0); __builtin_amdgcn_sched_group_barrier(0x100, 1, 0); } \
    __builtin_amdgcn_sched_group_barrier(0x008, 2, 0);                                                             \
    asm volatile("" : "+v"(acc[0][0]), "+v"(acc[0][1]), "+v"(acc[0][2]), "+v"(acc[1][0]), "+v"(acc[1][1]), "+v"(acc[1][2]), \
                      "+v"(acc[2][0]), "+v"(acc[2][1]), "+v"(acc[2][2]), "+v"(acc[3][0]), "+v"(acc[3][1]), "+v"(acc[3][2]) :: "memory"); \
    __builtin_amdgcn_sched_barrier(0);
    unsigned vo = 1024u + (unsigned)(g4 & 1) * 512u + (unsigned)((g4 >> 1) * 16 + (l15 & 3) + 8 * ((l15 >> 2) & 1) + 4 * (l15 >> 3)) * 16u;
    unsigned eq = L_Q + 2 * j * 1024 + lane * 16, ek = 2 * j * TILE + lane * 16, ev = 2 * j * TILE + vo;
    unsigned go = KVOFF(hd, bi * 4 + j);
    asm volatile("" : "+v"(vo), "+v"(eq), "+v"(ek), "+v"(ev), "+s"(go));
    unsigned buf = 0, nbuf = 2 * STG;
    WAITV(0);
#pragma unroll 1
    for (int n = 0; n < 8; ++n) {
      BARRIER();
      QKV_RD(xA, wA, 0)
      QKV_MM(xB, wB)
      if (n < 6) qkv_issue(n + 2, nbuf);
      QKV_RD(xB, wB, 1)
      QKV_MM(xA, wA)
      NEXT_BUF(buf); NEXT_BUF(nbuf);
      if (n < 6) WAITV(2); else WAITV(0);
    }
    QKV_MM(xB, wB)
    u32x4 pub[3][2];
#pragma unroll
    for (int c = 2; c >= 0; --c)
#pragma unroll
      for (int tp = 0; tp < 2; ++tp) {
        const float sc = c ? 1.f : 0.25f * 1.4426950408889634f;
        const f32x4 a = acc[2 * tp][c] * sc, e = acc[2 * tp + 1][c] * sc;
        const auto r0 = __builtin_amdgcn_permlane16_swap(pk_bf16(a[0], a[1]), pk_bf16(e[0], e[1]), false, false);
        const auto r1 = __builtin_amdgcn_permlane16_swap(pk_bf16(a[2], a[3]), pk_bf16(e[2], e[3]), false, false);
        pub[c][tp] = (u32x4){r0[0], r1[0], r0[1], r1[1]};
        if (c) ST128(pub[c][tp], (c == 1 ? lane * 16u : vo) + tp * 2048, go);
        *(LAS u32x4*)(sm + (c == 0 ? eq + tp * 1024 : (c == 1 ? ek : ev) + tp * TILE)) = pub[c][tp];
      }
    asm volatile("" :: "v"(pub[1][0]), "v"(pub[1][1]), "v"(pub[2][0]), "v"(pub[2][1]));
    BARRIER();
    get_tag();
    {
      const unsigned co = WS_CK + (unsigned)(hd * 32 + bi * 4 + j) * 512u;
      __builtin_amdgcn_raw_buffer_store_b32(ck_mix(pub[1][0], pub[1][1], tag.x), rws, lane * 8u, co, AUX_SC1);
      __builtin_amdgcn_raw_buffer_store_b32(ck_mix(pub[2][0], pub[2][1], tag.y), rws, ((vo - 1024u) >> 1) + 4u, co, AUX_SC1);
    }
    WAITV(0);
    if (lane == 0) __builtin_amdgcn_raw_buffer_store_b64(tag, rws, WS_FKV + (unsigned)((hd * 8 + bi) * 4 + j) * 8u, 0, AUX_SC1);
  } else {
    const int j = wave - 8;
    u32x2 ep = {0u, 0u};
    if (j == 0) ep = __builtin_amdgcn_raw_buffer_load_b64(rws, WS_EPOCH, 0, AUX_SC1);
    auto put_tag = [&]() { if (j == 0) *(LAS u32x2*)(sm + L_FL + 256) = mk_tag(ep); };
    auto retile = [&](unsigned img, int ks) {
      const unsigned ta = img + (unsigned)(8 * hh + q4) * 128u + (unsigned)(4 * (g4 & 1) + 2 * (p4 & 1) + (p4 >> 1)) * 8u;
      ST128(__builtin_bit_cast(u32x4, lds_tr2(sm, ta, ta + 512)), WS_WT + lane * 16, (2 * bi * 144 + ks) * 1024);
      ST128(__builtin_bit_cast(u32x4, lds_tr2(sm, ta + 64, ta + 576)), WS_WT + lane * 16, ((2 * bi + 1) * 144 + ks) * 1024);
    };
    auto clear_o = [&]() {
      const u32x4 inv = {0xffffffffu, 0xffffffffu, 0xffffffffu, 0xffffffffu};
      const unsigned oo = WS_OF + (unsigned)((8 * bi + 2 * j) * 32 + hd) * 1024u + lane * 16u;
      ST128(inv, oo, 0); ST128(inv, oo + 32768, 0);
    };
    const unsigned fkv = WS_FKV + (unsigned)((hd * 8 + ((bi + lane) & 7)) * 4 + j) * 8u;
    u32x4 fd[FAST_N][4];
    u32x2 fc[FAST_N];
    auto fast_load = [&](int u) __attribute__((always_inline)) {
      const unsigned g = (unsigned)(((bi + 1 + u) & 7) * 4 + j);
      fc[u] = __builtin_amdgcn_raw_buffer_load_b64(rws, lane * 8u, WS_CK + (unsigned)(hd * 32 + g) * 512u, AUX_SC1);
#pragma unroll
      for (int e = 0; e < 4; ++e) fd[u][e] = LD128(lane * 16 + e * 1024, KVOFF(hd, g));
    };
    auto fast_take = [&]() __attribute__((always_inline)) {
#pragma unroll
      for (int u = 0; u < FAST_N; ++u) {
#pragma unroll 1
        for (unsigned s = 0; s < SPIN_CAP; ++s) {
          if (__all(ck_mix(fd[u][0], fd[u][2], tag.x) == fc[u].x && ck_mix(fd[u][1], fd[u][3], tag.y) == fc[u].y)) break;
          CFENCE();
#pragma unroll
          for (int v = u; v < FAST_N; ++v) fast_load(v);
        }
        LAS char* dst = sm + ((1 + u) * 8 + 2 * j) * TILE + lane * 16;
#pragma unroll
        for (int e = 0; e < 4; ++e) *(LAS u32x4*)(dst + (e >> 1) * TILE + (e & 1) * 1024) = fd[u][e];
        WAITL0();
        if (lane == 0) lds_add(sm, 1 + u, 2u);
      }
    };
    auto gather = [&](auto pre) __attribute__((always_inline)) {
      u32x2 fv = __builtin_amdgcn_raw_buffer_load_b64(rws, fkv, 0, AUX_SC1);
      fast_take();
      const u32x4 dws = mk_desc(ws, WS_END);
      unsigned spins = 0;
      bool done = false;
#pragma unroll 1
      for (int n0 = 1 + FAST_N; n0 < 8;) {
        const unsigned rdy = ((unsigned)__builtin_amdgcn_ballot_w64(fv.x == tag.x && fv.y == tag.y) & 0xffu) >> n0;
        int cnt = __builtin_ctz(~rdy);
        if (++spins > SPIN_CAP) cnt = 8 - n0;
        CFENCE();
#pragma unroll 1
        for (int u = n0; u < n0 + cnt; ++u) {
          const unsigned go = KVOFF(hd, ((bi + u) & 7) * 4 + j), d = sb + (unsigned)(u * 8 + 2 * j) * TILE;
          const unsigned gu = __builtin_amdgcn_readfirstlane(go), du = __builtin_amdgcn_readfirstlane(d);
          dma16x2_sc1(dws, du, lane * 16u, gu);
          dma16x2_sc1(dws, du + TILE, lane * 16u, gu + 2048u);
        }
        if (n0 + cnt < 8) {
          if (!cnt) __builtin_amdgcn_s_sleep(4);
          fv = __builtin_amdgcn_raw_buffer_load_b64(rws, fkv, 0, AUX_SC1);
        }
        WAITV(0);
        if (lane < cnt) lds_add(sm, n0 + lane, 2u);
        n0 += cnt;
        if (!done && n0 > 4) {
          pre();
          WAITV(0);
          if (lane == 0) __builtin_amdgcn_raw_buffer_store_b64(tag, rws, WS_FACT + (unsigned)((bi * 4 + j) * 32 + hd) * 8u, 0, AUX_SC1);
          done = true;
        }
      }
    };
    if (has_w1) {
      f32x4 acc[4][4];
      const unsigned wa = 32768u + 1536u + (unsigned)(g4 >> 1) * 3584u + (unsigned)(8 * (g4 & 1) + q4) * 128u +
                          (unsigned)((q4 >> 1) | ((g4 & 1) << 1)) * 32u + p4 * 8u;
      bf16x8 xA[4], wA[4], xB[4], wB[4];
#pragma unroll
      for (int t = 0; t < 4; ++t) { xB[t] = __builtin_bit_cast(bf16x8, (u32x4){0u, 0u, 0u, 0u}); wB[t] = xB[t]; }
#define W1_RD(X, W, kk)                                                                                            \
      _Pragma("unroll") for (int c = 0; c < 4; ++c) W[c] = lds_tr2(sm, buf + (wa ^ (unsigned)(c * 32)) + kk * 7168, buf + (wa ^ (unsigned)(c * 32)) + kk * 7168 + 512); \
      _Pragma("unroll") for (int t = 0; t < 4; ++t) X[t] = lds_frag(sm, buf + (kk ? xa1 : xa0) + t * 2048);
#define W1_MM(X, W)                                                                                                \
      _Pragma("unroll") for (int t = 0; t < 4; ++t)                                                                \
        _Pragma("unroll") for (int c = 0; c < 4; ++c) acc[t][c] = __builtin_amdgcn_mfma_f32_16x16x32_bf16(W[c], X[t], acc[t][c], 0, 0, 0); \
      _Pragma("unroll") for (int u = 0; u < 12; ++u) { __builtin_amdgcn_sched_group_barrier(0x008, 1, 0); __builtin_amdgcn_sched_group_barrier(0x100, 1, 0); } \
      __builtin_amdgcn_sched_group_barrier(0x008, 4, 0);                                                           \
      asm volatile("" : "+v"(acc[0][0]), "+v"(acc[0][1]), "+v"(acc[0][2]), "+v"(acc[0][3]), "+v"(acc[1][0]), "+v"(acc[1][1]), "+v"(acc[1][2]), "+v"(acc[1][3]), \
                        "+v"(acc[2][0]), "+v"(acc[2][1]), "+v"(acc[2][2]), "+v"(acc[2][3]), "+v"(acc[3][0]), "+v"(acc[3][1]), "+v"(acc[3][2]), "+v"(acc[3][3]) :: "memory"); \
      __builtin_amdgcn_sched_barrier(0);
      unsigned buf = 0;
      BARRIER();
#pragma unroll
      for (int c = 0; c < 4; ++c) {
        const u32x2 bv = *(LAS u32x2*)(sm + L_B1 + (16 * c + 4 * g4) * 2);
#pragma unroll
        for (int t = 0; t < 4; ++t) acc[t][c] = (f32x4){bf_lo(bv.x), bf_hi(bv.x), bf_lo(bv.y), bf_hi(bv.y)};
      }
#pragma unroll 1
      for (int n = 0; n < 8; ++n) {
        W1_RD(xA, wA, 0)
        W1_MM(xB, wB)
        W1_RD(xB, wB, 1)
        W1_MM(xA, wA)
        NEXT_BUF(buf);
        if (n == 2) put_tag();
        if (n < 7) BARRIER();
      }
      BARRIER();
      __builtin_amdgcn_s_setprio(3);
      get_tag();
      W1_MM(xB, wB)
      u32x4 actv[4][2];
      auto mish1 = [&](int t) __attribute__((always_inline)) {
#pragma unroll
          for (int pc = 0; pc < 2; ++pc) {
            u32x4 o;
#pragma unroll
            for (int e = 0; e < 4; ++e) {
              f32x2 hv = {acc[t][2 * pc + (e >> 1)][2 * (e & 1)], acc[t][2 * pc + (e >> 1)][2 * (e & 1) + 1]};
              f32x2 a = hv * 1.4426950408889634f;
              f32x2 ex = {__builtin_amdgcn_exp2f(a.x), __builtin_amdgcn_exp2f(a.y)};
              f32x2 d = ex * (ex + 2.f) + 2.f;
              f32x2 r = {__builtin_amdgcn_rcpf(d.x), __builtin_amdgcn_rcpf(d.y)};
              f32x2 y = hv * (r * -2.f + 1.f);
              o[e] = pk_bf16(y.x, y.y);
            }
            asm volatile("" : "+v"(o));
            actv[t][pc] = o;
          }
      };
      mish1(0); mish1(1);
      mish1(2);
#pragma unroll
      for (int u = 0; u < FAST_N; ++u) fast_load(u);
      mish1(3);
      gather([&]() __attribute__((always_inline)) {
        const unsigned ao = WS_ACT + (unsigned)(8 * bi + 2 * j) * 114688u + (unsigned)cw * 4096u + (unsigned)g4 * 512u + l15 * 16u;
#pragma unroll
        for (int t = 0; t < 4; ++t)
#pragma unroll
          for (int pc = 0; pc < 2; ++pc) { ST128(actv[t][pc], ao + (t >> 1) * 114688 + pc * 2048 + (t & 1) * 256, 0); }
        retile(L_RT + j * 2048u, 4 * cw + j);
      });
    } else {
      f32x4 acc[4];
#pragma unroll
      for (int t = 0; t < 4; ++t) acc[t] = (f32x4){0.f, 0.f, 0.f, 0.f};
      unsigned buf = 0;
#pragma unroll 1
      for (int n = 0; n < 8; ++n) {
        BARRIER();
        if (hd == 7) {
#pragma unroll
          for (int kk = 0; kk < 2; ++kk) {
            const bf16x8 gf = lds_frag(sm, L_G + ((n + rot) & 7) * 128 + kk * 64 + g4 * 16);
#pragma unroll
            for (int t = 0; t < 4; ++t) acc[t] = __builtin_amdgcn_mfma_f32_16x16x32_bf16(gf, lds_frag(sm, buf + (kk ? xa1 : xa0) + t * 2048), acc[t], 0, 0, 0);
          }
        }
        if (n == 2) put_tag();
        if (n == 5 || n == 6) retile(n == 5 ? L_RT + j * 2048u : 2 * STG + 32768u + 1536u + j * 3584u, 112 + (hd >> 3) * 8 + 2 * j + (n - 5));
        NEXT_BUF(buf);
      }
      WAITV(0);
      get_tag();
      BARRIER();
      __builtin_amdgcn_s_setprio(3);
      if (hd == 7) {
        const float lg = g4 == 0 ? acc[0][0] : g4 == 1 ? acc[1][0] : g4 == 2 ? acc[2][0] : acc[3][0];
        const float gv = __builtin_amdgcn_rcpf(1.f + __builtin_amdgcn_exp2f(lg * -1.4426950408889634f));
        __builtin_amdgcn_raw_buffer_store_b32(__float_as_uint(gv), rws, WS_G + (unsigned)(256 * bi + 64 * j + lane) * 4u, 0, AUX_SC1);
      }
      __builtin_amdgcn_s_sleep(16);
#pragma unroll
      for (int u = 0; u < FAST_N; ++u) fast_load(u);
      gather([&]() {});
    }
    clear_o();
    WAITV(0);
    if (lane == 0) { __builtin_amdgcn_raw_buffer_store_b64(tag, rws, WS_FCL + (unsigned)((bi * 4 + j) * 32 + hd) * 8u, 0, AUX_SC1); lds_add(sm, 8, 2u); }
    const int tm = hd >> 3, cp = hd & 7;
    if (j == 0) {
      const unsigned o1 = WS_FACT + (unsigned)((4 * bi + tm) * 32 + l31) * 8u;
#pragma unroll 1
      for (unsigned s = 0; s < SPIN_CAP; ++s) {
        CFENCE();
        const u32x2 v = __builtin_amdgcn_raw_buffer_load_b64(rws, o1, 0, AUX_SC1);
        const u32x4 y = LD128(WS_FACT + (unsigned)cp * 1024u + lane * 16u, 0);
        if (__all(v.x == tag.x && v.y == tag.y && y[0] == tag.x && y[1] == tag.y && y[2] == tag.x && y[3] == tag.y)) break;
        __builtin_amdgcn_s_sleep(4);
      }
      CFENCE();
      if (lane == 0) lds_add(sm, 18, 1u);
    } else {
      if (b == 0 && j == 2) {
#pragma unroll 1
        for (int u = 0; u < 4; ++u) glb_wait(rws, WS_FKV + (unsigned)(64 * u + lane) * 32u, tag);
        if (lane == 0) __builtin_amdgcn_raw_buffer_store_b64(tag, rws, WS_EPOCH, 0, AUX_SC1);
      }
      lds_wait(sm, 18, 1u);
    }
    {
      f32x16 acc[2][2];
#pragma unroll
      for (int t = 0; t < 2; ++t)
#pragma unroll
        for (int c = 0; c < 2; ++c)
#pragma unroll
          for (int r = 0; r < 16; ++r) acc[t][c][r] = 0.f;
      const unsigned ab = WS_ACT + (unsigned)(8 * bi + 2 * tm) * 114688u + (unsigned)(28 * j) * 1024u;
      const unsigned ab1 = WS_ACT + (unsigned)(8 * bi + 2 * tm + 1) * 114688u + (unsigned)(28 * j) * 1024u;
      const unsigned wb = WS_WT + (unsigned)((2 * cp) * 144 + 28 * j) * 1024u;
      const unsigned wb1 = WS_WT + (unsigned)((2 * cp + 1) * 144 + 28 * j) * 1024u;
      u32x4 af[4][2], wf[4][2];
#pragma unroll
      for (int u = 0; u < 4; ++u) {
        af[u][0] = LD128(lane * 16 + u * 1024, ab); af[u][1] = LD128(lane * 16 + u * 1024, ab1);
        wf[u][0] = LD128(lane * 16 + u * 1024, wb); wf[u][1] = LD128(lane * 16 + u * 1024, wb1);
      }
      __builtin_amdgcn_s_setprio(1);
#pragma unroll 1
      for (int it = 1; it <= 7; ++it) {
        const unsigned nx = (it < 7) ? it * 4096u : 0u;
        if (it == 7) {
          lds_wait(sm, 25, 8u);
          const u32x4 dws = mk_desc(ws, WS_END);
#pragma unroll 1
          for (int u = 0; u < 8; ++u)
            dma16x2_sc1(dws, sb + (unsigned)(j * 16 + 2 * u) * 1024u, lane * 16u, WS_WT + (unsigned)((2 * cp + (u >> 2)) * 144 + 112 + 8 * j + 2 * (u & 3)) * 1024u);
        }
        if (it == 4 && j == 1) {
          glb_wait(rws, WS_FCL + (unsigned)((4 * bi + tm) * 32 + l31) * 8u, tag);
          *(LAS unsigned*)(sm + L_G + lane * 4) = __builtin_amdgcn_raw_buffer_load_b32(rws, WS_G + (unsigned)(256 * bi + 64 * tm + lane) * 4u, 0, AUX_SC1);
          WAITL0();
          if (lane == 0) lds_add(sm, 24, 1u);
        }
        const rsrc_t rws = mk_rsrc(ws, it < 7 ? WS_END : 0u);
#pragma unroll
        for (int u = 0; u < 4; ++u) {
#pragma unroll
          for (int t = 0; t < 2; ++t)
#pragma unroll
            for (int c = 0; c < 2; ++c)
              acc[t][c] = __builtin_amdgcn_mfma_f32_32x32x16_bf16(__builtin_bit_cast(bf16x8, wf[u][c]), __builtin_bit_cast(bf16x8, af[u][t]), acc[t][c], 0, 0, 0);
          af[u][0] = LD128(lane * 16 + u * 1024, ab + nx); af[u][1] = LD128(lane * 16 + u * 1024, ab1 + nx);
          wf[u][0] = LD128(lane * 16 + u * 1024, wb + nx); wf[u][1] = LD128(lane * 16 + u * 1024, wb1 + nx);
          __builtin_amdgcn_sched_barrier(0);
        }
      }
      WAITV(0);
      if (lane == 0) lds_add(sm, 24, 1u);
      __builtin_amdgcn_s_setprio(0);
      lds_wait(sm, 21, 8u);
      __builtin_amdgcn_s_setprio(3);
      LAS f32x4* xw = (LAS f32x4*)(sm + 65536 + j * 2048 + lane * 16);
#pragma unroll
      for (int u = 0; u < 16; ++u) {
        const f32x16& av = acc[u >> 3][(u >> 2) & 1];
        xw[(u >> 1) * 512 + (u & 1) * 64] = (f32x4){av[4 * (u & 3)], av[4 * (u & 3) + 1], av[4 * (u & 3) + 2], av[4 * (u & 3) + 3]};
      }
    }
    BARRIER();
    return;
  }

  const int w = wave;
  const unsigned ka = lane * 16;
  const unsigned va = 1024u + (unsigned)(hh * 16 + l15) * 16u;
  const unsigned vc = l31 == 16 ? 0x3f803f80u : 0u;
  const int IDX = 0xDDDD;
  const bf16x8 qf = lds_frag(sm, L_Q + w * 1024 + lane * 16);
  u32x4 kf = *(LAS u32x4*)(sm + ka);
  f32x16 negm, O;
  {
    f32x16 z;
#pragma unroll
    for (int r = 0; r < 16; ++r) { z[r] = 0.f; O[r] = 0.f; }
    f32x16 s0 = __builtin_amdgcn_mfma_f32_32x32x16_bf16(__builtin_bit_cast(bf16x8, kf), qf, z, 0, 0, 0);
    float m = s0[0];
#pragma unroll
    for (int r = 1; r < 16; ++r) m = fmaxf(m, s0[r]);
    const auto sw = __builtin_amdgcn_permlane32_swap(__float_as_uint(m), __float_as_uint(m), false, false);
    m = fmaxf(__uint_as_float(sw[0]), __uint_as_float(sw[1]));
#pragma unroll
    for (int r = 0; r < 16; ++r) negm[r] = -m;
  }
  const int tm = hd >> 3, cp = hd & 7;
  const int tt = w & 1, kq = w >> 1;
  unsigned xwo = (unsigned)(tt * 16 + kq) * 2048u + lane * 16u, xro = (unsigned)(tt * 16 + kq * 4) * 2048u + lane * 16u;
  unsigned oo = (unsigned)(256 * bi + 64 * tm + 32 * tt + l31) * 1024u + (unsigned)(64 * cp + 16 * kq + 8 * hh) * 2u;
  unsigned wfo = (unsigned)kq * 16384u + lane * 16u, gao = L_G + (unsigned)(32 * tt + l31) * 4u;
  unsigned osb = WS_OF + (unsigned)((8 * bi + w) * 32 + hd) * 1024u, ob = WS_OF + (unsigned)((8 * bi + 2 * tm + tt) * 32 + 8 * kq) * 1024u;
  asm volatile("" : "+v"(xwo), "+v"(xro), "+v"(oo), "+v"(wfo), "+v"(gao), "+s"(osb), "+s"(ob));
  {
    u32x4 v0 = {vc, vc, vc, vc}, v1 = v0;
    unsigned kb = sb + ka, vb = sb + va, fb = sb + L_FL + 4, ft;
    int cnt = 8, st, sp = 0x400000, sw = (w < 4) ? 2 : -1;
    if (w < 4) __builtin_amdgcn_s_setprio(2); else __builtin_amdgcn_s_setprio(1);
    asm volatile(ATTN_LOOP
                 : "+v"(O), "+v"(kf), "+v"(v0), "+v"(v1), "+v"(kb), "+v"(vb), "+s"(cnt), "+v"(fb), "=&v"(ft), "=&s"(st), "+s"(sp)
                 : "v"(qf), "v"(negm), "v"(IDX), "s"(sw), "s"(0x0000ffff0000ffffull)
                 : ATTN_CLOBS);
  }
  __builtin_amdgcn_s_setprio(3);
  u32x4 wf[2][8];
  const unsigned rdy = __hip_atomic_load((LAS unsigned*)(sm + L_FL) + 24, __ATOMIC_RELAXED, __HIP_MEMORY_SCOPE_WORKGROUP);
  CFENCE();
  float gv = *(LAS float*)(sm + gao);
#pragma unroll
  for (int u = 0; u < 16; ++u) wf[u >> 3][u & 7] = *(LAS u32x4*)(sm + wfo + u * 1024);
  CFENCE();
  float lsum = __uint_as_float(__builtin_amdgcn_permlane32_swap(__float_as_uint(O[8]), __float_as_uint(O[8]), false, false)[0]);
  {
    float chk = lsum;
#pragma unroll
    for (int r = 0; r < 8; ++r) chk = __builtin_fmaf(O[r], 0.f, chk);
    if (__any(!(chk > 1.0e-6f && chk < 1.0e26f))) {
      get_tag();
      glb_wait(rws, WS_FKV + (unsigned)(hd * 32 + l31) * 8u, tag);
      float m = -3.0e38f;
      f32x16 z;
#pragma unroll
      for (int r = 0; r < 16; ++r) { z[r] = 0.f; O[r] = 0.f; }
#pragma unroll 1
      for (int t = 0; t < 64; ++t) {
        const unsigned go = KVOFF(hd, t >> 1) + (t & 1) * 2048u;
        const u32x4 kg = LD128(lane * 16, go);
        u32x4 va0 = LD128(va, go), va1 = LD128(va + 512, go);
        if (l31 >= 16) { va0 = (u32x4){vc, vc, vc, vc}; va1 = va0; }
        f32x16 S = __builtin_amdgcn_mfma_f32_32x32x16_bf16(__builtin_bit_cast(bf16x8, kg), qf, z, 0, 0, 0);
        float mx = m;
#pragma unroll
        for (int r = 0; r < 16; ++r) mx = fmaxf(mx, S[r]);
        mx = fmaxf(mx, __shfl_xor(mx, 32));
        const float al = __builtin_amdgcn_exp2f(m - mx);
        m = mx;
        f32x8 p0, p1;
#pragma unroll
        for (int r = 0; r < 16; ++r) O[r] *= al;
#pragma unroll
        for (int r = 0; r < 8; ++r) { p0[r] = __builtin_amdgcn_exp2f(S[r] - m); p1[r] = __builtin_amdgcn_exp2f(S[8 + r] - m); }
        O = __builtin_amdgcn_smfmac_f32_32x32x32_bf16(__builtin_bit_cast(bf16x8, va0), __builtin_bit_cast(bf16x16, p0), O, IDX, 0, 0);
        O = __builtin_amdgcn_smfmac_f32_32x32x32_bf16(__builtin_bit_cast(bf16x8, va1), __builtin_bit_cast(bf16x16, p1), O, IDX, 0, 0);
      }
      lsum = __uint_as_float(__builtin_amdgcn_permlane32_swap(__float_as_uint(O[8]), __float_as_uint(O[8]), false, false)[0]);
    }
  }
  {
    const float sc = __builtin_amdgcn_rcpf(lsum);
    u32x4 o;
#pragma unroll
    for (int e = 0; e < 4; ++e) o[e] = min(pk_bf16(O[2 * e] * sc, O[2 * e + 1] * sc), 0xfffffffeu);
    ST128(o, lane * 16u, osb);
  }
  {
    LAS f32x4* xw = (LAS f32x4*)(sm + xwo);
    LAS f32x4* xr = (LAS f32x4*)(sm + xro);
    if ((unsigned)__builtin_amdgcn_readfirstlane(rdy) < 5u) {
      __builtin_amdgcn_s_setprio(0);
      lds_wait(sm, 24, 5u);
      __builtin_amdgcn_s_setprio(3);
      gv = *(LAS float*)(sm + gao);
#pragma unroll
      for (int u = 0; u < 16; ++u) wf[u >> 3][u & 7] = *(LAS u32x4*)(sm + wfo + u * 1024);
    }
    WAITL0();
    if (lane == 0) lds_add(sm, 21, 1u);
    __builtin_amdgcn_s_setprio(0); lds_wait(sm, 21, 8u); __builtin_amdgcn_s_setprio(3);
    WAITV(0);
    f32x16 acc[2];
#pragma unroll 1
    for (unsigned s = 0; s < SPIN_CAP; ++s) {
      CFENCE();
      u32x4 of[8];
#pragma unroll
      for (int u = 0; u < 8; ++u) { of[u] = LD128(lane * 16 + (u & 3) * 1024, ob + (u >> 2) * 4096); __builtin_amdgcn_sched_barrier(0); }
      __builtin_amdgcn_sched_barrier(0);
#pragma unroll
      for (int r = 0; r < 16; ++r) { acc[0][r] = 0.f; acc[1][r] = 0.f; }
      unsigned mx = 0;
#pragma unroll
      for (int u = 0; u < 8; ++u) {
        acc[0] = __builtin_amdgcn_mfma_f32_32x32x16_bf16(__builtin_bit_cast(bf16x8, wf[0][u]), __builtin_bit_cast(bf16x8, of[u]), acc[0], 0, 0, 0);
        acc[1] = __builtin_amdgcn_mfma_f32_32x32x16_bf16(__builtin_bit_cast(bf16x8, wf[1][u]), __builtin_bit_cast(bf16x8, of[u]), acc[1], 0, 0, 0);
        mx = max(max(mx, of[u][0]), of[u][1]);
        mx = max(max(mx, of[u][2]), of[u][3]);
        asm volatile("" : "+v"(acc[0]), "+v"(acc[1]), "+v"(mx));
      }
      if (__all(mx != 0xffffffffu)) break;
    }
#pragma unroll
    for (int p = 0; p < 4; ++p) {
      xw[p * 512] = (f32x4){acc[p >> 1][8 * (p & 1)], acc[p >> 1][8 * (p & 1) + 1], acc[p >> 1][8 * (p & 1) + 2], acc[p >> 1][8 * (p & 1) + 3]};
      xw[p * 512 + 64] = (f32x4){acc[p >> 1][8 * (p & 1) + 4], acc[p >> 1][8 * (p & 1) + 5], acc[p >> 1][8 * (p & 1) + 6], acc[p >> 1][8 * (p & 1) + 7]};
    }
    BARRIER();
    f32x4 pv[8], bv[8];
#pragma unroll
    for (int q = 0; q < 8; ++q) bv[q] = xr[4096 + q * 64];
#pragma unroll
    for (int q = 0; q < 8; ++q) pv[q] = xr[q * 64];
    const float om = 1.f - gv;
    const f32x4 a = ((bv[0] + bv[2]) + (bv[4] + bv[6])) * om + ((pv[0] + pv[2]) + (pv[4] + pv[6])) * gv;
    const f32x4 c = ((bv[1] + bv[3]) + (bv[5] + bv[7])) * om + ((pv[1] + pv[3]) + (pv[5] + pv[7])) * gv;
    const u32x4 o = {pk_bf16(a[0], a[1]), pk_bf16(a[2], a[3]), pk_bf16(c[0], c[1]), pk_bf16(c[2], c[3])};
    __builtin_amdgcn_raw_buffer_store_b128(o, mk_rsrc(out, 2048u * 1024u), oo, 0, 0);
  }
}

extern "C" void kernel_launch(void* const* d_in, const int* in_sizes, int n_in, void* d_out, int out_size,
                              void* d_ws, size_t ws_size, hipStream_t stream) {
  fused_kernel<<<256, 768, 0, stream>>>((const u16*)d_in[0], (const u16*)d_in[1], (const u16*)d_in[2], (const u16*)d_in[4],
                                        (const u16*)d_in[5], (const u16*)d_in[6], (const u16*)d_in[7], (u16*)d_out,
                                        (unsigned char*)d_ws);
}
```

```cpp
#include <hip/hip_runtime.h>
#include <hip/hip_bf16.h>

typedef unsigned short u16;
typedef unsigned long long u64;
typedef __bf16 bf16x8 __attribute__((ext_vector_type(8)));
typedef __bf16 bf16x16 __attribute__((ext_vector_type(16)));
typedef __bf16 bf16x2 __attribute__((ext_vector_type(2)));
typedef short s16x4 __attribute__((ext_vector_type(4)));
typedef float f32x2 __attribute__((ext_vector_type(2)));
typedef float f32x4 __attribute__((ext_vector_type(4)));
typedef float f32x8 __attribute__((ext_vector_type(8)));
typedef float f32x16 __attribute__((ext_vector_type(16)));
typedef unsigned u32x2 __attribute__((ext_vector_type(2)));
typedef unsigned u32x4 __attribute__((ext_vector_type(4)));
#define LAS __attribute__((address_space(3)))
typedef __amdgpu_buffer_rsrc_t rsrc_t;

constexpr unsigned WS_EPOCH = 0;
constexpr unsigned WS_FKV = 4096;
constexpr unsigned WS_FACT = WS_FKV + 8192;
constexpr unsigned WS_FCL = WS_FACT + 8192;
constexpr unsigned WS_G = 32768;
constexpr unsigned WS_KV = 65536;
constexpr unsigned WS_ACT = WS_KV + 32u * 131072u;
constexpr unsigned WS_WT = WS_ACT + 64u * 114688u;
constexpr unsigned WS_OF = WS_WT + 16u * 144u * 1024u;
constexpr unsigned WS_CK = WS_OF + 64u * 32u * 1024u;
constexpr unsigned WS_END = WS_CK + 32u * 32u * 512u;

constexpr unsigned STG = 47104;
constexpr unsigned TILE = 2048;
constexpr unsigned L_RT = 3 * STG;
constexpr unsigned L_Q = L_RT + 8192;
constexpr unsigned L_B1 = L_Q + 8192;
constexpr unsigned L_G = L_B1 + 1024;
constexpr unsigned L_FL = L_G + 1024;
constexpr unsigned L_END = L_FL + 512;

constexpr unsigned SPIN_CAP = 1u << 18;
constexpr int AUX_SC1 = 16;
constexpr int FAST_N = 2;

__device__ __forceinline__ unsigned pk_bf16(float a, float b) {
  f32x2 v = {a, b};
  return __builtin_bit_cast(unsigned, __builtin_convertvector(v, bf16x2));
}
__device__ __forceinline__ unsigned ck_mix(u32x4 a, u32x4 b, unsigned seed) {
  unsigned s = seed;
#pragma unroll
  for (int e = 0; e < 4; ++e) {
    s = __builtin_rotateleft32(s, 5) + a[e];
    s = __builtin_rotateleft32(s, 11) ^ b[e];
  }
  return s;
}
__device__ __forceinline__ float bf_lo(unsigned u) { return __uint_as_float(u << 16); }
__device__ __forceinline__ float bf_hi(unsigned u) { return __uint_as_float(u & 0xffff0000u); }
__device__ __forceinline__ rsrc_t mk_rsrc(const void* p, unsigned bytes) {
  return __builtin_amdgcn_make_buffer_rsrc((void*)p, 0, bytes, 0x00020000);
}
__device__ __forceinline__ u32x4 mk_desc(const void* p, unsigned bytes) {
  const u64 a = (u64)p;
  return (u32x4){(unsigned)a, (unsigned)(a >> 32) & 0xffffu, bytes, 0x00020000u};
}
__device__ __forceinline__ void dma16(u32x4 desc, unsigned lds_addr, unsigned voff, unsigned soff) {
  asm volatile("s_mov_b32 m0, %0\n\ts_nop 3\n\tbuffer_load_dwordx4 %1, %2, %3 offen lds"
               :: "s"(lds_addr), "v"(voff), "s"(desc), "s"(soff) : "memory");
}
__device__ __forceinline__ void dma16x2_sc1(u32x4 desc, unsigned lds_addr, unsigned voff, unsigned soff) {
  asm volatile("s_mov_b32 m0, %0\n\ts_nop 3\n\tbuffer_load_dwordx4 %1, %2, %3 offen sc1 lds\n\tbuffer_load_dwordx4 %1, %2, %3 offen offset:1024 sc1 lds"
               :: "s"(lds_addr), "v"(voff), "s"(desc), "s"(soff) : "memory");
}
template <int OFF>
__device__ __forceinline__ void dma16_next(u32x4 desc, unsigned voff, unsigned soff) {
  asm volatile("buffer_load_dwordx4 %0, %1, %2 offen offset:%3 lds" :: "v"(voff), "s"(desc), "s"(soff), "i"(OFF) : "memory");
}
#define BARRIER() asm volatile("s_waitcnt lgkmcnt(0)\n\ts_barrier" ::: "memory")
#define WAITV(n) asm volatile("s_waitcnt vmcnt(" #n ")" ::: "memory")
#define WAITL0() asm volatile("s_waitcnt lgkmcnt(0)" ::: "memory")
#define CFENCE() asm volatile("" ::: "memory")
#define LD128(off, soff) __builtin_amdgcn_raw_buffer_load_b128(rws, off, soff, AUX_SC1)
#define ST128(v, off, soff) __builtin_amdgcn_raw_buffer_store_b128(v, rws, off, soff, AUX_SC1)


__device__ __forceinline__ bf16x8 lds_frag(LAS char* sm, unsigned off) { return *(LAS bf16x8*)(sm + off); }
__device__ __forceinline__ bf16x8 lds_tr2(LAS char* sm, unsigned o0, unsigned o1) {
  s16x4 a = __builtin_amdgcn_ds_read_tr16_b64_v4i16((LAS s16x4*)(sm + o0));
  s16x4 b = __builtin_amdgcn_ds_read_tr16_b64_v4i16((LAS s16x4*)(sm + o1));
  return __builtin_bit_cast(bf16x8, __builtin_shufflevector(a, b, 0, 1, 2, 3, 4, 5, 6, 7));
}
__device__ __forceinline__ void lds_add(LAS char* sm, unsigned idx, unsigned v) {
  __hip_atomic_fetch_add((LAS unsigned*)(sm + L_FL) + idx, v, __ATOMIC_RELAXED, __HIP_MEMORY_SCOPE_WORKGROUP);
}
__device__ __forceinline__ void lds_wait(LAS char* sm, unsigned idx, unsigned want) {
#pragma unroll 1
  for (unsigned s = 0; s < SPIN_CAP * 16; ++s) {
    CFENCE();
    unsigned v = __hip_atomic_load((LAS unsigned*)(sm + L_FL) + idx, __ATOMIC_RELAXED, __HIP_MEMORY_SCOPE_WORKGROUP);
    if ((unsigned)__builtin_amdgcn_readfirstlane(v) >= want) break;
    __builtin_amdgcn_s_sleep(1);
  }
  CFENCE();
}
__device__ __forceinline__ void glb_wait(rsrc_t rs, unsigned off, u32x2 tag) {
#pragma unroll 1
  for (unsigned s = 0; s < SPIN_CAP; ++s) {
    CFENCE();
    u32x2 v = __builtin_amdgcn_raw_buffer_load_b64(rs, off, 0, AUX_SC1);
    if (__all(v.x == tag.x && v.y == tag.y)) break;
    __builtin_amdgcn_s_sleep(4);
  }
  CFENCE();
}

#define AE(b, i) "v_exp_f32 v[" #b "+" #i "], v[" #b "+" #i "]\n"
#define AV(r, off) "s_mov_b64 exec, %15\n ds_read_b128 " r ", %5 offset:" off "\n s_mov_b64 exec, -1\n"
#define ATILE(cur, oth, t)                                                                                          \
  AE(cur, 0) AE(cur, 1)                                                                                              \
  "s_waitcnt lgkmcnt(2)\n v_smfmac_f32_32x32x32_bf16 %0, %3, v[" #oth "+8:" #oth "+15], %13\n"                       \
  AV("%3", "2048*" #t "+512")                                                                                        \
  AE(cur, 2) AE(cur, 3) AE(cur, 4) AE(cur, 5)                                                                        \
  "s_waitcnt lgkmcnt(2)\n v_mfma_f32_32x32x16_bf16 v[" #oth ":" #oth "+15], %1, %11, %12\n"                          \
  "ds_read_b128 %1, %4 offset:2048*(" #t "+2)\n"                                                                     \
  AE(cur, 6) AE(cur, 7) AE(cur, 8) AE(cur, 9) AE(cur, 10)                                                            \
  "s_waitcnt lgkmcnt(2)\n v_smfmac_f32_32x32x32_bf16 %0, %2, v[" #cur ":" #cur "+7], %13\n"                          \
  AV("%2", "2048*(" #t "+1)")                                                                                        \
  AE(cur, 11) AE(cur, 12) AE(cur, 13) AE(cur, 14) AE(cur, 15)
#define ATTN_LOOP                                                                                                   \
  "v_mov_b32 v120, 0\n v_mov_b32 v121, 0\n v_mov_b32 v122, 0\n v_mov_b32 v123, 0\n"                                  \
  "v_mov_b32 v124, 0\n v_mov_b32 v125, 0\n v_mov_b32 v126, 0\n v_mov_b32 v127, 0\n"                                  \
  "v_mfma_f32_32x32x16_bf16 v[96:111], %1, %11, %12\n"                                                               \
  "ds_read_b128 %1, %4 offset:2048\n" AV("%2", "0") "s_nop 11\n"                                                     \
  "1:\n"                                                                                                             \
  ATILE(96, 112, 0) ATILE(112, 96, 1) ATILE(96, 112, 2) ATILE(112, 96, 3) ATILE(96, 112, 4)                          \
  "ds_read_b32 %8, %7\n"                                                                                             \
  ATILE(112, 96, 5)                                                                                                  \
  "v_readfirstlane_b32 %9, %8\n s_nop 3\n s_cmp_eq_u32 %9, 8\n s_cbranch_scc1 3f\n"                                  \
  "2:\n s_sleep 1\n ds_read_b32 %8, %7\n s_waitcnt lgkmcnt(0)\n v_readfirstlane_b32 %9, %8\n s_sub_u32 %10, %10, 1\n" \
  "s_cmp_le_i32 %10, 0\n s_cbranch_scc1 3f\n s_cmp_eq_u32 %9, 8\n s_cbranch_scc0 2b\n"                               \
  "3:\n"                                                                                                             \
  ATILE(96, 112, 6) ATILE(112, 96, 7)                                                                                \
  "v_add_u32 %4, 8*2048, %4\n v_add_u32 %5, 8*2048, %5\n v_add_u32 %7, 4, %7\n s_sub_u32 %6, %6, 1\n"                \
  "s_cmp_lg_u32 %6, 4\n s_cbranch_scc1 4f\n v_mov_b32 %8, 1\n s_mov_b64 exec, 1\n ds_add_u32 %7, %8 offset:80\n s_mov_b64 exec, -1\n 4:\n" \
  "s_cmp_lg_u32 %6, %14\n s_cbranch_scc1 5f\n s_setprio 0\n 5:\n"                                                    \
  "s_cmp_lg_u32 %6, 0\n s_cbranch_scc1 1b\n"                                                                         \
  "s_waitcnt lgkmcnt(0)\n v_smfmac_f32_32x32x32_bf16 %0, %3, v[120:127], %13\n s_nop 15\n"
#define ATTN_CLOBS "v96","v97","v98","v99","v100","v101","v102","v103","v104","v105","v106","v107","v108","v109","v110","v111", \
  "v112","v113","v114","v115","v116","v117","v118","v119","v120","v121","v122","v123","v124","v125","v126","v127","scc","memory"
#define KEYED(off) ((off) ^ ((tag.x & 63u) << 4))
#define KVOFF(h, g) (WS_KV + (unsigned)((h) * 32 + (g)) * 4096u)

__global__ void __launch_bounds__(768, 3)
fused_kernel(const u16* __restrict__ x, const u16* __restrict__ wqkv, const u16* __restrict__ wo,
             const u16* __restrict__ w1, const u16* __restrict__ b1, const u16* __restrict__ w2,
             const u16* __restrict__ gw, u16* __restrict__ out, unsigned char* __restrict__ ws) {
  __shared__ __attribute__((aligned(16))) char smem_[L_END];
  LAS char* sm = (LAS char*)smem_;
  const unsigned sb = (unsigned)(size_t)sm;
  asm volatile("" :: "s"(x), "s"(wqkv), "s"(wo), "s"(w1), "s"(b1), "s"(w2), "s"(gw), "s"(out), "s"(ws));
  const int lane = threadIdx.x & 63;
  const int wave = __builtin_amdgcn_readfirstlane(threadIdx.x >> 6);
  const int b = blockIdx.x, xcd = b & 7, idx = b >> 3;
  const int bi = 4 * (xcd & 1) + (idx & 3), hd = 8 * (xcd >> 1) + (idx >> 2);
  const bool has_w1 = (hd & 7) != 7;
  const int cw = hd - (hd >> 3);
  const int rot = ((hd + bi) & 1) * 4;
  const int hh = lane >> 5, l31 = lane & 31, l15 = lane & 15, g4 = lane >> 4;
  const int q4 = l15 >> 2, p4 = lane & 3, r8 = lane >> 3, sl8 = lane & 7;
  if (wave < 8) {
    const u32x4 dx = mk_desc(x, 2048u * 1024u);
    const unsigned xo0 = (unsigned)(256 * bi + 32 * wave + r8) * 1024u + (unsigned)(sl8 ^ (r8 >> 1)) * 16u, xo1 = (xo0 ^ 64u) + 7168u;
    dma16(dx, sb + wave * 4096u, xo0, rot * 128u);
    dma16_next<1024>(dx, xo1, rot * 128u);
    dma16_next<2048>(dx, xo0, rot * 128u + 14336u);
    dma16_next<3072>(dx, xo1, rot * 128u + 14336u);
  }
  __builtin_amdgcn_sched_barrier(0);
  const rsrc_t rws = mk_rsrc(ws, WS_END);

  auto mk_tag = [&](u32x2 e) {
    u64 z = ((u64)(unsigned)__builtin_amdgcn_readfirstlane(e.y) << 32 | (u64)(unsigned)__builtin_amdgcn_readfirstlane(e.x)) + 0x9E3779B97F4A7C15ull;
    z = (z ^ (z >> 30)) * 0xBF58476D1CE4E5B9ull;
    z = (z ^ (z >> 27)) * 0x94D049BB133111EBull;
    z ^= z >> 31;
    const unsigned key = ((unsigned)__builtin_amdgcn_readfirstlane(e.x) + 1u + (((unsigned)z >> 8) & 31u)) & 63u;
    return (u32x2){((unsigned)z & ~63u) | key, (unsigned)(z >> 32)};
  };
  u32x2 tag;
  auto get_tag = [&]() {
    const u32x2 t = *(LAS u32x2*)(sm + L_FL + 256);
    tag = (u32x2){(unsigned)__builtin_amdgcn_readfirstlane(t.x), (unsigned)__builtin_amdgcn_readfirstlane(t.y)};
  };
  const unsigned xa0 = (unsigned)(64 * (wave & 3) + l15) * 128u + (unsigned)(g4 ^ (l15 >> 1)) * 16u;
  const unsigned xa1 = xa0 ^ 64u;
#define NEXT_BUF(v) v = (v == 2 * STG) ? 0 : v + STG
  const unsigned qo = (unsigned)(16 * (wave & 3) + (((lane >> 1) & 15) ^ ((g4 & 1) << 2))) * 3072u + (unsigned)hh * 1024u + (unsigned)hd * 32u + (lane & 1) * 16u;
  auto qkv_issue = [&](int n, unsigned buf) {
    const u32x4 d = mk_desc(wqkv, 512u * 3072u);
    const unsigned so = ((unsigned)(n + rot) & 7u) * (64u * 3072u);
    dma16(d, sb + buf + 32768u + (wave & 3) * 3584u, qo, so);
    if (lane < 32) dma16_next<1024>(d, qo, so + 1024u);
  };

  if (__builtin_expect(wave >= 4 && wave < 8, 1)) {
    const int lw = wave - 4;
    const u32x4 dx = mk_desc(x, 2048u * 1024u);
    const unsigned xo0 = (unsigned)(256 * bi + 64 * lw + r8) * 1024u + (unsigned)(sl8 ^ (r8 >> 1)) * 16u;
    const unsigned xo1 = (xo0 ^ 64u) + 8192u - 1024u;
    const u32x4 d1 = mk_desc(w1, 512u * 3584u);
    const unsigned wo0 = (unsigned)(16 * lw + r8) * 3584u + (unsigned)cw * 128u + (unsigned)(sl8 ^ (r8 & 2)) * 16u;
    const unsigned wo1 = (wo0 ^ 64u) + 8u * 3584u - 1024u;
    unsigned buf = 0;
#pragma unroll 1
    for (int n = 0; n < 8; ++n) {
      const unsigned ks = (unsigned)(n + rot) & 7u, d = sb + buf + lw * 8192u;
      if (n < 2) qkv_issue(n, buf);
      if (n)
#pragma unroll
      for (int u = 0; u < 2; ++u) {
        dma16(dx, d + u * 4096u, xo0, ks * 128u + u * 32768u);
        dma16_next<1024>(dx, xo1, ks * 128u + u * 32768u);
        dma16_next<2048>(dx, xo0, ks * 128u + u * 32768u + 16384u - 2048u);
        dma16_next<3072>(dx, xo1, ks * 128u + u * 32768u + 16384u - 2048u);
      }
      if (has_w1) {
        dma16(d1, sb + buf + 32768u + 1536u + lw * 3584u, wo0, ks * (64u * 3584u));
        dma16_next<1024>(d1, wo1, ks * (64u * 3584u));
      }
      if (n == 0 && lw == 0) {
        if (has_w1) { if (lane < 8) dma16(mk_desc(b1, 3584u), sb + L_B1, lane * 16u + (unsigned)cw * 128u, 0u); }
        else dma16(mk_desc(gw, 1024u), sb + L_G, lane * 16u, 0u);
      }
      if (n == 5) {
        if (has_w1) {
          const u32x4 d2 = mk_desc(w2, 1792u * 1024u);
          const unsigned o2 = (unsigned)(16 * (r8 >> 2) + (r8 & 3)) * 1024u + (unsigned)bi * 128u + (unsigned)sl8 * 16u;
          const unsigned so = (unsigned)(64 * cw + 32 * (lw >> 1) + 8 * (lw & 1)) * 1024u;
          dma16(d2, sb + L_RT + lw * 2048u, o2, so); dma16_next<1024>(d2, o2, so + 3072u);
        } else {
          const u32x4 dO = mk_desc(wo, 512u * 1024u);
          const unsigned oo = (unsigned)r8 * 1024u + (unsigned)bi * 128u + (unsigned)sl8 * 16u;
          const unsigned so = (unsigned)((hd >> 3) * 8 + 2 * lw) * 16384u;
          dma16(dO, sb + L_RT + lw * 2048u, oo, so); dma16_next<1024>(dO, oo, so + 7168u);
          dma16(dO, sb + 2 * STG + 32768u + 1536u + lw * 3584u, oo, so + 16384u); dma16_next<1024>(dO, oo, so + 16384u + 7168u);
        }
      }
      NEXT_BUF(buf);
      if (n >= 1) {
        if (n == 5) WAITV(12); else if (n == 1) { if (has_w1) WAITV(12); else WAITV(10); } else if (has_w1) WAITV(10); else WAITV(8);
        BARRIER();
      }
    }
    WAITV(0);
    BARRIER();
    BARRIER();
  } else if (wave < 4) {
    const int j = wave;
    __builtin_amdgcn_s_setprio(3);
    if (wave == 0) ((LAS unsigned*)(sm + L_FL))[lane] = 0u;
    f32x4 acc[4][3];
#pragma unroll
    for (int t = 0; t < 4; ++t)
#pragma unroll
      for (int c = 0; c < 3; ++c) acc[t][c] = (f32x4){0.f, 0.f, 0.f, 0.f};
    const unsigned wa0 = 32768u + (unsigned)(g4 >> 1) * 3584u + (unsigned)(12 * (g4 & 1) + q4) * 32u + p4 * 8u;
    const unsigned wa1 = wa0 ^ 128u;
    bf16x8 xA[4], wA[3], xB[4], wB[3];
#pragma unroll
    for (int t = 0; t < 4; ++t) xB[t] = __builtin_bit_cast(bf16x8, (u32x4){0u, 0u, 0u, 0u});
#pragma unroll
    for (int c = 0; c < 3; ++c) wB[c] = __builtin_bit_cast(bf16x8, (u32x4){0u, 0u, 0u, 0u});
#define QKV_RD(X, W, kk)                                                                                           \
    _Pragma("unroll") for (int c = 0; c < 3; ++c) W[c] = lds_tr2(sm, buf + wa0 + kk * 7168 + c * 512, buf + wa1 + kk * 7168 + c * 512); \
    _Pragma("unroll") for (int t = 0; t < 4; ++t) X[t] = lds_frag(sm, buf + (kk ? xa1 : xa0) + t * 2048);
#define QKV_MM(X, W)                                                                                               \
    _Pragma("unroll") for (int t = 0; t < 4; ++t) {                                                                \
      acc[t][0] = __builtin_amdgcn_mfma_f32_16x16x32_bf16(W[0], X[t], acc[t][0], 0, 0, 0);                         \
      acc[t][1] = __builtin_amdgcn_mfma_f32_16x16x32_bf16(W[1], X[t], acc[t][1], 0, 0, 0);                         \
      acc[t][2] = __builtin_amdgcn_mfma_f32_16x16x32_bf16(X[t], W[2], acc[t][2], 0, 0, 0);                         \
    }                                                                                                              \
    _Pragma("unroll") for (int u = 0; u < 10; ++u) { __builtin_amdgcn_sched_group_barrier(0x008, 1, 0); __builtin_amdgcn_sched_group_barrier(0x100, 1, 0); } \
    __builtin_amdgcn_sched_group_barrier(0x008, 2, 0);                                                             \
    asm volatile("" : "+v"(acc[0][0]), "+v"(acc[0][1]), "+v"(acc[0][2]), "+v"(acc[1][0]), "+v"(acc[1][1]), "+v"(acc[1][2]), \
                      "+v"(acc[2][0]), "+v"(acc[2][1]), "+v"(acc[2][2]), "+v"(acc[3][0]), "+v"(acc[3][1]), "+v"(acc[3][2]) :: "memory"); \
    __builtin_amdgcn_sched_barrier(0);
    unsigned vo = 1024u + (unsigned)(g4 & 1) * 512u + (unsigned)((g4 >> 1) * 16 + (l15 & 3) + 8 * ((l15 >> 2) & 1) + 4 * (l15 >> 3)) * 16u;
    unsigned eq = L_Q + 2 * j * 1024 + lane * 16, ek = 2 * j * TILE + lane * 16, ev = 2 * j * TILE + vo;
    unsigned go = KVOFF(hd, bi * 4 + j);
    asm volatile("" : "+v"(vo), "+v"(eq), "+v"(ek), "+v"(ev), "+s"(go));
    unsigned buf = 0, nbuf = 2 * STG, gk = 0, gv = 0;
    tag = (u32x2){0u, 0u};
    WAITV(0);
#pragma unroll 1
    for (int n = 0; n < 8; ++n) {
      BARRIER();
      QKV_RD(xA, wA, 0)
      QKV_MM(xB, wB)
      if (n < 6) qkv_issue(n + 2, nbuf);
      QKV_RD(xB, wB, 1)
      QKV_MM(xA, wA)
      NEXT_BUF(buf); NEXT_BUF(nbuf);
      if (n < 6) WAITV(2); else WAITV(0);
      if (n == 5) { get_tag(); gk = KEYED(lane * 16u); gv = KEYED(vo); }
    }
    QKV_MM(xB, wB)
    u32x4 pub[3][2];
#pragma unroll
    for (int c = 2; c >= 0; --c)
#pragma unroll
      for (int tp = 0; tp < 2; ++tp) {
        const float sc = c ? 1.f : 0.25f * 1.4426950408889634f;
        const f32x4 a = acc[2 * tp][c] * sc, e = acc[2 * tp + 1][c] * sc;
        const auto r0 = __builtin_amdgcn_permlane16_swap(pk_bf16(a[0], a[1]), pk_bf16(e[0], e[1]), false, false);
        const auto r1 = __builtin_amdgcn_permlane16_swap(pk_bf16(a[2], a[3]), pk_bf16(e[2], e[3]), false, false);
        pub[c][tp] = (u32x4){r0[0], r1[0], r0[1], r1[1]};
        if (c) ST128(pub[c][tp], (c == 1 ? gk : gv) + tp * 2048, go);
        *(LAS u32x4*)(sm + (c == 0 ? eq + tp * 1024 : (c == 1 ? ek : ev) + tp * TILE)) = pub[c][tp];
      }
    asm volatile("" :: "v"(pub[1][0]), "v"(pub[1][1]), "v"(pub[2][0]), "v"(pub[2][1]));
    BARRIER();
    {
      const unsigned co = WS_CK + (unsigned)(hd * 32 + bi * 4 + j) * 512u;
      __builtin_amdgcn_raw_buffer_store_b32(ck_mix(pub[1][0], pub[1][1], tag.x), rws, lane * 8u, co, AUX_SC1);
      __builtin_amdgcn_raw_buffer_store_b32(ck_mix(pub[2][0], pub[2][1], tag.y), rws, ((vo - 1024u) >> 1) + 4u, co, AUX_SC1);
    }
    WAITV(0);
    if (lane == 0) __builtin_amdgcn_raw_buffer_store_b64(tag, rws, WS_FKV + (unsigned)((hd * 8 + bi) * 4 + j) * 8u, 0, AUX_SC1);
  } else {
    const int j = wave - 8;
    u32x2 ep = {0u, 0u};
    if (j == 0) ep = __builtin_amdgcn_raw_buffer_load_b64(rws, WS_EPOCH, 0, AUX_SC1);
    auto put_tag = [&]() { if (j == 0) *(LAS u32x2*)(sm + L_FL + 256) = mk_tag(ep); };
    auto retile = [&](unsigned img, int ks) {
      const unsigned ta = img + (unsigned)(8 * hh + q4) * 128u + (unsigned)(4 * (g4 & 1) + 2 * (p4 & 1) + (p4 >> 1)) * 8u;
      ST128(__builtin_bit_cast(u32x4, lds_tr2(sm, ta, ta + 512)), WS_WT + lane * 16, (2 * bi * 144 + ks) * 1024);
      ST128(__builtin_bit_cast(u32x4, lds_tr2(sm, ta + 64, ta + 576)), WS_WT + lane * 16, ((2 * bi + 1) * 144 + ks) * 1024);
    };
    auto clear_o = [&]() {
      const u32x4 inv = {0xffffffffu, 0xffffffffu, 0xffffffffu, 0xffffffffu};
      const unsigned oo = WS_OF + (unsigned)((8 * bi + 2 * j) * 32 + hd) * 1024u + lane * 16u;
      ST128(inv, oo, 0); ST128(inv, oo + 32768, 0);
    };
    const unsigned fkv = WS_FKV + (unsigned)((hd * 8 + ((bi + lane) & 7)) * 4 + j) * 8u;
    u32x4 fd[FAST_N][4];
    u32x2 fc[FAST_N];
    auto fast_load = [&](int u) __attribute__((always_inline)) {
      const unsigned g = (unsigned)(((bi + 1 + u) & 7) * 4 + j);
      fc[u] = __builtin_amdgcn_raw_buffer_load_b64(rws, lane * 8u, WS_CK + (unsigned)(hd * 32 + g) * 512u, AUX_SC1);
#pragma unroll
      for (int e = 0; e < 4; ++e) fd[u][e] = LD128(KEYED(lane * 16u) + e * 1024, KVOFF(hd, g));
    };
    auto fast_take = [&]() __attribute__((always_inline)) {
#pragma unroll
      for (int u = 0; u < FAST_N; ++u) {
#pragma unroll 1
        for (unsigned s = 0; s < SPIN_CAP; ++s) {
          if (__all(ck_mix(fd[u][0], fd[u][2], tag.x) == fc[u].x && ck_mix(fd[u][1], fd[u][3], tag.y) == fc[u].y)) break;
          CFENCE();
#pragma unroll
          for (int v = u; v < FAST_N; ++v) fast_load(v);
        }
        LAS char* dst = sm + ((1 + u) * 8 + 2 * j) * TILE + lane * 16;
#pragma unroll
        for (int e = 0; e < 4; ++e) *(LAS u32x4*)(dst + (e >> 1) * TILE + (e & 1) * 1024) = fd[u][e];
        WAITL0();
        if (lane == 0) lds_add(sm, 1 + u, 2u);
      }
    };
    auto gather = [&](auto pre) __attribute__((always_inline)) {
      u32x2 fv = __builtin_amdgcn_raw_buffer_load_b64(rws, fkv, 0, AUX_SC1);
      fast_take();
      const u32x4 dws = mk_desc(ws, WS_END);
      unsigned spins = 0;
      bool done = false;
#pragma unroll 1
      for (int n0 = 1 + FAST_N; n0 < 8;) {
        const unsigned rdy = ((unsigned)__builtin_amdgcn_ballot_w64(fv.x == tag.x && fv.y == tag.y) & 0xffu) >> n0;
        int cnt = __builtin_ctz(~rdy);
        if (++spins > SPIN_CAP) cnt = 8 - n0;
        CFENCE();
#pragma unroll 1
        for (int u = n0; u < n0 + cnt; ++u) {
          const unsigned go = KVOFF(hd, ((bi + u) & 7) * 4 + j), d = sb + (unsigned)(u * 8 + 2 * j) * TILE;
          const unsigned gu = __builtin_amdgcn_readfirstlane(go), du = __builtin_amdgcn_readfirstlane(d);
          dma16x2_sc1(dws, du, KEYED(lane * 16u), gu);
          dma16x2_sc1(dws, du + TILE, KEYED(lane * 16u), gu + 2048u);
        }
        if (n0 + cnt < 8) {
          if (!cnt) __builtin_amdgcn_s_sleep(4);
          fv = __builtin_amdgcn_raw_buffer_load_b64(rws, fkv, 0, AUX_SC1);
        }
        WAITV(0);
        if (lane < cnt) lds_add(sm, n0 + lane, 2u);
        n0 += cnt;
        if (!done && n0 > 4) {
          pre();
          WAITV(0);
          if (lane == 0) __builtin_amdgcn_raw_buffer_store_b64(tag, rws, WS_FACT + (unsigned)((bi * 4 + j) * 32 + hd) * 8u, 0, AUX_SC1);
          done = true;
        }
      }
    };
    if (has_w1) {
      f32x4 acc[4][4];
      const unsigned wa = 32768u + 1536u + (unsigned)(g4 >> 1) * 3584u + (unsigned)(8 * (g4 & 1) + q4) * 128u +
                          (unsigned)((q4 >> 1) | ((g4 & 1) << 1)) * 32u + p4 * 8u;
      bf16x8 xA[4], wA[4], xB[4], wB[4];
#pragma unroll
      for (int t = 0; t < 4; ++t) { xB[t] = __builtin_bit_cast(bf16x8, (u32x4){0u, 0u, 0u, 0u}); wB[t] = xB[t]; }
#define W1_RD(X, W, kk)                                                                                            \
      _Pragma("unroll") for (int c = 0; c < 4; ++c) W[c] = lds_tr2(sm, buf + (wa ^ (unsigned)(c * 32)) + kk * 7168, buf + (wa ^ (unsigned)(c * 32)) + kk * 7168 + 512); \
      _Pragma("unroll") for (int t = 0; t < 4; ++t) X[t] = lds_frag(sm, buf + (kk ? xa1 : xa0) + t * 2048);
#define W1_MM(X, W)                                                                                                \
      _Pragma("unroll") for (int t = 0; t < 4; ++t)                                                                \
        _Pragma("unroll") for (int c = 0; c < 4; ++c) acc[t][c] = __builtin_amdgcn_mfma_f32_16x16x32_bf16(W[c], X[t], acc[t][c], 0, 0, 0); \
      _Pragma("unroll") for (int u = 0; u < 12; ++u) { __builtin_amdgcn_sched_group_barrier(0x008, 1, 0); __builtin_amdgcn_sched_group_barrier(0x100, 1, 0); } \
      __builtin_amdgcn_sched_group_barrier(0x008, 4, 0);                                                           \
      asm volatile("" : "+v"(acc[0][0]), "+v"(acc[0][1]), "+v"(acc[0][2]), "+v"(acc[0][3]), "+v"(acc[1][0]), "+v"(acc[1][1]), "+v"(acc[1][2]), "+v"(acc[1][3]), \
                        "+v"(acc[2][0]), "+v"(acc[2][1]), "+v"(acc[2][2]), "+v"(acc[2][3]), "+v"(acc[3][0]), "+v"(acc[3][1]), "+v"(acc[3][2]), "+v"(acc[3][3]) :: "memory"); \
      __builtin_amdgcn_sched_barrier(0);
      unsigned buf = 0;
      BARRIER();
#pragma unroll
      for (int c = 0; c < 4; ++c) {
        const u32x2 bv = *(LAS u32x2*)(sm + L_B1 + (16 * c + 4 * g4) * 2);
#pragma unroll
        for (int t = 0; t < 4; ++t) acc[t][c] = (f32x4){bf_lo(bv.x), bf_hi(bv.x), bf_lo(bv.y), bf_hi(bv.y)};
      }
#pragma unroll 1
      for (int n = 0; n < 8; ++n) {
        W1_RD(xA, wA, 0)
        W1_MM(xB, wB)
        W1_RD(xB, wB, 1)
        W1_MM(xA, wA)
        NEXT_BUF(buf);
        if (n == 2) put_tag();
        if (n < 7) BARRIER();
      }
      BARRIER();
      __builtin_amdgcn_s_setprio(3);
      get_tag();
      W1_MM(xB, wB)
      u32x4 actv[4][2];
      auto mish1 = [&](int t) __attribute__((always_inline)) {
#pragma unroll
          for (int pc = 0; pc < 2; ++pc) {
            u32x4 o;
#pragma unroll
            for (int e = 0; e < 4; ++e) {
              f32x2 hv = {acc[t][2 * pc + (e >> 1)][2 * (e & 1)], acc[t][2 * pc + (e >> 1)][2 * (e & 1) + 1]};
              f32x2 a = hv * 1.4426950408889634f;
              f32x2 ex = {__builtin_amdgcn_exp2f(a.x), __builtin_amdgcn_exp2f(a.y)};
              f32x2 d = ex * (ex + 2.f) + 2.f;
              f32x2 r = {__builtin_amdgcn_rcpf(d.x), __builtin_amdgcn_rcpf(d.y)};
              f32x2 y = hv * (r * -2.f + 1.f);
              o[e] = pk_bf16(y.x, y.y);
            }
            asm volatile("" : "+v"(o));
            actv[t][pc] = o;
          }
      };
      mish1(0); mish1(1);
      mish1(2);
#pragma unroll
      for (int u = 0; u < FAST_N; ++u) fast_load(u);
      mish1(3);
      gather([&]() __attribute__((always_inline)) {
        const unsigned ao = WS_ACT + (unsigned)(8 * bi + 2 * j) * 114688u + (unsigned)cw * 4096u + (unsigned)g4 * 512u + l15 * 16u;
#pragma unroll
        for (int t = 0; t < 4; ++t)
#pragma unroll
          for (int pc = 0; pc < 2; ++pc) { ST128(actv[t][pc], ao + (t >> 1) * 114688 + pc * 2048 + (t & 1) * 256, 0); }
        retile(L_RT + j * 2048u, 4 * cw + j);
      });
    } else {
      f32x4 acc[4];
#pragma unroll
      for (int t = 0; t < 4; ++t) acc[t] = (f32x4){0.f, 0.f, 0.f, 0.f};
      unsigned buf = 0;
#pragma unroll 1
      for (int n = 0; n < 8; ++n) {
        BARRIER();
        if (hd == 7) {
#pragma unroll
          for (int kk = 0; kk < 2; ++kk) {
            const bf16x8 gf = lds_frag(sm, L_G + ((n + rot) & 7) * 128 + kk * 64 + g4 * 16);
#pragma unroll
            for (int t = 0; t < 4; ++t) acc[t] = __builtin_amdgcn_mfma_f32_16x16x32_bf16(gf, lds_frag(sm, buf + (kk ? xa1 : xa0) + t * 2048), acc[t], 0, 0, 0);
          }
        }
        if (n == 2) put_tag();
        if (n == 5 || n == 6) retile(n == 5 ? L_RT + j * 2048u : 2 * STG + 32768u + 1536u + j * 3584u, 112 + (hd >> 3) * 8 + 2 * j + (n - 5));
        NEXT_BUF(buf);
      }
      WAITV(0);
      get_tag();
      BARRIER();
      __builtin_amdgcn_s_setprio(3);
      if (hd == 7) {
        const float lg = g4 == 0 ? acc[0][0] : g4 == 1 ? acc[1][0] : g4 == 2 ? acc[2][0] : acc[3][0];
        const float gv = __builtin_amdgcn_rcpf(1.f + __builtin_amdgcn_exp2f(lg * -1.4426950408889634f));
        __builtin_amdgcn_raw_buffer_store_b32(__float_as_uint(gv), rws, WS_G + (unsigned)(256 * bi + 64 * j + lane) * 4u, 0, AUX_SC1);
      }
      __builtin_amdgcn_s_sleep(16);
#pragma unroll
      for (int u = 0; u < FAST_N; ++u) fast_load(u);
      gather([&]() {});
    }
    clear_o();
    WAITV(0);
    if (lane == 0) { __builtin_amdgcn_raw_buffer_store_b64(tag, rws, WS_FCL + (unsigned)((bi * 4 + j) * 32 + hd) * 8u, 0, AUX_SC1); lds_add(sm, 8, 2u); }
    const int tm = hd >> 3, cp = hd & 7;
    if (j == 0) {
      const unsigned o1 = WS_FACT + (unsigned)((4 * bi + tm) * 32 + l31) * 8u;
#pragma unroll 1
      for (unsigned s = 0; s < SPIN_CAP; ++s) {
        CFENCE();
        const u32x2 v = __builtin_amdgcn_raw_buffer_load_b64(rws, o1, 0, AUX_SC1);
        const u32x4 y = LD128(WS_FACT + (unsigned)cp * 1024u + lane * 16u, 0);
        if (__all(v.x == tag.x && v.y == tag.y && y[0] == tag.x && y[1] == tag.y && y[2] == tag.x && y[3] == tag.y)) break;
        __builtin_amdgcn_s_sleep(4);
      }
      CFENCE();
      if (lane == 0) lds_add(sm, 18, 1u);
    } else {
      if (b == 0 && j == 2) {
#pragma unroll 1
        for (int u = 0; u < 4; ++u) glb_wait(rws, WS_FKV + (unsigned)(64 * u + lane) * 32u, tag);
        if (lane == 0) __builtin_amdgcn_raw_buffer_store_b64(tag, rws, WS_EPOCH, 0, AUX_SC1);
      }
      lds_wait(sm, 18, 1u);
    }
    {
      f32x16 acc[2][2];
#pragma unroll
      for (int t = 0; t < 2; ++t)
#pragma unroll
        for (int c = 0; c < 2; ++c)
#pragma unroll
          for (int r = 0; r < 16; ++r) acc[t][c][r] = 0.f;
      const unsigned ab = WS_ACT + (unsigned)(8 * bi + 2 * tm) * 114688u + (unsigned)(28 * j) * 1024u;
      const unsigned ab1 = WS_ACT + (unsigned)(8 * bi + 2 * tm + 1) * 114688u + (unsigned)(28 * j) * 1024u;
      const unsigned wb = WS_WT + (unsigned)((2 * cp) * 144 + 28 * j) * 1024u;
      const unsigned wb1 = WS_WT + (unsigned)((2 * cp + 1) * 144 + 28 * j) * 1024u;
      u32x4 af[4][2], wf[4][2];
#pragma unroll
      for (int u = 0; u < 4; ++u) {
        af[u][0] = LD128(lane * 16 + u * 1024, ab); af[u][1] = LD128(lane * 16 + u * 1024, ab1);
        wf[u][0] = LD128(lane * 16 + u * 1024, wb); wf[u][1] = LD128(lane * 16 + u * 1024, wb1);
      }
      __builtin_amdgcn_s_setprio(1);
#pragma unroll 1
      for (int it = 1; it <= 7; ++it) {
        const unsigned nx = (it < 7) ? it * 4096u : 0u;
        if (it == 7) {
          lds_wait(sm, 25, 8u);
          const u32x4 dws = mk_desc(ws, WS_END);
#pragma unroll 1
          for (int u = 0; u < 8; ++u)
            dma16x2_sc1(dws, sb + (unsigned)(j * 16 + 2 * u) * 1024u, lane * 16u, WS_WT + (unsigned)((2 * cp + (u >> 2)) * 144 + 112 + 8 * j + 2 * (u & 3)) * 1024u);
        }
        if (it == 4 && j == 1) {
          glb_wait(rws, WS_FCL + (unsigned)((4 * bi + tm) * 32 + l31) * 8u, tag);
          *(LAS unsigned*)(sm + L_G + lane * 4) = __builtin_amdgcn_raw_buffer_load_b32(rws, WS_G + (unsigned)(256 * bi + 64 * tm + lane) * 4u, 0, AUX_SC1);
          WAITL0();
          if (lane == 0) lds_add(sm, 24, 1u);
        }
        const rsrc_t rws = mk_rsrc(ws, it < 7 ? WS_END : 0u);
#pragma unroll
        for (int u = 0; u < 4; ++u) {
#pragma unroll
          for (int t = 0; t < 2; ++t)
#pragma unroll
            for (int c = 0; c < 2; ++c)
              acc[t][c] = __builtin_amdgcn_mfma_f32_32x32x16_bf16(__builtin_bit_cast(bf16x8, wf[u][c]), __builtin_bit_cast(bf16x8, af[u][t]), acc[t][c], 0, 0, 0);
          af[u][0] = LD128(lane * 16 + u * 1024, ab + nx); af[u][1] = LD128(lane * 16 + u * 1024, ab1 + nx);
          wf[u][0] = LD128(lane * 16 + u * 1024, wb + nx); wf[u][1] = LD128(lane * 16 + u * 1024, wb1 + nx);
          __builtin_amdgcn_sched_barrier(0);
        }
      }
      WAITV(0);
      if (lane == 0) lds_add(sm, 24, 1u);
      __builtin_amdgcn_s_setprio(0);
      lds_wait(sm, 21, 8u);
      __builtin_amdgcn_s_setprio(3);
      LAS f32x4* xw = (LAS f32x4*)(sm + 65536 + j * 2048 + lane * 16);
#pragma unroll
      for (int u = 0; u < 16; ++u) {
        const f32x16& av = acc[u >> 3][(u >> 2) & 1];
        xw[(u >> 1) * 512 + (u & 1) * 64] = (f32x4){av[4 * (u & 3)], av[4 * (u & 3) + 1], av[4 * (u & 3) + 2], av[4 * (u & 3) + 3]};
      }
    }
    BARRIER();
    return;
  }

  const int w = wave;
  const unsigned ka = lane * 16;
  const unsigned va = 1024u + (unsigned)(hh * 16 + l15) * 16u;
  const unsigned vc = l31 == 16 ? 0x3f803f80u : 0u;
  const int IDX = 0xDDDD;
  const bf16x8 qf = lds_frag(sm, L_Q + w * 1024 + lane * 16);
  u32x4 kf = *(LAS u32x4*)(sm + ka);
  f32x16 negm, O;
  {
    f32x16 z;
#pragma unroll
    for (int r = 0; r < 16; ++r) { z[r] = 0.f; O[r] = 0.f; }
    f32x16 s0 = __builtin_amdgcn_mfma_f32_32x32x16_bf16(__builtin_bit_cast(bf16x8, kf), qf, z, 0, 0, 0);
    float m = s0[0];
#pragma unroll
    for (int r = 1; r < 16; ++r) m = fmaxf(m, s0[r]);
    const auto sw = __builtin_amdgcn_permlane32_swap(__float_as_uint(m), __float_as_uint(m), false, false);
    m = fmaxf(__uint_as_float(sw[0]), __uint_as_float(sw[1]));
#pragma unroll
    for (int r = 0; r < 16; ++r) negm[r] = -m;
  }
  const int tm = hd >> 3, cp = hd & 7;
  const int tt = w & 1, kq = w >> 1;
  unsigned xwo = (unsigned)(tt * 16 + kq) * 2048u + lane * 16u, xro = (unsigned)(tt * 16 + kq * 4) * 2048u + lane * 16u;
  unsigned oo = (unsigned)(256 * bi + 64 * tm + 32 * tt + l31) * 1024u + (unsigned)(64 * cp + 16 * kq + 8 * hh) * 2u;
  unsigned wfo = (unsigned)kq * 16384u + lane * 16u, gao = L_G + (unsigned)(32 * tt + l31) * 4u;
  unsigned osb = WS_OF + (unsigned)((8 * bi + w) * 32 + hd) * 1024u, ob = WS_OF + (unsigned)((8 * bi + 2 * tm + tt) * 32 + 8 * kq) * 1024u;
  asm volatile("" : "+v"(xwo), "+v"(xro), "+v"(oo), "+v"(wfo), "+v"(gao), "+s"(osb), "+s"(ob));
  {
    u32x4 v0 = {vc, vc, vc, vc}, v1 = v0;
    unsigned kb = sb + ka, vb = sb + va, fb = sb + L_FL + 4, ft;
    int cnt = 8, st, sp = 0x400000, sw = (w < 4) ? 2 : -1;
    if (w < 4) __builtin_amdgcn_s_setprio(2); else __builtin_amdgcn_s_setprio(1);
    asm volatile(ATTN_LOOP
                 : "+v"(O), "+v"(kf), "+v"(v0), "+v"(v1), "+v"(kb), "+v"(vb), "+s"(cnt), "+v"(fb), "=&v"(ft), "=&s"(st), "+s"(sp)
                 : "v"(qf), "v"(negm), "v"(IDX), "s"(sw), "s"(0x0000ffff0000ffffull)
                 : ATTN_CLOBS);
  }
  __builtin_amdgcn_s_setprio(3);
  u32x4 wf[2][8];
  const unsigned rdy = __hip_atomic_load((LAS unsigned*)(sm + L_FL) + 24, __ATOMIC_RELAXED, __HIP_MEMORY_SCOPE_WORKGROUP);
  CFENCE();
  float gv = *(LAS float*)(sm + gao);
#pragma unroll
  for (int u = 0; u < 16; ++u) wf[u >> 3][u & 7] = *(LAS u32x4*)(sm + wfo + u * 1024);
  CFENCE();
  float lsum = __uint_as_float(__builtin_amdgcn_permlane32_swap(__float_as_uint(O[8]), __float_as_uint(O[8]), false, false)[0]);
  {
    float chk = lsum;
#pragma unroll
    for (int r = 0; r < 8; ++r) chk = __builtin_fmaf(O[r], 0.f, chk);
    if (__any(!(chk > 1.0e-6f && chk < 1.0e26f))) {
      get_tag();
      glb_wait(rws, WS_FKV + (unsigned)(hd * 32 + l31) * 8u, tag);
      float m = -3.0e38f;
      f32x16 z;
#pragma unroll
      for (int r = 0; r < 16; ++r) { z[r] = 0.f; O[r] = 0.f; }
#pragma unroll 1
      for (int t = 0; t < 64; ++t) {
        const unsigned go = KVOFF(hd, t >> 1) + (t & 1) * 2048u;
        const u32x4 kg = LD128(KEYED(lane * 16u), go);
        u32x4 va0 = LD128(KEYED(va), go), va1 = LD128(KEYED(va + 512u), go);
        if (l31 >= 16) { va0 = (u32x4){vc, vc, vc, vc}; va1 = va0; }
        f32x16 S = __builtin_amdgcn_mfma_f32_32x32x16_bf16(__builtin_bit_cast(bf16x8, kg), qf, z, 0, 0, 0);
        float mx = m;
#pragma unroll
        for (int r = 0; r < 16; ++r) mx = fmaxf(mx, S[r]);
        mx = fmaxf(mx, __shfl_xor(mx, 32));
        const float al = __builtin_amdgcn_exp2f(m - mx);
        m = mx;
        f32x8 p0, p1;
#pragma unroll
        for (int r = 0; r < 16; ++r) O[r] *= al;
#pragma unroll
        for (int r = 0; r < 8; ++r) { p0[r] = __builtin_amdgcn_exp2f(S[r] - m); p1[r] = __builtin_amdgcn_exp2f(S[8 + r] - m); }
        O = __builtin_amdgcn_smfmac_f32_32x32x32_bf16(__builtin_bit_cast(bf16x8, va0), __builtin_bit_cast(bf16x16, p0), O, IDX, 0, 0);
        O = __builtin_amdgcn_smfmac_f32_32x32x32_bf16(__builtin_bit_cast(bf16x8, va1), __builtin_bit_cast(bf16x16, p1), O, IDX, 0, 0);
      }
      lsum = __uint_as_float(__builtin_amdgcn_permlane32_swap(__float_as_uint(O[8]), __float_as_uint(O[8]), false, false)[0]);
    }
  }
  {
    const float sc = __builtin_amdgcn_rcpf(lsum);
    u32x4 o;
#pragma unroll
    for (int e = 0; e < 4; ++e) o[e] = min(pk_bf16(O[2 * e] * sc, O[2 * e + 1] * sc), 0xfffffffeu);
    ST128(o, lane * 16u, osb);
  }
  {
    LAS f32x4* xw = (LAS f32x4*)(sm + xwo);
    LAS f32x4* xr = (LAS f32x4*)(sm + xro);
    if ((unsigned)__builtin_amdgcn_readfirstlane(rdy) < 5u) {
      __builtin_amdgcn_s_setprio(0);
      lds_wait(sm, 24, 5u);
      __builtin_amdgcn_s_setprio(3);
      gv = *(LAS float*)(sm + gao);
#pragma unroll
      for (int u = 0; u < 16; ++u) wf[u >> 3][u & 7] = *(LAS u32x4*)(sm + wfo + u * 1024);
    }
    WAITL0();
    if (lane == 0) lds_add(sm, 21, 1u);
    __builtin_amdgcn_s_setprio(0); lds_wait(sm, 21, 8u); __builtin_amdgcn_s_setprio(3);
    WAITV(0);
    f32x16 acc[2];
#pragma unroll 1
    for (unsigned s = 0; s < SPIN_CAP; ++s) {
      CFENCE();
      u32x4 of[8];
#pragma unroll
      for (int u = 0; u < 8; ++u) { of[u] = LD128(lane * 16 + (u & 3) * 1024, ob + (u >> 2) * 4096); __builtin_amdgcn_sched_barrier(0); }
      __builtin_amdgcn_sched_barrier(0);
#pragma unroll
      for (int r = 0; r < 16; ++r) { acc[0][r] = 0.f; acc[1][r] = 0.f; }
      unsigned mx = 0;
#pragma unroll
      for (int u = 0; u < 8; ++u) {
        acc[0] = __builtin_amdgcn_mfma_f32_32x32x16_bf16(__builtin_bit_cast(bf16x8, wf[0][u]), __builtin_bit_cast(bf16x8, of[u]), acc[0], 0, 0, 0);
        acc[1] = __builtin_amdgcn_mfma_f32_32x32x16_bf16(__builtin_bit_cast(bf16x8, wf[1][u]), __builtin_bit_cast(bf16x8, of[u]), acc[1], 0, 0, 0);
        mx = max(max(mx, of[u][0]), of[u][1]);
        mx = max(max(mx, of[u][2]), of[u][3]);
        asm volatile("" : "+v"(acc[0]), "+v"(acc[1]), "+v"(mx));
      }
      if (__all(mx != 0xffffffffu)) break;
    }
#pragma unroll
    for (int p = 0; p < 4; ++p) {
      xw[p * 512] = (f32x4){acc[p >> 1][8 * (p & 1)], acc[p >> 1][8 * (p & 1) + 1], acc[p >> 1][8 * (p & 1) + 2], acc[p >> 1][8 * (p & 1) + 3]};
      xw[p * 512 + 64] = (f32x4){acc[p >> 1][8 * (p & 1) + 4], acc[p >> 1][8 * (p & 1) + 5], acc[p >> 1][8 * (p & 1) + 6], acc[p >> 1][8 * (p & 1) + 7]};
    }
    BARRIER();
    f32x4 pv[8], bv[8];
#pragma unroll
    for (int q = 0; q < 8; ++q) bv[q] = xr[4096 + q * 64];
#pragma unroll
    for (int q = 0; q < 8; ++q) pv[q] = xr[q * 64];
    const float om = 1.f - gv;
    const f32x4 a = ((bv[0] + bv[2]) + (bv[4] + bv[6])) * om + ((pv[0] + pv[2]) + (pv[4] + pv[6])) * gv;
    const f32x4 c = ((bv[1] + bv[3]) + (bv[5] + bv[7])) * om + ((pv[1] + pv[3]) + (pv[5] + pv[7])) * gv;
    const u32x4 o = {pk_bf16(a[0], a[1]), pk_bf16(a[2], a[3]), pk_bf16(c[0], c[1]), pk_bf16(c[2], c[3])};
    __builtin_amdgcn_raw_buffer_store_b128(o, mk_rsrc(out, 2048u * 1024u), oo, 0, 0);
  }
}

extern "C" void kernel_launch(void* const* d_in, const int* in_sizes, int n_in, void* d_out, int out_size,
                              void* d_ws, size_t ws_size, hipStream_t stream) {
  fused_kernel<<<256, 768, 0, stream>>>((const u16*)d_in[0], (const u16*)d_in[1], (const u16*)d_in[2], (const u16*)d_in[4],
                                        (const u16*)d_in[5], (const u16*)d_in[6], (const u16*)d_in[7], (u16*)d_out,
                                        (unsigned char*)d_ws);
}
```
